```python
import torch

def _softmax(x, axis=-1):
    return torch.softmax(x, dim=axis)
def _gelu(x):
    return torch.nn.functional.gelu(x, approximate="tanh")
def _silu(x):
    return torch.nn.functional.silu(x)
def _sigmoid(x):
    return torch.sigmoid(x)
def _layernorm(x, axis=-1, eps=1e-5):
    m = x.mean(dim=axis, keepdim=True)
    v = x.var(dim=axis, keepdim=True, unbiased=False)
    return (x - m) / torch.sqrt(v + eps)
def _scatter_add(tgt, idx, src, axis=0):
    return tgt.clone().index_add_(axis, idx, src)
def _segment_sum(src, seg, n):
    out = torch.zeros((n,) + src.shape[1:], dtype=src.dtype)
    return out.index_add_(0, seg, src)
def _pad(x, pw):
    flat = tuple(v for pair in reversed(pw) for v in pair)
    return torch.nn.functional.pad(x, flat)

kernel_name = 'parallel_sum__sliding_local_dilated__glu_gated_swiglu__prompt'

def setup_inputs():

    _w = (lambda a: (lambda t: t.to(torch.bfloat16) if t.is_floating_point() else t)(torch.as_tensor(a)))
    def _A_setup():
        B, S, D = 1, 2048, 512
        return {
            "x": _w((lambda *s: torch.randn(*s, dtype=torch.bfloat16) * 0.05)(B, S, D)),
            "wqkv": _w((lambda *s: torch.randn(*s, dtype=torch.bfloat16) * 0.05)(D, 3 * D)),
            "wo": _w((lambda *s: torch.randn(*s, dtype=torch.bfloat16) * 0.05)(D, D)),
        }
    _A_in = _A_setup()

    def _B_setup():
        B, S, D, F = 1, 2048, 512, 1792
        return {
            "x": _w((lambda *s: torch.randn(*s, dtype=torch.bfloat16) * 0.05)(B, S, D)),
            "w1": _w((lambda *s: torch.randn(*s, dtype=torch.bfloat16) * 0.8)(D, F) * (2.0 / D) ** 0.5),
            "w3": _w((lambda *s: torch.randn(*s, dtype=torch.bfloat16) * 0.8)(D, F) * (2.0 / D) ** 0.5),
            "w2": _w((lambda *s: torch.randn(*s, dtype=torch.bfloat16) * 0.8)(F, D) * (2.0 / F) ** 0.5),
        }
    _B_in = _B_setup()

    return {
        "A_x": _A_in["x"],
        "A_wqkv": _A_in["wqkv"],
        "A_wo": _A_in["wo"],
        "B_x": _B_in["x"],
        "B_w1": _B_in["w1"],
        "B_w3": _B_in["w3"],
        "B_w2": _B_in["w2"],
    }

def reference(A_x, A_wqkv, A_wo, B_x, B_w1, B_w3, B_w2):
    def _A_ref(x, wqkv, wo):
        B, S, D = x.shape
        H = 32; Dh = D // H
        W, dil = 8, 2
        qkv = (lambda x, *d: x.permute(*d))((x @ wqkv).reshape(B, S, 3, H, Dh), 2, 0, 3, 1, 4)
        q, k, v = qkv[0], qkv[1], qkv[2]
        scale = Dh ** -0.5
        pos = torch.arange(S)
        off = torch.arange(W) * dil
        idx = pos[:, None] - off[None, :]
        mask = idx >= 0
        idx_c = torch.where(mask, idx, 0).reshape(-1)
        kg = k[:, :, idx_c, :].reshape(B, H, S, W, Dh)
        vg = v[:, :, idx_c, :].reshape(B, H, S, W, Dh)
        logits = torch.einsum("bhsd,bhswd->bhsw", q, kg) * scale
        logits = torch.where(mask[None, None, :, :], logits, -1e9)
        attn = _softmax(logits, axis=-1)
        o = torch.einsum("bhsw,bhswd->bhsd", attn, vg)
        return (lambda x, *d: x.permute(*d))(o, 0, 2, 1, 3).reshape(B, S, D) @ wo

    def _B_ref(x, w1, w3, w2):
        gate = torch.einsum("bsd,df->bsf", x, w1)
        up = torch.einsum("bsd,df->bsf", x, w3)
        h = _silu(gate) * up
        out = torch.einsum("bsf,fd->bsd", h, w2)
        return out

    x = A_x
    xn = _layernorm(x, axis=-1)
    return x + _A_ref(xn, A_wqkv, A_wo) + _B_ref(xn, B_w1, B_w3, B_w2)
```

```cpp
#include <hip/hip_runtime.h>
#include <hip/hip_bf16.h>
typedef unsigned short u16;
__device__ __forceinline__ float bf2f(u16 b){ return __uint_as_float(((unsigned)b)<<16); }
__device__ __forceinline__ u16 f2bf(float f){ unsigned u=__float_as_uint(f); u += 0x7fffu + ((u>>16)&1u); return (u16)(u>>16); }
constexpr int S=2048, D=512, F=1792, H=32, DH=16;

__global__ void k_ln(const u16* x, u16* xn){
  int row=blockIdx.x; int t=threadIdx.x;
  float v[8]; float s=0;
  for(int i=0;i<8;i++){ v[i]=bf2f(x[row*D+t*8+i]); s+=v[i]; }
  for(int o=32;o;o>>=1) s+=__shfl_xor(s,o);
  float m=s/D; float q=0;
  for(int i=0;i<8;i++){ float d=v[i]-m; q+=d*d; }
  for(int o=32;o;o>>=1) q+=__shfl_xor(q,o);
  float r=1.0f/sqrtf(q/D+1e-5f);
  for(int i=0;i<8;i++) xn[row*D+t*8+i]=f2bf((v[i]-m)*r);
}
__global__ void k_gemm(const u16* A,const u16* B,float* C,int M,int N,int K){
  int n=blockIdx.x*64+threadIdx.x; int m=blockIdx.y;
  __shared__ float a[2048];
  for(int k=threadIdx.x;k<K;k+=64) a[k]=bf2f(A[m*K+k]);
  __syncthreads();
  float acc=0;
  for(int k=0;k<K;k++) acc+=a[k]*bf2f(B[(size_t)k*N+n]);
  C[(size_t)m*N+n]=acc;
}
__global__ void k_attn(const float* qkv,u16* o){
  int s=blockIdx.x; int t=threadIdx.x;
  if(t>=H) return;
  float q[DH]; for(int d=0;d<DH;d++) q[d]=bf2f(f2bf(qkv[(size_t)s*1536+t*DH+d]));
  float l[8]; float mx=-1e30f;
  for(int w=0;w<8;w++){ int p=s-2*w; if(p<0){l[w]=-1e9f;} else { float a=0; for(int d=0;d<DH;d++) a+=q[d]*bf2f(f2bf(qkv[(size_t)p*1536+512+t*DH+d])); l[w]=a*0.25f; } mx=fmaxf(mx,l[w]); }
  float sum=0; for(int w=0;w<8;w++){ l[w]=__expf(l[w]-mx); sum+=l[w]; }
  for(int d=0;d<DH;d++){ float a=0; for(int w=0;w<8;w++){ int p=s-2*w; if(p<0) p=0; a+=l[w]*bf2f(f2bf(qkv[(size_t)p*1536+1024+t*DH+d])); } o[(size_t)s*D+t*DH+d]=f2bf(a/sum); }
}
__global__ void k_h(const float* g,const float* u,u16* h){
  int i=blockIdx.x*256+threadIdx.x; float a=g[i]; h[i]=f2bf(a/(1.0f+__expf(-a))*u[i]);
}
__global__ void k_fin(const u16* x,const float* a,const float* b,u16* out){
  int i=blockIdx.x*256+threadIdx.x; out[i]=f2bf(bf2f(x[i])+a[i]+b[i]);
}
extern "C" void kernel_launch(void* const* d_in, const int* in_sizes, int n_in, void* d_out, int out_size, void* d_ws, size_t ws_size, hipStream_t stream) {
  const u16* x=(const u16*)d_in[0]; const u16* wqkv=(const u16*)d_in[1]; const u16* wo=(const u16*)d_in[2];
  const u16* w1=(const u16*)d_in[4]; const u16* w3=(const u16*)d_in[5]; const u16* w2=(const u16*)d_in[6];
  char* ws=(char*)d_ws;
  u16* xn=(u16*)ws; ws+=S*D*2;
  float* qkv=(float*)ws; ws+=(size_t)S*1536*4;
  float* g=(float*)ws; ws+=(size_t)S*F*4;
  float* u=(float*)ws; ws+=(size_t)S*F*4;
  u16* h=(u16*)ws; ws+=(size_t)S*F*2;
  u16* o=(u16*)ws; ws+=(size_t)S*D*2;
  float* a=(float*)ws; ws+=(size_t)S*D*4;
  float* b=(float*)ws; ws+=(size_t)S*D*4;
  k_ln<<<S,64,0,stream>>>(x,xn);
  k_gemm<<<dim3(1536/64,S),64,0,stream>>>(xn,wqkv,qkv,S,1536,D);
  k_gemm<<<dim3(F/64,S),64,0,stream>>>(xn,w1,g,S,F,D);
  k_gemm<<<dim3(F/64,S),64,0,stream>>>(xn,w3,u,S,F,D);
  k_attn<<<S,64,0,stream>>>(qkv,o);
  k_h<<<S*F/256,256,0,stream>>>(g,u,h);
  k_gemm<<<dim3(D/64,S),64,0,stream>>>(o,wo,a,S,D,D);
  k_gemm<<<dim3(D/64,S),64,0,stream>>>(h,w2,b,S,D,F);
  k_fin<<<S*D/256,256,0,stream>>>(x,a,b,(u16*)d_out);
}
```

```cpp
#include <hip/hip_runtime.h>
#include <hip/hip_bf16.h>

namespace {
constexpr int LDS_BYTES = 163840;

__global__ __launch_bounds__(512, 2) void k_fused(const void* x, const void* wqkv, const void* wo, const void* w1, const void* w3,
                                                  const void* w2, void* out, void* ws) {
  unsigned tid = threadIdx.x;
  const unsigned bid = blockIdx.x;
  asm volatile(
    "s_and_b32 s86, %1, 1\n"
    "s_cmp_eq_u32 s86, 1\n"
    "s_cbranch_scc1 LTYPE_B\n"
    "v_and_b32 v235, 63, %0\n"
    "v_lshrrev_b32 v237, 6, %0\n"
    "s_nop 0\n"
    "v_readfirstlane_b32 s68, v237\n"
    "s_lshr_b32 s86, %1, 3\n"
    "s_and_b32 s72, s86, 7\n"
    "s_lshr_b32 s86, s86, 3\n"
    "s_lshr_b32 s87, %1, 1\n"
    "s_and_b32 s87, s87, 3\n"
    "s_lshl_b32 s87, s87, 2\n"
    "s_add_u32 s71, s86, s87\n"
    "s_lshl_b32 s77, s71, 7\n"
    "s_mov_b64 s[52:53], %9\n"
    "s_and_b32 s53, s53, 0xffff\n"
    "s_mov_b32 s54, 0x90a000\n"
    "s_mov_b32 s55, 0x20000\n"
    "s_mov_b64 s[36:37], %2\n"
    "s_and_b32 s37, s37, 0xffff\n"
    "s_mov_b32 s38, 0x200000\n"
    "s_mov_b32 s39, 0x20000\n"
    "s_mul_i32 s90, s71, 0x200\n"
    "s_add_u32 s90, s90, 0x900000\n"
    "s_and_b32 s69, s68, 3\n"
    "s_lshr_b32 s70, s68, 2\n"
    "s_lshl_b32 s73, s68, 10\n"
    "s_add_u32 s73, s73, 0x8000\n"
    "s_lshl_b32 s74, s68, 11\n"
    "s_add_u32 s74, s74, 0x8000\n"
    "v_mov_b32 v237, 0.0\n"
    "buffer_load_dwordx2 v[168:169], v237, s[52:55], s90 offen\n"
    "v_lshlrev_b32 v238, 4, v235\n"
    "s_lshl_b32 s86, s72, 3\n"
    "s_add_u32 s86, s86, s68\n"
    "s_add_u32 s86, s86, 0\n"
    "s_add_u32 s86, s86, s77\n"
    "s_lshl_b32 s86, s86, 10\n"
    "buffer_load_dwordx4 v[180:183], v238, s[36:39], s86 offen\n"
    "s_sub_i32 s87, s77, 16\n"
    "s_max_i32 s87, s87, 0\n"
    "s_lshl_b32 s86, s68, 1\n"
    "s_add_u32 s86, s86, s87\n"
    "s_lshl_b32 s86, s86, 10\n"
    "buffer_load_dwordx4 v[184:187], v238, s[36:39], s86 offen\n"
    "buffer_load_dwordx4 v[188:191], v238, s[36:39], s86 offen offset:1024\n"
    "v_lshrrev_b32 v239, 3, v235\n"
    "v_and_b32 v240, 7, v235\n"
    "s_lshl_b32 s86, s68, 4\n"
    "s_add_u32 s86, s86, s77\n"
    "s_lshl_b32 s86, s86, 10\n"
    "v_lshrrev_b32 v241, 1, v239\n"
    "v_xor_b32 v241, v241, v240\n"
    "v_lshlrev_b32 v241, 4, v241\n"
    "v_lshl_add_u32 v242, v239, 10, v241\n"
    "v_add_u32 v160, s86, v242\n"
    "v_xor_b32 v242, 64, v242\n"
    "v_add_u32 v161, s86, v242\n"
    "v_add_u32 v161, 0x2000, v161\n"
    "s_lshl_b32 s86, s68, 3\n"
    "s_mov_b64 s[40:41], %3\n"
    "s_and_b32 s41, s41, 0xffff\n"
    "s_mov_b32 s42, 0x180000\n"
    "s_mov_b32 s43, 0x20000\n"
    "v_lshrrev_b32 v241, 2, v239\n"
    "v_xor_b32 v241, s68, v241\n"
    "v_and_b32 v241, 1, v241\n"
    "v_lshlrev_b32 v241, 2, v241\n"
    "v_and_b32 v242, 2, v239\n"
    "v_or_b32 v241, v241, v242\n"
    "v_xor_b32 v241, v241, v240\n"
    "v_lshlrev_b32 v241, 4, v241\n"
    "v_add_u32 v242, s86, v239\n"
    "v_mul_u32_u24 v242, 0xc00, v242\n"
    "v_add_u32 v162, v241, v242\n"
    "s_lshl_b32 s75, s72, 7\n"
    "s_sub_i32 s87, s77, 16\n"
    "s_max_i32 s87, s87, 0\n"
    "s_lshl_b32 s87, s87, 10\n"
    "s_lshl_b32 s88, s68, 7\n"
    "s_add_u32 s87, s87, s88\n"
    "v_lshrrev_b32 v241, 1, v239\n"
    "v_xor_b32 v241, v241, v240\n"
    "v_lshlrev_b32 v241, 4, v241\n"
    "v_lshl_add_u32 v242, v239, 10, v241\n"
    "v_add_u32 v244, s87, v242\n"
    "v_xor_b32 v242, 64, v242\n"
    "v_add_u32 v245, s87, v242\n"
    "v_add_u32 v245, 0x2000, v245\n"
    "v_mov_b32 v212, 0x3f803f80\n"
    "v_mov_b32 v213, 0x3f803f80\n"
    "v_mov_b32 v214, 0x3f803f80\n"
    "v_mov_b32 v215, 0x3f803f80\n"
    "s_lshl_b32 s88, s68, 11\n"
    "v_lshlrev_b32 v246, 4, v235\n"
    "v_add_u32 v246, s88, v246\n"
    "s_mov_b64 exec, 0xff\n"
    "ds_write_b128 v246, v[212:215] offset:0\n"
    "s_mov_b32 exec_lo, 0xffffff00\n"
    "s_mov_b32 exec_hi, 0xffffffff\n"
    "s_add_u32 m0, s88, 0\n"
    "s_nop 0\n"
    "buffer_load_dwordx4 v244, s[36:39], 0 offen lds\n"
    "s_mov_b64 exec, -1\n"
    "s_add_u32 m0, s88, 0x400\n"
    "s_nop 0\n"
    "buffer_load_dwordx4 v245, s[36:39], 0 offen lds\n"
    "s_add_u32 m0, s74, 0\n"
    "s_mov_b32 s89, 0\n"
    "buffer_load_dwordx4 v160, s[36:39], s89 offen lds\n"
    "s_add_u32 m0, s74, 0x400\n"
    "s_nop 0\n"
    "buffer_load_dwordx4 v161, s[36:39], s89 offen lds\n"
    "s_add_u32 m0, s73, 0x4000\n"
    "s_add_u32 s89, s75, 0x400\n"
    "buffer_load_dwordx4 v162, s[40:43], s89 offen lds\n"
    "s_add_u32 m0, s73, 0x6000\n"
    "s_add_u32 s89, s75, 0\n"
    "buffer_load_dwordx4 v162, s[40:43], s89 offen lds\n"
    "s_add_u32 m0, s74, 0x8000\n"
    "s_mov_b32 s89, 0x80\n"
    "buffer_load_dwordx4 v160, s[36:39], s89 offen lds\n"
    "s_add_u32 m0, s74, 0x8400\n"
    "s_nop 0\n"
    "buffer_load_dwordx4 v161, s[36:39], s89 offen lds\n"
    "s_add_u32 m0, s73, 0xc000\n"
    "s_add_u32 s89, s75, 0x30400\n"
    "buffer_load_dwordx4 v162, s[40:43], s89 offen lds\n"
    "s_add_u32 m0, s73, 0xe000\n"
    "s_add_u32 s89, s75, 0x30000\n"
    "buffer_load_dwordx4 v162, s[40:43], s89 offen lds\n"
    "s_add_u32 m0, s74, 0x10000\n"
    "s_mov_b32 s89, 0x100\n"
    "buffer_load_dwordx4 v160, s[36:39], s89 offen lds\n"
    "s_add_u32 m0, s74, 0x10400\n"
    "s_nop 0\n"
    "buffer_load_dwordx4 v161, s[36:39], s89 offen lds\n"
    "s_add_u32 m0, s73, 0x14000\n"
    "s_add_u32 s89, s75, 0x60400\n"
    "buffer_load_dwordx4 v162, s[40:43], s89 offen lds\n"
    "s_add_u32 m0, s73, 0x16000\n"
    "s_add_u32 s89, s75, 0x60000\n"
    "buffer_load_dwordx4 v162, s[40:43], s89 offen lds\n"
    "s_add_u32 m0, s74, 0x18000\n"
    "s_mov_b32 s89, 0x180\n"
    "buffer_load_dwordx4 v160, s[36:39], s89 offen lds\n"
    "s_add_u32 m0, s74, 0x18400\n"
    "s_nop 0\n"
    "buffer_load_dwordx4 v161, s[36:39], s89 offen lds\n"
    "s_add_u32 m0, s73, 0x1c000\n"
    "s_add_u32 s89, s75, 0x90400\n"
    "buffer_load_dwordx4 v162, s[40:43], s89 offen lds\n"
    "s_add_u32 m0, s73, 0x1e000\n"
    "s_add_u32 s89, s75, 0x90000\n"
    "buffer_load_dwordx4 v162, s[40:43], s89 offen lds\n"
    "s_mov_b64 s[44:45], %5\n"
    "s_and_b32 s45, s45, 0xffff\n"
    "s_mov_b32 s46, 0x1c0000\n"
    "s_mov_b32 s47, 0x20000\n"
    "s_mov_b64 s[48:49], %6\n"
    "s_and_b32 s49, s49, 0xffff\n"
    "s_mov_b32 s50, 0x1c0000\n"
    "s_mov_b32 s51, 0x20000\n"
    "s_lshl_b32 s86, s68, 3\n"
    "v_lshrrev_b32 v241, 2, v239\n"
    "v_xor_b32 v241, s68, v241\n"
    "v_and_b32 v241, 1, v241\n"
    "v_lshlrev_b32 v241, 2, v241\n"
    "v_and_b32 v242, 2, v239\n"
    "v_or_b32 v241, v241, v242\n"
    "v_xor_b32 v241, v241, v240\n"
    "v_lshlrev_b32 v241, 4, v241\n"
    "v_add_u32 v242, s86, v239\n"
    "v_mul_u32_u24 v242, 0xe00, v242\n"
    "v_add_u32 v228, v241, v242\n"
    "s_lshl_b32 s86, s68, 4\n"
    "v_lshrrev_b32 v241, 2, v239\n"
    "v_and_b32 v241, 1, v241\n"
    "v_lshlrev_b32 v241, 2, v241\n"
    "v_and_b32 v242, 2, v239\n"
    "v_or_b32 v241, v241, v242\n"
    "v_xor_b32 v241, v241, v240\n"
    "v_lshlrev_b32 v241, 4, v241\n"
    "v_add_u32 v242, s86, v239\n"
    "v_mul_u32_u24 v242, 0xc00, v242\n"
    "v_add_u32 v176, v241, v242\n"
    "s_add_u32 s86, s86, 8\n"
    "v_lshrrev_b32 v241, 2, v239\n"
    "v_xor_b32 v241, 1, v241\n"
    "v_and_b32 v241, 1, v241\n"
    "v_lshlrev_b32 v241, 2, v241\n"
    "v_and_b32 v242, 2, v239\n"
    "v_or_b32 v241, v241, v242\n"
    "v_xor_b32 v241, v241, v240\n"
    "v_lshlrev_b32 v241, 4, v241\n"
    "v_add_u32 v242, s86, v239\n"
    "v_mul_u32_u24 v242, 0xc00, v242\n"
    "v_add_u32 v177, v241, v242\n"
    "s_mov_b64 s[56:57], %4\n"
    "s_and_b32 s57, s57, 0xffff\n"
    "s_mov_b32 s58, 0x80000\n"
    "s_mov_b32 s59, 0x20000\n"
    "s_mov_b64 s[60:61], %7\n"
    "s_and_b32 s61, s61, 0xffff\n"
    "s_mov_b32 s62, 0x1c0000\n"
    "s_mov_b32 s63, 0x20000\n"
    "s_mov_b64 s[64:65], %8\n"
    "s_and_b32 s65, s65, 0xffff\n"
    "s_mov_b32 s66, 0x200000\n"
    "s_mov_b32 s67, 0x20000\n"
    "s_and_b32 s93, s68, 3\n"
    "s_lshl_b32 s86, s93, 3\n"
    "v_lshrrev_b32 v241, 2, v239\n"
    "v_xor_b32 v241, s93, v241\n"
    "v_and_b32 v241, 1, v241\n"
    "v_lshlrev_b32 v241, 2, v241\n"
    "v_and_b32 v242, 2, v239\n"
    "v_or_b32 v241, v241, v242\n"
    "v_xor_b32 v241, v241, v240\n"
    "v_lshlrev_b32 v241, 4, v241\n"
    "v_add_u32 v242, s86, v239\n"
    "v_mul_u32_u24 v242, 0x400, v242\n"
    "v_add_u32 v229, v241, v242\n"
    "s_lshl_b32 s86, s72, 7\n"
    "v_add_u32 v229, s86, v229\n"
    "s_lshl_b32 s93, s70, 13\n"
    "s_lshl_b32 s86, s69, 10\n"
    "s_add_u32 s93, s93, s86\n"
    "s_add_u32 s93, s93, 0x8000\n"
    "v_and_b32 v239, 15, v235\n"
    "v_lshrrev_b32 v240, 4, v235\n"
    "v_lshrrev_b32 v244, 2, v239\n"
    "v_and_b32 v245, 3, v239\n"
    "v_lshrrev_b32 v241, 1, v239\n"
    "v_xor_b32 v241, v241, v240\n"
    "v_lshlrev_b32 v241, 4, v241\n"
    "v_lshl_add_u32 v241, v239, 7, v241\n"
    "s_lshl_b32 s86, s69, 12\n"
    "s_add_u32 s86, s86, 0x8000\n"
    "v_add_u32 v163, s86, v241\n"
    "v_xor_b32 v164, 64, v163\n"
    "v_mov_b32 v226, v241\n"
    "v_xor_b32 v227, 64, v226\n"
    "v_lshrrev_b32 v246, 1, v244\n"
    "v_and_b32 v241, 1, v240\n"
    "v_lshl_or_b32 v246, v241, 1, v246\n"
    "v_lshl_add_u32 v241, v240, 3, v244\n"
    "v_lshlrev_b32 v241, 7, v241\n"
    "v_lshl_add_u32 v247, v245, 3, v241\n"
    "v_add_u32 v247, 0x8000, v247\n"
    "s_lshl_b32 s86, s70, 1\n"
    "v_xor_b32 v241, s86, v246\n"
    "v_lshl_add_u32 v220, v241, 5, v247\n"
    "v_xor_b32 v222, 32, v220\n"
    "v_xor_b32 v241, 64, v220\n"
    "v_add_u32 v221, 0x200, v241\n"
    "v_xor_b32 v223, 32, v221\n"
    "v_xor_b32 v241, s69, v246\n"
    "v_lshl_add_u32 v241, v241, 5, v247\n"
    "s_lshl_b32 s86, s70, 13\n"
    "v_add_u32 v224, s86, v241\n"
    "v_xor_b32 v241, 64, v224\n"
    "v_add_u32 v225, 0x200, v241\n"
    "s_lshl_b32 s86, s71, 3\n"
    "s_lshl_b32 s87, s69, 1\n"
    "s_add_u32 s86, s86, s87\n"
    "s_mul_i32 s86, s86, 0x12000\n"
    "v_lshlrev_b32 v241, 4, v235\n"
    "v_add_u32 v230, s86, v241\n"
    "s_lshl_b32 s86, s70, 8\n"
    "v_lshlrev_b32 v241, 4, v240\n"
    "v_add_u32 v236, s86, v241\n"
    "s_waitcnt vmcnt(21)\n"
    "v_readfirstlane_b32 s86, v168\n"
    "v_readfirstlane_b32 s87, v169\n"
    "s_nop 3\n"
    "s_mul_i32 s78, s86, 0x9e3779b1\n"
    "s_add_u32 s78, s78, s87\n"
    "s_add_u32 s78, s78, 0x7f4a7c15\n"
    "s_mul_i32 s79, s87, 0x85ebca77\n"
    "s_add_u32 s79, s79, s78\n"
    "s_xor_b32 s79, s79, 0xc2b2ae3d\n"
    "s_waitcnt vmcnt(12) lgkmcnt(0)\n"
    "s_barrier\n"
    "ds_read_b128 v[0:3], v163 offset:0\n"
    "ds_read_b128 v[64:67], v163 offset:2048\n"
    "ds_read_b128 v[4:7], v164 offset:0\n"
    "ds_read_b128 v[68:71], v164 offset:2048\n"
    "ds_read_b64_tr_b16 v[192:193], v220 offset:16384\n"
    "ds_read_b64_tr_b16 v[194:195], v221 offset:16384\n"
    "ds_read_b64_tr_b16 v[196:197], v222 offset:16384\n"
    "ds_read_b64_tr_b16 v[198:199], v223 offset:16384\n"
    "ds_read_b64_tr_b16 v[200:201], v220 offset:24576\n"
    "ds_read_b64_tr_b16 v[202:203], v221 offset:24576\n"
    "ds_read_b64_tr_b16 v[204:205], v222 offset:24576\n"
    "ds_read_b64_tr_b16 v[206:207], v223 offset:24576\n"
    "ds_read_b64_tr_b16 v[208:209], v224 offset:16384\n"
    "ds_read_b64_tr_b16 v[210:211], v225 offset:16384\n"
    "ds_read_b128 v[212:215], v226 offset:0\n"
    "s_waitcnt lgkmcnt(9)\n"
    "v_mfma_f32_16x16x32_bf16 v[128:131], v[192:195], v[0:3], 0\n"
    "ds_read_b64_tr_b16 v[252:253], v220 offset:20480\n"
    "v_lshlrev_b32 v237, 16, v180\n"
    "v_and_b32 v238, 0xffff0000, v180\n"
    "v_mfma_f32_16x16x32_bf16 v[132:135], v[192:195], v[64:67], 0\n"
    "ds_read_b64_tr_b16 v[254:255], v221 offset:20480\n"
    "v_lshlrev_b32 v239, 16, v181\n"
    "v_and_b32 v240, 0xffff0000, v181\n"
    "s_waitcnt lgkmcnt(9)\n"
    "v_mfma_f32_16x16x32_bf16 v[136:139], v[196:199], v[0:3], 0\n"
    "ds_read_b64_tr_b16 v[192:193], v222 offset:20480\n"
    "v_lshlrev_b32 v241, 16, v182\n"
    "v_and_b32 v242, 0xffff0000, v182\n"
    "v_mfma_f32_16x16x32_bf16 v[140:143], v[196:199], v[64:67], 0\n"
    "ds_read_b64_tr_b16 v[194:195], v223 offset:20480\n"
    "v_lshlrev_b32 v243, 16, v183\n"
    "v_and_b32 v244, 0xffff0000, v183\n"
    "s_waitcnt lgkmcnt(9)\n"
    "v_mfma_f32_16x16x32_bf16 v[144:147], v[200:203], v[0:3], 0\n"
    "ds_read_b64_tr_b16 v[196:197], v220 offset:28672\n"
    "v_add_f32 v245, v237, v238\n"
    "v_add_f32 v246, v239, v240\n"
    "v_mfma_f32_16x16x32_bf16 v[148:151], v[200:203], v[64:67], 0\n"
    "ds_read_b64_tr_b16 v[198:199], v221 offset:28672\n"
    "v_add_f32 v245, v245, v241\n"
    "v_add_f32 v246, v246, v242\n"
    "s_waitcnt lgkmcnt(9)\n"
    "v_mfma_f32_16x16x32_bf16 v[152:155], v[204:207], v[0:3], 0\n"
    "ds_read_b64_tr_b16 v[200:201], v222 offset:28672\n"
    "v_add_f32 v245, v245, v243\n"
    "v_add_f32 v246, v246, v244\n"
    "v_mfma_f32_16x16x32_bf16 v[156:159], v[204:207], v[64:67], 0\n"
    "ds_read_b64_tr_b16 v[202:203], v223 offset:28672\n"
    "v_add_f32 v245, v245, v246\n"
    "s_nop 1\n"
    "v_add_f32_dpp v245, v245, v245 quad_perm:[1,0,3,2] row_mask:0xf bank_mask:0xf\n"
    "s_waitcnt lgkmcnt(8)\n"
    "v_mfma_f32_16x16x32_bf16 v[216:219], v[208:211], v[212:215], 0\n"
    "ds_read_b64_tr_b16 v[204:205], v224 offset:20480\n"
    "ds_read_b64_tr_b16 v[206:207], v225 offset:20480\n"
    "ds_read_b128 v[212:215], v227 offset:0\n"
    "v_add_f32_dpp v245, v245, v245 quad_perm:[2,3,0,1] row_mask:0xf bank_mask:0xf\n"
    "s_nop 1\n"
    "v_add_f32_dpp v245, v245, v245 row_half_mirror row_mask:0xf bank_mask:0xf\n"
    "s_waitcnt vmcnt(8)\n"
    "s_barrier\n"
    "s_waitcnt lgkmcnt(9)\n"
    "v_mfma_f32_16x16x32_bf16 v[128:131], v[252:255], v[4:7], v[128:131]\n"
    "ds_read_b64_tr_b16 v[208:209], v220 offset:49152\n"
    "ds_read_b128 v[8:11], v163 offset:32768\n"
    "v_add_f32_dpp v245, v245, v245 row_mirror row_mask:0xf bank_mask:0xf\n"
    "s_nop 0\n"
    "v_mov_b32 v247, v245\n"
    "v_mfma_f32_16x16x32_bf16 v[132:135], v[252:255], v[68:71], v[132:135]\n"
    "ds_read_b64_tr_b16 v[210:211], v221 offset:49152\n"
    "v_permlane16_swap_b32 v245, v247\n"
    "s_nop 0\n"
    "v_add_f32 v245, v245, v247\n"
    "s_waitcnt lgkmcnt(10)\n"
    "v_mfma_f32_16x16x32_bf16 v[136:139], v[192:195], v[4:7], v[136:139]\n"
    "ds_read_b64_tr_b16 v[252:253], v222 offset:49152\n"
    "ds_read_b128 v[72:75], v163 offset:34816\n"
    "v_mov_b32 v247, v245\n"
    "s_nop 1\n"
    "v_permlane32_swap_b32 v245, v247\n"
    "v_mfma_f32_16x16x32_bf16 v[140:143], v[192:195], v[68:71], v[140:143]\n"
    "ds_read_b64_tr_b16 v[254:255], v223 offset:49152\n"
    "v_add_f32 v245, v245, v247\n"
    "v_mul_f32 v245, 0x3b000000, v245\n"
    "s_waitcnt lgkmcnt(11)\n"
    "v_mfma_f32_16x16x32_bf16 v[144:147], v[196:199], v[4:7], v[144:147]\n"
    "ds_read_b64_tr_b16 v[192:193], v220 offset:57344\n"
    "ds_read_b128 v[12:15], v164 offset:32768\n"
    "v_sub_f32 v237, v237, v245\n"
    "v_sub_f32 v238, v238, v245\n"
    "v_mfma_f32_16x16x32_bf16 v[148:151], v[196:199], v[68:71], v[148:151]\n"
    "ds_read_b64_tr_b16 v[194:195], v221 offset:57344\n"
    "v_sub_f32 v239, v239, v245\n"
    "v_sub_f32 v240, v240, v245\n"
    "s_waitcnt lgkmcnt(12)\n"
    "v_mfma_f32_16x16x32_bf16 v[152:155], v[200:203], v[4:7], v[152:155]\n"
    "ds_read_b64_tr_b16 v[196:197], v222 offset:57344\n"
    "ds_read_b128 v[76:79], v164 offset:34816\n"
    "v_sub_f32 v241, v241, v245\n"
    "v_sub_f32 v242, v242, v245\n"
    "v_mfma_f32_16x16x32_bf16 v[156:159], v[200:203], v[68:71], v[156:159]\n"
    "ds_read_b64_tr_b16 v[198:199], v223 offset:57344\n"
    "v_sub_f32 v243, v243, v245\n"
    "v_sub_f32 v244, v244, v245\n"
    "s_waitcnt lgkmcnt(12)\n"
    "v_mfma_f32_16x16x32_bf16 v[216:219], v[204:207], v[212:215], v[216:219]\n"
    "ds_read_b64_tr_b16 v[200:201], v224 offset:49152\n"
    "ds_read_b64_tr_b16 v[202:203], v225 offset:49152\n"
    "ds_read_b128 v[212:215], v226 offset:2048\n"
    "s_lshl_b32 s98, s72, 11\n"
    "s_lshl_b32 s87, s70, 10\n"
    "s_add_u32 s98, s98, s87\n"
    "s_add_u32 s99, s98, 0xa000\n"
    "s_or_b32 s87, s71, s69\n"
    "s_cmp_eq_u32 s87, 0\n"
    "s_cselect_b32 s91, 1, 0\n"
    "v_mul_f32 v246, v237, v237\n"
    "v_fmac_f32 v246, v238, v238\n"
    "s_waitcnt lgkmcnt(12)\n"
    "v_mfma_f32_16x16x32_bf16 v[128:131], v[208:211], v[8:11], v[128:131]\n"
    "ds_read_b64_tr_b16 v[204:205], v220 offset:53248\n"
    "v_fmac_f32 v246, v239, v239\n"
    "v_fmac_f32 v246, v240, v240\n"
    "s_waitcnt lgkmcnt(11)\n"
    "v_mfma_f32_16x16x32_bf16 v[132:135], v[208:211], v[72:75], v[132:135]\n"
    "ds_read_b64_tr_b16 v[206:207], v221 offset:53248\n"
    "v_fmac_f32 v246, v241, v241\n"
    "v_fmac_f32 v246, v242, v242\n"
    "s_waitcnt lgkmcnt(11)\n"
    "v_mfma_f32_16x16x32_bf16 v[136:139], v[252:255], v[8:11], v[136:139]\n"
    "ds_read_b64_tr_b16 v[208:209], v222 offset:53248\n"
    "v_fmac_f32 v246, v243, v243\n"
    "v_fmac_f32 v246, v244, v244\n"
    "v_mfma_f32_16x16x32_bf16 v[140:143], v[252:255], v[72:75], v[140:143]\n"
    "ds_read_b64_tr_b16 v[210:211], v223 offset:53248\n"
    "v_add_f32_dpp v246, v246, v246 quad_perm:[1,0,3,2] row_mask:0xf bank_mask:0xf\n"
    "s_nop 1\n"
    "v_add_f32_dpp v246, v246, v246 quad_perm:[2,3,0,1] row_mask:0xf bank_mask:0xf\n"
    "s_waitcnt lgkmcnt(10)\n"
    "v_mfma_f32_16x16x32_bf16 v[144:147], v[192:195], v[8:11], v[144:147]\n"
    "ds_read_b64_tr_b16 v[252:253], v220 offset:61440\n"
    "v_add_f32_dpp v246, v246, v246 row_half_mirror row_mask:0xf bank_mask:0xf\n"
    "s_nop 1\n"
    "v_add_f32_dpp v246, v246, v246 row_mirror row_mask:0xf bank_mask:0xf\n"
    "v_mfma_f32_16x16x32_bf16 v[148:151], v[192:195], v[72:75], v[148:151]\n"
    "ds_read_b64_tr_b16 v[254:255], v221 offset:61440\n"
    "v_mov_b32 v247, v246\n"
    "s_nop 1\n"
    "v_permlane16_swap_b32 v246, v247\n"
    "s_waitcnt lgkmcnt(9)\n"
    "v_mfma_f32_16x16x32_bf16 v[152:155], v[196:199], v[8:11], v[152:155]\n"
    "ds_read_b64_tr_b16 v[192:193], v222 offset:61440\n"
    "v_add_f32 v246, v246, v247\n"
    "v_mov_b32 v247, v246\n"
    "v_mfma_f32_16x16x32_bf16 v[156:159], v[196:199], v[72:75], v[156:159]\n"
    "ds_read_b64_tr_b16 v[194:195], v223 offset:61440\n"
    "v_permlane32_swap_b32 v246, v247\n"
    "s_nop 0\n"
    "v_add_f32 v246, v246, v247\n"
    "s_waitcnt lgkmcnt(8)\n"
    "v_mfma_f32_16x16x32_bf16 v[216:219], v[200:203], v[212:215], v[216:219]\n"
    "ds_read_b64_tr_b16 v[196:197], v224 offset:53248\n"
    "ds_read_b64_tr_b16 v[198:199], v225 offset:53248\n"
    "ds_read_b128 v[212:215], v227 offset:2048\n"
    "v_mov_b32 v247, 0x3727c5ac\n"
    "v_fmac_f32 v247, 0x3b000000, v246\n"
    "s_waitcnt vmcnt(4)\n"
    "s_barrier\n"
    "s_waitcnt lgkmcnt(9)\n"
    "v_mfma_f32_16x16x32_bf16 v[128:131], v[204:207], v[12:15], v[128:131]\n"
    "s_add_u32 m0, s74, 0\n"
    "s_mov_b32 s89, 0x200\n"
    "buffer_load_dwordx4 v160, s[36:39], s89 offen lds\n"
    "v_xor_b32 v220, 0x10000, v220\n"
    "v_xor_b32 v221, 0x10000, v221\n"
    "v_xor_b32 v222, 0x10000, v222\n"
    "v_xor_b32 v223, 0x10000, v223\n"
    "ds_read_b64_tr_b16 v[200:201], v220 offset:16384\n"
    "v_xor_b32 v163, 0x10000, v163\n"
    "v_xor_b32 v164, 0x10000, v164\n"
    "ds_read_b128 v[16:19], v163 offset:0\n"
    "v_mfma_f32_16x16x32_bf16 v[132:135], v[204:207], v[76:79], v[132:135]\n"
    "ds_read_b64_tr_b16 v[202:203], v221 offset:16384\n"
    "v_rsq_f32 v246, v247\n"
    "s_waitcnt lgkmcnt(10)\n"
    "v_mfma_f32_16x16x32_bf16 v[136:139], v[208:211], v[12:15], v[136:139]\n"
    "ds_read_b64_tr_b16 v[204:205], v222 offset:16384\n"
    "ds_read_b128 v[80:83], v163 offset:2048\n"
    "v_mov_b32 v248, v245\n"
    "v_mov_b32 v249, s78\n"
    "v_mfma_f32_16x16x32_bf16 v[140:143], v[208:211], v[76:79], v[140:143]\n"
    "s_add_u32 m0, s74, 0x400\n"
    "s_nop 0\n"
    "buffer_load_dwordx4 v161, s[36:39], s89 offen lds\n"
    "ds_read_b64_tr_b16 v[206:207], v223 offset:16384\n"
    "v_mov_b32 v250, v246\n"
    "s_waitcnt lgkmcnt(11)\n"
    "v_mfma_f32_16x16x32_bf16 v[144:147], v[252:255], v[12:15], v[144:147]\n"
    "ds_read_b64_tr_b16 v[208:209], v220 offset:24576\n"
    "ds_read_b128 v[20:23], v164 offset:0\n"
    "v_mov_b32 v251, s79\n"
    "v_mov_b32 v237, 0.0\n"
    "v_mfma_f32_16x16x32_bf16 v[148:151], v[252:255], v[76:79], v[148:151]\n"
    "ds_read_b64_tr_b16 v[210:211], v221 offset:24576\n"
    "s_lshl_b32 s87, s72, 3\n"
    "s_add_u32 s87, s87, s68\n"
    "s_add_u32 s87, s87, 0\n"
    "s_add_u32 s87, s87, s77\n"
    "s_lshl_b32 s87, s87, 4\n"
    "s_add_u32 s87, s87, 0x902000\n"
    "s_mov_b64 exec, 1\n"
    "buffer_store_dwordx4 v[248:251], v237, s[52:55], s87 offen sc1\n"
    "s_mov_b64 exec, -1\n"
    "s_nop 0\n"
    "v_lshlrev_b32 v237, 16, v184\n"
    "v_and_b32 v238, 0xffff0000, v184\n"
    "s_waitcnt lgkmcnt(12)\n"
    "v_mfma_f32_16x16x32_bf16 v[152:155], v[192:195], v[12:15], v[152:155]\n"
    "s_add_u32 m0, s73, 0x4000\n"
    "s_add_u32 s89, s75, 0xc0400\n"
    "buffer_load_dwordx4 v162, s[40:43], s89 offen lds\n"
    "ds_read_b64_tr_b16 v[252:253], v222 offset:24576\n"
    "ds_read_b128 v[84:87], v164 offset:2048\n"
    "v_lshlrev_b32 v239, 16, v185\n"
    "v_mfma_f32_16x16x32_bf16 v[156:159], v[192:195], v[76:79], v[156:159]\n"
    "ds_read_b64_tr_b16 v[254:255], v223 offset:24576\n"
    "v_and_b32 v240, 0xffff0000, v185\n"
    "v_lshlrev_b32 v241, 16, v186\n"
    "s_waitcnt lgkmcnt(12)\n"
    "v_mfma_f32_16x16x32_bf16 v[216:219], v[196:199], v[212:215], v[216:219]\n"
    "v_xor_b32 v224, 0x10000, v224\n"
    "v_xor_b32 v225, 0x10000, v225\n"
    "ds_read_b64_tr_b16 v[192:193], v224 offset:16384\n"
    "ds_read_b64_tr_b16 v[194:195], v225 offset:16384\n"
    "ds_read_b128 v[212:215], v226 offset:4096\n"
    "s_waitcnt lgkmcnt(12)\n"
    "v_mfma_f32_16x16x32_bf16 v[128:131], v[200:203], v[16:19], v[128:131]\n"
    "s_add_u32 m0, s73, 0x6000\n"
    "s_add_u32 s89, s75, 0xc0000\n"
    "buffer_load_dwordx4 v162, s[40:43], s89 offen lds\n"
    "ds_read_b64_tr_b16 v[196:197], v220 offset:20480\n"
    "v_and_b32 v242, 0xffff0000, v186\n"
    "s_waitcnt lgkmcnt(11)\n"
    "v_mfma_f32_16x16x32_bf16 v[132:135], v[200:203], v[80:83], v[132:135]\n"
    "ds_read_b64_tr_b16 v[198:199], v221 offset:20480\n"
    "v_lshlrev_b32 v243, 16, v187\n"
    "v_and_b32 v244, 0xffff0000, v187\n"
    "s_waitcnt lgkmcnt(11)\n"
    "v_mfma_f32_16x16x32_bf16 v[136:139], v[204:207], v[16:19], v[136:139]\n"
    "ds_read_b64_tr_b16 v[200:201], v222 offset:20480\n"
    "v_add_f32 v245, v237, v238\n"
    "v_add_f32 v246, v239, v240\n"
    "v_mfma_f32_16x16x32_bf16 v[140:143], v[204:207], v[80:83], v[140:143]\n"
    "ds_read_b64_tr_b16 v[202:203], v223 offset:20480\n"
    "v_add_f32 v245, v245, v241\n"
    "v_add_f32 v246, v246, v242\n"
    "s_waitcnt lgkmcnt(10)\n"
    "v_mfma_f32_16x16x32_bf16 v[144:147], v[208:211], v[16:19], v[144:147]\n"
    "ds_read_b64_tr_b16 v[204:205], v220 offset:28672\n"
    "v_add_f32 v245, v245, v243\n"
    "v_add_f32 v246, v246, v244\n"
    "v_mfma_f32_16x16x32_bf16 v[148:151], v[208:211], v[80:83], v[148:151]\n"
    "ds_read_b64_tr_b16 v[206:207], v221 offset:28672\n"
    "v_add_f32 v245, v245, v246\n"
    "s_nop 1\n"
    "v_add_f32_dpp v245, v245, v245 quad_perm:[1,0,3,2] row_mask:0xf bank_mask:0xf\n"
    "s_waitcnt lgkmcnt(9)\n"
    "v_mfma_f32_16x16x32_bf16 v[152:155], v[252:255], v[16:19], v[152:155]\n"
    "ds_read_b64_tr_b16 v[208:209], v222 offset:28672\n"
    "v_add_f32_dpp v245, v245, v245 quad_perm:[2,3,0,1] row_mask:0xf bank_mask:0xf\n"
    "s_nop 1\n"
    "v_add_f32_dpp v245, v245, v245 row_half_mirror row_mask:0xf bank_mask:0xf\n"
    "v_mfma_f32_16x16x32_bf16 v[156:159], v[252:255], v[80:83], v[156:159]\n"
    "ds_read_b64_tr_b16 v[210:211], v223 offset:28672\n"
    "v_add_f32_dpp v245, v245, v245 row_mirror row_mask:0xf bank_mask:0xf\n"
    "s_nop 0\n"
    "v_mov_b32 v247, v245\n"
    "s_waitcnt lgkmcnt(8)\n"
    "v_mfma_f32_16x16x32_bf16 v[216:219], v[192:195], v[212:215], v[216:219]\n"
    "ds_read_b64_tr_b16 v[252:253], v224 offset:20480\n"
    "ds_read_b64_tr_b16 v[254:255], v225 offset:20480\n"
    "ds_read_b128 v[212:215], v227 offset:4096\n"
    "v_permlane16_swap_b32 v245, v247\n"
    "s_nop 0\n"
    "v_add_f32 v245, v245, v247\n"
    "s_waitcnt vmcnt(5)\n"
    "s_barrier\n"
    "s_waitcnt lgkmcnt(9)\n"
    "v_mfma_f32_16x16x32_bf16 v[128:131], v[196:199], v[20:23], v[128:131]\n"
    "s_add_u32 m0, s74, 0x8000\n"
    "s_mov_b32 s89, 0x280\n"
    "buffer_load_dwordx4 v160, s[36:39], s89 offen lds\n"
    "ds_read_b64_tr_b16 v[192:193], v220 offset:49152\n"
    "ds_read_b128 v[24:27], v163 offset:32768\n"
    "v_mov_b32 v247, v245\n"
    "v_mfma_f32_16x16x32_bf16 v[132:135], v[196:199], v[84:87], v[132:135]\n"
    "ds_read_b64_tr_b16 v[194:195], v221 offset:49152\n"
    "v_permlane32_swap_b32 v245, v247\n"
    "s_nop 0\n"
    "v_add_f32 v245, v245, v247\n"
    "s_waitcnt lgkmcnt(10)\n"
    "v_mfma_f32_16x16x32_bf16 v[136:139], v[200:203], v[20:23], v[136:139]\n"
    "ds_read_b64_tr_b16 v[196:197], v222 offset:49152\n"
    "ds_read_b128 v[88:91], v163 offset:34816\n"
    "v_mul_f32 v245, 0x3b000000, v245\n"
    "v_sub_f32 v237, v237, v245\n"
    "v_mfma_f32_16x16x32_bf16 v[140:143], v[200:203], v[84:87], v[140:143]\n"
    "s_add_u32 m0, s74, 0x8400\n"
    "s_nop 0\n"
    "buffer_load_dwordx4 v161, s[36:39], s89 offen lds\n"
    "ds_read_b64_tr_b16 v[198:199], v223 offset:49152\n"
    "v_sub_f32 v238, v238, v245\n"
    "s_waitcnt lgkmcnt(11)\n"
    "v_mfma_f32_16x16x32_bf16 v[144:147], v[204:207], v[20:23], v[144:147]\n"
    "ds_read_b64_tr_b16 v[200:201], v220 offset:57344\n"
    "ds_read_b128 v[28:31], v164 offset:32768\n"
    "v_sub_f32 v239, v239, v245\n"
    "v_sub_f32 v240, v240, v245\n"
    "v_mfma_f32_16x16x32_bf16 v[148:151], v[204:207], v[84:87], v[148:151]\n"
    "ds_read_b64_tr_b16 v[202:203], v221 offset:57344\n"
    "v_sub_f32 v241, v241, v245\n"
    "v_sub_f32 v242, v242, v245\n"
    "s_waitcnt lgkmcnt(12)\n"
    "v_mfma_f32_16x16x32_bf16 v[152:155], v[208:211], v[20:23], v[152:155]\n"
    "s_add_u32 m0, s73, 0xc000\n"
    "s_add_u32 s89, s75, 0xf0400\n"
    "buffer_load_dwordx4 v162, s[40:43], s89 offen lds\n"
    "ds_read_b64_tr_b16 v[204:205], v222 offset:57344\n"
    "ds_read_b128 v[92:95], v164 offset:34816\n"
    "v_sub_f32 v243, v243, v245\n"
    "v_mfma_f32_16x16x32_bf16 v[156:159], v[208:211], v[84:87], v[156:159]\n"
    "ds_read_b64_tr_b16 v[206:207], v223 offset:57344\n"
    "v_sub_f32 v244, v244, v245\n"
    "v_mul_f32 v246, v237, v237\n"
    "s_waitcnt lgkmcnt(12)\n"
    "v_mfma_f32_16x16x32_bf16 v[216:219], v[252:255], v[212:215], v[216:219]\n"
    "ds_read_b64_tr_b16 v[208:209], v224 offset:49152\n"
    "ds_read_b64_tr_b16 v[210:211], v225 offset:49152\n"
    "ds_read_b128 v[212:215], v226 offset:6144\n"
    "v_fmac_f32 v246, v238, v238\n"
    "v_fmac_f32 v246, v239, v239\n"
    "s_waitcnt lgkmcnt(12)\n"
    "v_mfma_f32_16x16x32_bf16 v[128:131], v[192:195], v[24:27], v[128:131]\n"
    "s_add_u32 m0, s73, 0xe000\n"
    "s_add_u32 s89, s75, 0xf0000\n"
    "buffer_load_dwordx4 v162, s[40:43], s89 offen lds\n"
    "ds_read_b64_tr_b16 v[252:253], v220 offset:53248\n"
    "v_fmac_f32 v246, v240, v240\n"
    "s_waitcnt lgkmcnt(11)\n"
    "v_mfma_f32_16x16x32_bf16 v[132:135], v[192:195], v[88:91], v[132:135]\n"
    "ds_read_b64_tr_b16 v[254:255], v221 offset:53248\n"
    "v_fmac_f32 v246, v241, v241\n"
    "v_fmac_f32 v246, v242, v242\n"
    "s_waitcnt lgkmcnt(11)\n"
    "v_mfma_f32_16x16x32_bf16 v[136:139], v[196:199], v[24:27], v[136:139]\n"
    "ds_read_b64_tr_b16 v[192:193], v222 offset:53248\n"
    "v_fmac_f32 v246, v243, v243\n"
    "v_fmac_f32 v246, v244, v244\n"
    "v_mfma_f32_16x16x32_bf16 v[140:143], v[196:199], v[88:91], v[140:143]\n"
    "ds_read_b64_tr_b16 v[194:195], v223 offset:53248\n"
    "v_add_f32_dpp v246, v246, v246 quad_perm:[1,0,3,2] row_mask:0xf bank_mask:0xf\n"
    "s_nop 1\n"
    "v_add_f32_dpp v246, v246, v246 quad_perm:[2,3,0,1] row_mask:0xf bank_mask:0xf\n"
    "s_waitcnt lgkmcnt(10)\n"
    "v_mfma_f32_16x16x32_bf16 v[144:147], v[200:203], v[24:27], v[144:147]\n"
    "ds_read_b64_tr_b16 v[196:197], v220 offset:61440\n"
    "v_add_f32_dpp v246, v246, v246 row_half_mirror row_mask:0xf bank_mask:0xf\n"
    "s_nop 1\n"
    "v_add_f32_dpp v246, v246, v246 row_mirror row_mask:0xf bank_mask:0xf\n"
    "v_mfma_f32_16x16x32_bf16 v[148:151], v[200:203], v[88:91], v[148:151]\n"
    "ds_read_b64_tr_b16 v[198:199], v221 offset:61440\n"
    "v_mov_b32 v247, v246\n"
    "s_nop 1\n"
    "v_permlane16_swap_b32 v246, v247\n"
    "s_waitcnt lgkmcnt(9)\n"
    "v_mfma_f32_16x16x32_bf16 v[152:155], v[204:207], v[24:27], v[152:155]\n"
    "ds_read_b64_tr_b16 v[200:201], v222 offset:61440\n"
    "v_add_f32 v246, v246, v247\n"
    "v_mov_b32 v247, v246\n"
    "v_mfma_f32_16x16x32_bf16 v[156:159], v[204:207], v[88:91], v[156:159]\n"
    "ds_read_b64_tr_b16 v[202:203], v223 offset:61440\n"
    "v_permlane32_swap_b32 v246, v247\n"
    "s_nop 0\n"
    "v_add_f32 v246, v246, v247\n"
    "s_waitcnt lgkmcnt(8)\n"
    "v_mfma_f32_16x16x32_bf16 v[216:219], v[208:211], v[212:215], v[216:219]\n"
    "ds_read_b64_tr_b16 v[204:205], v224 offset:53248\n"
    "ds_read_b64_tr_b16 v[206:207], v225 offset:53248\n"
    "ds_read_b128 v[212:215], v227 offset:6144\n"
    "v_mov_b32 v247, 0x3727c5ac\n"
    "v_fmac_f32 v247, 0x3b000000, v246\n"
    "s_waitcnt vmcnt(4)\n"
    "s_barrier\n"
    "s_waitcnt lgkmcnt(9)\n"
    "v_mfma_f32_16x16x32_bf16 v[128:131], v[252:255], v[28:31], v[128:131]\n"
    "s_add_u32 m0, s74, 0x10000\n"
    "s_mov_b32 s89, 0x300\n"
    "buffer_load_dwordx4 v160, s[36:39], s89 offen lds\n"
    "v_xor_b32 v220, 0x10000, v220\n"
    "v_xor_b32 v221, 0x10000, v221\n"
    "v_xor_b32 v222, 0x10000, v222\n"
    "v_xor_b32 v223, 0x10000, v223\n"
    "ds_read_b64_tr_b16 v[208:209], v220 offset:16384\n"
    "v_xor_b32 v163, 0x10000, v163\n"
    "v_xor_b32 v164, 0x10000, v164\n"
    "ds_read_b128 v[32:35], v163 offset:0\n"
    "v_mfma_f32_16x16x32_bf16 v[132:135], v[252:255], v[92:95], v[132:135]\n"
    "ds_read_b64_tr_b16 v[210:211], v221 offset:16384\n"
    "v_rsq_f32 v246, v247\n"
    "s_waitcnt lgkmcnt(10)\n"
    "v_mfma_f32_16x16x32_bf16 v[136:139], v[192:195], v[28:31], v[136:139]\n"
    "ds_read_b64_tr_b16 v[252:253], v222 offset:16384\n"
    "ds_read_b128 v[96:99], v163 offset:2048\n"
    "s_lshl_b32 s87, s68, 3\n"
    "s_add_u32 s87, s87, 0\n"
    "v_mov_b32 v237, s87\n"
    "s_mov_b64 exec, 1\n"
    "ds_write_b32 v237, v245 offset:28160\n"
    "ds_write_b32 v237, v246 offset:28736\n"
    "s_mov_b64 exec, -1\n"
    "v_lshlrev_b32 v237, 16, v188\n"
    "s_nop 2\n"
    "v_mfma_f32_16x16x32_bf16 v[140:143], v[192:195], v[92:95], v[140:143]\n"
    "s_add_u32 m0, s74, 0x10400\n"
    "s_nop 0\n"
    "buffer_load_dwordx4 v161, s[36:39], s89 offen lds\n"
    "ds_read_b64_tr_b16 v[254:255], v223 offset:16384\n"
    "v_and_b32 v238, 0xffff0000, v188\n"
    "s_waitcnt lgkmcnt(13)\n"
    "v_mfma_f32_16x16x32_bf16 v[144:147], v[196:199], v[28:31], v[144:147]\n"
    "ds_read_b64_tr_b16 v[192:193], v220 offset:24576\n"
    "ds_read_b128 v[36:39], v164 offset:0\n"
    "v_lshlrev_b32 v239, 16, v189\n"
    "v_and_b32 v240, 0xffff0000, v189\n"
    "v_mfma_f32_16x16x32_bf16 v[148:151], v[196:199], v[92:95], v[148:151]\n"
    "ds_read_b64_tr_b16 v[194:195], v221 offset:24576\n"
    "v_lshlrev_b32 v241, 16, v190\n"
    "v_and_b32 v242, 0xffff0000, v190\n"
    "s_waitcnt lgkmcnt(14)\n"
    "v_mfma_f32_16x16x32_bf16 v[152:155], v[200:203], v[28:31], v[152:155]\n"
    "s_add_u32 m0, s73, 0x14000\n"
    "s_add_u32 s89, s75, 0x120400\n"
    "buffer_load_dwordx4 v162, s[40:43], s89 offen lds\n"
    "ds_read_b64_tr_b16 v[196:197], v222 offset:24576\n"
    "ds_read_b128 v[100:103], v164 offset:2048\n"
    "v_lshlrev_b32 v243, 16, v191\n"
    "v_mfma_f32_16x16x32_bf16 v[156:159], v[200:203], v[92:95], v[156:159]\n"
    "ds_read_b64_tr_b16 v[198:199], v223 offset:24576\n"
    "v_and_b32 v244, 0xffff0000, v191\n"
    "v_add_f32 v245, v237, v238\n"
    "s_waitcnt lgkmcnt(14)\n"
    "v_mfma_f32_16x16x32_bf16 v[216:219], v[204:207], v[212:215], v[216:219]\n"
    "v_xor_b32 v224, 0x10000, v224\n"
    "v_xor_b32 v225, 0x10000, v225\n"
    "ds_read_b64_tr_b16 v[200:201], v224 offset:16384\n"
    "ds_read_b64_tr_b16 v[202:203], v225 offset:16384\n"
    "ds_read_b128 v[212:215], v226 offset:8192\n"
    "s_waitcnt lgkmcnt(14)\n"
    "v_mfma_f32_16x16x32_bf16 v[128:131], v[208:211], v[32:35], v[128:131]\n"
    "s_add_u32 m0, s73, 0x16000\n"
    "s_add_u32 s89, s75, 0x120000\n"
    "buffer_load_dwordx4 v162, s[40:43], s89 offen lds\n"
    "ds_read_b64_tr_b16 v[204:205], v220 offset:20480\n"
    "v_add_f32 v246, v239, v240\n"
    "s_waitcnt lgkmcnt(13)\n"
    "v_mfma_f32_16x16x32_bf16 v[132:135], v[208:211], v[96:99], v[132:135]\n"
    "ds_read_b64_tr_b16 v[206:207], v221 offset:20480\n"
    "v_add_f32 v245, v245, v241\n"
    "v_add_f32 v246, v246, v242\n"
    "s_waitcnt lgkmcnt(11)\n"
    "v_mfma_f32_16x16x32_bf16 v[136:139], v[252:255], v[32:35], v[136:139]\n"
    "ds_read_b64_tr_b16 v[208:209], v222 offset:20480\n"
    "v_add_f32 v245, v245, v243\n"
    "v_add_f32 v246, v246, v244\n"
    "v_mfma_f32_16x16x32_bf16 v[140:143], v[252:255], v[96:99], v[140:143]\n"
    "ds_read_b64_tr_b16 v[210:211], v223 offset:20480\n"
    "v_add_f32 v245, v245, v246\n"
    "s_nop 1\n"
    "v_add_f32_dpp v245, v245, v245 quad_perm:[1,0,3,2] row_mask:0xf bank_mask:0xf\n"
    "s_waitcnt lgkmcnt(10)\n"
    "v_mfma_f32_16x16x32_bf16 v[144:147], v[192:195], v[32:35], v[144:147]\n"
    "ds_read_b64_tr_b16 v[252:253], v220 offset:28672\n"
    "v_add_f32_dpp v245, v245, v245 quad_perm:[2,3,0,1] row_mask:0xf bank_mask:0xf\n"
    "s_nop 1\n"
    "v_add_f32_dpp v245, v245, v245 row_half_mirror row_mask:0xf bank_mask:0xf\n"
    "v_mfma_f32_16x16x32_bf16 v[148:151], v[192:195], v[96:99], v[148:151]\n"
    "ds_read_b64_tr_b16 v[254:255], v221 offset:28672\n"
    "v_add_f32_dpp v245, v245, v245 row_mirror row_mask:0xf bank_mask:0xf\n"
    "s_nop 0\n"
    "v_mov_b32 v247, v245\n"
    "s_waitcnt lgkmcnt(9)\n"
    "v_mfma_f32_16x16x32_bf16 v[152:155], v[196:199], v[32:35], v[152:155]\n"
    "ds_read_b64_tr_b16 v[192:193], v222 offset:28672\n"
    "v_permlane16_swap_b32 v245, v247\n"
    "s_nop 0\n"
    "v_add_f32 v245, v245, v247\n"
    "v_mfma_f32_16x16x32_bf16 v[156:159], v[196:199], v[96:99], v[156:159]\n"
    "ds_read_b64_tr_b16 v[194:195], v223 offset:28672\n"
    "v_mov_b32 v247, v245\n"
    "s_nop 1\n"
    "v_permlane32_swap_b32 v245, v247\n"
    "s_waitcnt lgkmcnt(8)\n"
    "v_mfma_f32_16x16x32_bf16 v[216:219], v[200:203], v[212:215], v[216:219]\n"
    "ds_read_b64_tr_b16 v[196:197], v224 offset:20480\n"
    "ds_read_b64_tr_b16 v[198:199], v225 offset:20480\n"
    "ds_read_b128 v[212:215], v227 offset:8192\n"
    "v_add_f32 v245, v245, v247\n"
    "v_mul_f32 v245, 0x3b000000, v245\n"
    "s_waitcnt vmcnt(4)\n"
    "s_barrier\n"
    "s_waitcnt lgkmcnt(9)\n"
    "v_mfma_f32_16x16x32_bf16 v[128:131], v[204:207], v[36:39], v[128:131]\n"
    "s_add_u32 m0, s74, 0x18000\n"
    "s_mov_b32 s89, 0x380\n"
    "buffer_load_dwordx4 v160, s[36:39], s89 offen lds\n"
    "ds_read_b64_tr_b16 v[200:201], v220 offset:49152\n"
    "ds_read_b128 v[40:43], v163 offset:32768\n"
    "v_sub_f32 v237, v237, v245\n"
    "v_mfma_f32_16x16x32_bf16 v[132:135], v[204:207], v[100:103], v[132:135]\n"
    "ds_read_b64_tr_b16 v[202:203], v221 offset:49152\n"
    "v_sub_f32 v238, v238, v245\n"
    "v_sub_f32 v239, v239, v245\n"
    "s_waitcnt lgkmcnt(10)\n"
    "v_mfma_f32_16x16x32_bf16 v[136:139], v[208:211], v[36:39], v[136:139]\n"
    "ds_read_b64_tr_b16 v[204:205], v222 offset:49152\n"
    "ds_read_b128 v[104:107], v163 offset:34816\n"
    "v_sub_f32 v240, v240, v245\n"
    "v_sub_f32 v241, v241, v245\n"
    "v_mfma_f32_16x16x32_bf16 v[140:143], v[208:211], v[100:103], v[140:143]\n"
    "s_add_u32 m0, s74, 0x18400\n"
    "s_nop 0\n"
    "buffer_load_dwordx4 v161, s[36:39], s89 offen lds\n"
    "ds_read_b64_tr_b16 v[206:207], v223 offset:49152\n"
    "v_sub_f32 v242, v242, v245\n"
    "s_waitcnt lgkmcnt(11)\n"
    "v_mfma_f32_16x16x32_bf16 v[144:147], v[252:255], v[36:39], v[144:147]\n"
    "ds_read_b64_tr_b16 v[208:209], v220 offset:57344\n"
    "ds_read_b128 v[44:47], v164 offset:32768\n"
    "v_sub_f32 v243, v243, v245\n"
    "v_sub_f32 v244, v244, v245\n"
    "v_mfma_f32_16x16x32_bf16 v[148:151], v[252:255], v[100:103], v[148:151]\n"
    "ds_read_b64_tr_b16 v[210:211], v221 offset:57344\n"
    "v_mul_f32 v246, v237, v237\n"
    "v_fmac_f32 v246, v238, v238\n"
    "s_waitcnt lgkmcnt(12)\n"
    "v_mfma_f32_16x16x32_bf16 v[152:155], v[192:195], v[36:39], v[152:155]\n"
    "s_add_u32 m0, s73, 0x1c000\n"
    "s_add_u32 s89, s75, 0x150400\n"
    "buffer_load_dwordx4 v162, s[40:43], s89 offen lds\n"
    "ds_read_b64_tr_b16 v[252:253], v222 offset:57344\n"
    "ds_read_b128 v[108:111], v164 offset:34816\n"
    "v_fmac_f32 v246, v239, v239\n"
    "v_mfma_f32_16x16x32_bf16 v[156:159], v[192:195], v[100:103], v[156:159]\n"
    "ds_read_b64_tr_b16 v[254:255], v223 offset:57344\n"
    "v_fmac_f32 v246, v240, v240\n"
    "v_fmac_f32 v246, v241, v241\n"
    "s_waitcnt lgkmcnt(12)\n"
    "v_mfma_f32_16x16x32_bf16 v[216:219], v[196:199], v[212:215], v[216:219]\n"
    "ds_read_b64_tr_b16 v[192:193], v224 offset:49152\n"
    "ds_read_b64_tr_b16 v[194:195], v225 offset:49152\n"
    "ds_read_b128 v[212:215], v226 offset:10240\n"
    "v_fmac_f32 v246, v242, v242\n"
    "v_fmac_f32 v246, v243, v243\n"
    "s_waitcnt lgkmcnt(12)\n"
    "v_mfma_f32_16x16x32_bf16 v[128:131], v[200:203], v[40:43], v[128:131]\n"
    "s_add_u32 m0, s73, 0x1e000\n"
    "s_add_u32 s89, s75, 0x150000\n"
    "buffer_load_dwordx4 v162, s[40:43], s89 offen lds\n"
    "ds_read_b64_tr_b16 v[196:197], v220 offset:53248\n"
    "v_fmac_f32 v246, v244, v244\n"
    "s_waitcnt lgkmcnt(11)\n"
    "v_mfma_f32_16x16x32_bf16 v[132:135], v[200:203], v[104:107], v[132:135]\n"
    "ds_read_b64_tr_b16 v[198:199], v221 offset:53248\n"
    "v_add_f32_dpp v246, v246, v246 quad_perm:[1,0,3,2] row_mask:0xf bank_mask:0xf\n"
    "s_nop 1\n"
    "v_add_f32_dpp v246, v246, v246 quad_perm:[2,3,0,1] row_mask:0xf bank_mask:0xf\n"
    "s_waitcnt lgkmcnt(11)\n"
    "v_mfma_f32_16x16x32_bf16 v[136:139], v[204:207], v[40:43], v[136:139]\n"
    "ds_read_b64_tr_b16 v[200:201], v222 offset:53248\n"
    "v_add_f32_dpp v246, v246, v246 row_half_mirror row_mask:0xf bank_mask:0xf\n"
    "s_nop 1\n"
    "v_add_f32_dpp v246, v246, v246 row_mirror row_mask:0xf bank_mask:0xf\n"
    "v_mfma_f32_16x16x32_bf16 v[140:143], v[204:207], v[104:107], v[140:143]\n"
    "ds_read_b64_tr_b16 v[202:203], v223 offset:53248\n"
    "v_mov_b32 v247, v246\n"
    "s_nop 1\n"
    "v_permlane16_swap_b32 v246, v247\n"
    "s_waitcnt lgkmcnt(10)\n"
    "v_mfma_f32_16x16x32_bf16 v[144:147], v[208:211], v[40:43], v[144:147]\n"
    "ds_read_b64_tr_b16 v[204:205], v220 offset:61440\n"
    "v_add_f32 v246, v246, v247\n"
    "v_mov_b32 v247, v246\n"
    "v_mfma_f32_16x16x32_bf16 v[148:151], v[208:211], v[104:107], v[148:151]\n"
    "ds_read_b64_tr_b16 v[206:207], v221 offset:61440\n"
    "v_permlane32_swap_b32 v246, v247\n"
    "s_nop 0\n"
    "v_add_f32 v246, v246, v247\n"
    "s_waitcnt lgkmcnt(9)\n"
    "v_mfma_f32_16x16x32_bf16 v[152:155], v[252:255], v[40:43], v[152:155]\n"
    "ds_read_b64_tr_b16 v[208:209], v222 offset:61440\n"
    "v_mov_b32 v247, 0x3727c5ac\n"
    "v_fmac_f32 v247, 0x3b000000, v246\n"
    "v_mfma_f32_16x16x32_bf16 v[156:159], v[252:255], v[104:107], v[156:159]\n"
    "ds_read_b64_tr_b16 v[210:211], v223 offset:61440\n"
    "v_rsq_f32 v246, v247\n"
    "s_waitcnt lgkmcnt(8)\n"
    "v_mfma_f32_16x16x32_bf16 v[216:219], v[192:195], v[212:215], v[216:219]\n"
    "ds_read_b64_tr_b16 v[252:253], v224 offset:53248\n"
    "ds_read_b64_tr_b16 v[254:255], v225 offset:53248\n"
    "ds_read_b128 v[212:215], v227 offset:10240\n"
    "s_lshl_b32 s87, s68, 3\n"
    "s_add_u32 s87, s87, 4\n"
    "v_mov_b32 v237, s87\n"
    "s_mov_b64 exec, 1\n"
    "ds_write_b32 v237, v245 offset:28160\n"
    "ds_write_b32 v237, v246 offset:28736\n"
    "s_mov_b64 exec, -1\n"
    "v_and_b32 v237, 15, v235\n"
    "s_waitcnt vmcnt(4) lgkmcnt(0)\n"
    "s_barrier\n"
    "s_nop 0\n"
    "v_mfma_f32_16x16x32_bf16 v[128:131], v[196:199], v[44:47], v[128:131]\n"
    "s_add_u32 m0, s74, 0\n"
    "s_add_u32 s89, s75, 0x800\n"
    "buffer_load_dwordx4 v176, s[40:43], s89 offen lds\n"
    "v_xor_b32 v220, 0x10000, v220\n"
    "v_xor_b32 v221, 0x10000, v221\n"
    "v_xor_b32 v222, 0x10000, v222\n"
    "v_xor_b32 v223, 0x10000, v223\n"
    "ds_read_b64_tr_b16 v[192:193], v220 offset:16384\n"
    "v_xor_b32 v163, 0x10000, v163\n"
    "v_xor_b32 v164, 0x10000, v164\n"
    "ds_read_b128 v[48:51], v163 offset:0\n"
    "v_mfma_f32_16x16x32_bf16 v[132:135], v[196:199], v[108:111], v[132:135]\n"
    "ds_read_b64_tr_b16 v[194:195], v221 offset:16384\n"
    "v_lshlrev_b32 v237, 4, v237\n"
    "s_lshl_b32 s88, s68, 4\n"
    "s_add_u32 s88, s88, s77\n"
    "s_lshl_b32 s88, s88, 4\n"
    "s_add_u32 s88, s88, 0x902000\n"
    "buffer_load_dwordx4 v[168:171], v237, s[52:55], s88 offen sc1\n"
    "v_mfma_f32_16x16x32_bf16 v[136:139], v[200:203], v[44:47], v[136:139]\n"
    "ds_read_b64_tr_b16 v[196:197], v222 offset:16384\n"
    "ds_read_b128 v[112:115], v163 offset:2048\n"
    "v_mfma_f32_16x16x32_bf16 v[140:143], v[200:203], v[108:111], v[140:143]\n"
    "s_add_u32 m0, s74, 0x400\n"
    "s_nop 0\n"
    "buffer_load_dwordx4 v177, s[40:43], s89 offen lds\n"
    "ds_read_b64_tr_b16 v[198:199], v223 offset:16384\n"
    "v_mfma_f32_16x16x32_bf16 v[144:147], v[204:207], v[44:47], v[144:147]\n"
    "ds_read_b64_tr_b16 v[200:201], v220 offset:24576\n"
    "ds_read_b128 v[52:55], v164 offset:0\n"
    "v_mfma_f32_16x16x32_bf16 v[148:151], v[204:207], v[108:111], v[148:151]\n"
    "ds_read_b64_tr_b16 v[202:203], v221 offset:24576\n"
    "v_mfma_f32_16x16x32_bf16 v[152:155], v[208:211], v[44:47], v[152:155]\n"
    "s_add_u32 m0, s74, 0x4000\n"
    "s_add_u32 s89, s75, 0x60800\n"
    "buffer_load_dwordx4 v176, s[40:43], s89 offen lds\n"
    "ds_read_b64_tr_b16 v[204:205], v222 offset:24576\n"
    "ds_read_b128 v[116:119], v164 offset:2048\n"
    "v_mfma_f32_16x16x32_bf16 v[156:159], v[208:211], v[108:111], v[156:159]\n"
    "ds_read_b64_tr_b16 v[206:207], v223 offset:24576\n"
    "v_mfma_f32_16x16x32_bf16 v[216:219], v[252:255], v[212:215], v[216:219]\n"
    "v_xor_b32 v224, 0x10000, v224\n"
    "v_xor_b32 v225, 0x10000, v225\n"
    "ds_read_b64_tr_b16 v[208:209], v224 offset:16384\n"
    "ds_read_b64_tr_b16 v[210:211], v225 offset:16384\n"
    "ds_read_b128 v[212:215], v226 offset:12288\n"
    "s_waitcnt lgkmcnt(12)\n"
    "v_mfma_f32_16x16x32_bf16 v[128:131], v[192:195], v[48:51], v[128:131]\n"
    "s_add_u32 m0, s74, 0x4400\n"
    "s_nop 0\n"
    "buffer_load_dwordx4 v177, s[40:43], s89 offen lds\n"
    "ds_read_b64_tr_b16 v[252:253], v220 offset:20480\n"
    "s_waitcnt lgkmcnt(11)\n"
    "v_mfma_f32_16x16x32_bf16 v[132:135], v[192:195], v[112:115], v[132:135]\n"
    "ds_read_b64_tr_b16 v[254:255], v221 offset:20480\n"
    "s_waitcnt lgkmcnt(11)\n"
    "v_mfma_f32_16x16x32_bf16 v[136:139], v[196:199], v[48:51], v[136:139]\n"
    "ds_read_b64_tr_b16 v[192:193], v222 offset:20480\n"
    "v_mfma_f32_16x16x32_bf16 v[140:143], v[196:199], v[112:115], v[140:143]\n"
    "ds_read_b64_tr_b16 v[194:195], v223 offset:20480\n"
    "s_waitcnt lgkmcnt(10)\n"
    "v_mfma_f32_16x16x32_bf16 v[144:147], v[200:203], v[48:51], v[144:147]\n"
    "ds_read_b64_tr_b16 v[196:197], v220 offset:28672\n"
    "v_mfma_f32_16x16x32_bf16 v[148:151], v[200:203], v[112:115], v[148:151]\n"
    "ds_read_b64_tr_b16 v[198:199], v221 offset:28672\n"
    "s_waitcnt lgkmcnt(9)\n"
    "v_mfma_f32_16x16x32_bf16 v[152:155], v[204:207], v[48:51], v[152:155]\n"
    "ds_read_b64_tr_b16 v[200:201], v222 offset:28672\n"
    "v_mfma_f32_16x16x32_bf16 v[156:159], v[204:207], v[112:115], v[156:159]\n"
    "ds_read_b64_tr_b16 v[202:203], v223 offset:28672\n"
    "s_waitcnt lgkmcnt(8)\n"
    "v_mfma_f32_16x16x32_bf16 v[216:219], v[208:211], v[212:215], v[216:219]\n"
    "ds_read_b64_tr_b16 v[204:205], v224 offset:20480\n"
    "ds_read_b64_tr_b16 v[206:207], v225 offset:20480\n"
    "ds_read_b128 v[212:215], v227 offset:12288\n"
    "s_waitcnt vmcnt(5)\n"
    "s_barrier\n"
    "s_waitcnt lgkmcnt(9)\n"
    "v_mfma_f32_16x16x32_bf16 v[128:131], v[252:255], v[52:55], v[128:131]\n"
    "s_add_u32 m0, s74, 0x8000\n"
    "s_add_u32 s89, s75, 0xc0800\n"
    "buffer_load_dwordx4 v176, s[40:43], s89 offen lds\n"
    "ds_read_b64_tr_b16 v[208:209], v220 offset:49152\n"
    "ds_read_b128 v[56:59], v163 offset:32768\n"
    "s_waitcnt vmcnt(4)\n"
    "v_cmp_eq_u32 vcc, s78, v169\n"
    "v_cmp_eq_u32 s[94:95], s79, v171\n"
    "s_nop 4\n"
    "s_and_b64 vcc, vcc, s[94:95]\n"
    "s_and_b64 vcc, vcc, exec\n"
    "s_cmp_eq_u64 vcc, exec\n"
    "s_cbranch_scc0 LA_slowstat_1\n"
    "LA_backstat_2:\n"
    "v_mfma_f32_16x16x32_bf16 v[132:135], v[252:255], v[116:119], v[132:135]\n"
    "ds_read_b64_tr_b16 v[210:211], v221 offset:49152\n"
    "v_lshrrev_b32 v238, 2, v237\n"
    "s_lshl_b32 s87, s68, 6\n"
    "v_add_u32 v238, s87, v238\n"
    "s_waitcnt lgkmcnt(10)\n"
    "v_mfma_f32_16x16x32_bf16 v[136:139], v[192:195], v[52:55], v[136:139]\n"
    "ds_read_b64_tr_b16 v[252:253], v222 offset:49152\n"
    "ds_read_b128 v[120:123], v163 offset:34816\n"
    "ds_write_b32 v238, v168 offset:27648\n"
    "ds_write_b32 v238, v170 offset:28224\n"
    "v_mfma_f32_16x16x32_bf16 v[140:143], v[192:195], v[116:119], v[140:143]\n"
    "s_add_u32 m0, s74, 0x8400\n"
    "s_nop 0\n"
    "buffer_load_dwordx4 v177, s[40:43], s89 offen lds\n"
    "ds_read_b64_tr_b16 v[254:255], v223 offset:49152\n"
    "s_waitcnt lgkmcnt(13)\n"
    "v_mfma_f32_16x16x32_bf16 v[144:147], v[196:199], v[52:55], v[144:147]\n"
    "ds_read_b64_tr_b16 v[192:193], v220 offset:57344\n"
    "ds_read_b128 v[60:63], v164 offset:32768\n"
    "v_mfma_f32_16x16x32_bf16 v[148:151], v[196:199], v[116:119], v[148:151]\n"
    "ds_read_b64_tr_b16 v[194:195], v221 offset:57344\n"
    "s_waitcnt lgkmcnt(14)\n"
    "v_mfma_f32_16x16x32_bf16 v[152:155], v[200:203], v[52:55], v[152:155]\n"
    "s_add_u32 m0, s74, 0xc000\n"
    "s_add_u32 s89, s75, 0x120800\n"
    "buffer_load_dwordx4 v176, s[40:43], s89 offen lds\n"
    "ds_read_b64_tr_b16 v[196:197], v222 offset:57344\n"
    "ds_read_b128 v[124:127], v164 offset:34816\n"
    "v_mfma_f32_16x16x32_bf16 v[156:159], v[200:203], v[116:119], v[156:159]\n"
    "ds_read_b64_tr_b16 v[198:199], v223 offset:57344\n"
    "s_waitcnt lgkmcnt(14)\n"
    "v_mfma_f32_16x16x32_bf16 v[216:219], v[204:207], v[212:215], v[216:219]\n"
    "ds_read_b64_tr_b16 v[200:201], v224 offset:49152\n"
    "ds_read_b64_tr_b16 v[202:203], v225 offset:49152\n"
    "ds_read_b128 v[212:215], v226 offset:14336\n"
    "s_waitcnt lgkmcnt(14)\n"
    "v_mfma_f32_16x16x32_bf16 v[128:131], v[208:211], v[56:59], v[128:131]\n"
    "s_add_u32 m0, s74, 0xc400\n"
    "s_nop 0\n"
    "buffer_load_dwordx4 v177, s[40:43], s89 offen lds\n"
    "ds_read_b64_tr_b16 v[204:205], v220 offset:53248\n"
    "s_waitcnt lgkmcnt(13)\n"
    "v_mfma_f32_16x16x32_bf16 v[132:135], v[208:211], v[120:123], v[132:135]\n"
    "ds_read_b64_tr_b16 v[206:207], v221 offset:53248\n"
    "s_waitcnt lgkmcnt(11)\n"
    "v_mfma_f32_16x16x32_bf16 v[136:139], v[252:255], v[56:59], v[136:139]\n"
    "ds_read_b64_tr_b16 v[208:209], v222 offset:53248\n"
    "v_mfma_f32_16x16x32_bf16 v[140:143], v[252:255], v[120:123], v[140:143]\n"
    "ds_read_b64_tr_b16 v[210:211], v223 offset:53248\n"
    "s_waitcnt lgkmcnt(10)\n"
    "v_mfma_f32_16x16x32_bf16 v[144:147], v[192:195], v[56:59], v[144:147]\n"
    "ds_read_b64_tr_b16 v[252:253], v220 offset:61440\n"
    "v_mfma_f32_16x16x32_bf16 v[148:151], v[192:195], v[120:123], v[148:151]\n"
    "ds_read_b64_tr_b16 v[254:255], v221 offset:61440\n"
    "s_waitcnt lgkmcnt(9)\n"
    "v_mfma_f32_16x16x32_bf16 v[152:155], v[196:199], v[56:59], v[152:155]\n"
    "ds_read_b64_tr_b16 v[192:193], v222 offset:61440\n"
    "v_mfma_f32_16x16x32_bf16 v[156:159], v[196:199], v[120:123], v[156:159]\n"
    "ds_read_b64_tr_b16 v[194:195], v223 offset:61440\n"
    "s_waitcnt lgkmcnt(8)\n"
    "v_mfma_f32_16x16x32_bf16 v[216:219], v[200:203], v[212:215], v[216:219]\n"
    "ds_read_b64_tr_b16 v[196:197], v224 offset:53248\n"
    "ds_read_b64_tr_b16 v[198:199], v225 offset:53248\n"
    "ds_read_b128 v[212:215], v227 offset:14336\n"
    "s_waitcnt vmcnt(6)\n"
    "s_barrier\n"
    "s_waitcnt lgkmcnt(9)\n"
    "v_mfma_f32_16x16x32_bf16 v[128:131], v[204:207], v[60:63], v[128:131]\n"
    "s_add_u32 m0, s73, 0x10000\n"
    "s_add_u32 s89, s75, 0\n"
    "buffer_load_dwordx4 v228, s[44:47], s89 offen lds\n"
    "v_xor_b32 v220, 0x10000, v220\n"
    "v_xor_b32 v221, 0x10000, v221\n"
    "v_xor_b32 v222, 0x10000, v222\n"
    "v_xor_b32 v223, 0x10000, v223\n"
    "ds_read_b64_tr_b16 v[200:201], v220 offset:0\n"
    "v_mfma_f32_16x16x32_bf16 v[132:135], v[204:207], v[124:127], v[132:135]\n"
    "ds_read_b64_tr_b16 v[202:203], v221 offset:0\n"
    "v_and_b32 v237, 15, v235\n"
    "v_lshlrev_b32 v237, 2, v237\n"
    "s_waitcnt lgkmcnt(9)\n"
    "v_mfma_f32_16x16x32_bf16 v[136:139], v[208:211], v[60:63], v[136:139]\n"
    "ds_read_b64_tr_b16 v[204:205], v222 offset:0\n"
    "s_lshl_b32 s87, s69, 7\n"
    "v_add_u32 v237, s87, v237\n"
    "ds_read_b32 v231, v237 offset:27648\n"
    "ds_read_b32 v233, v237 offset:28224\n"
    "ds_read_b32 v232, v237 offset:27712\n"
    "ds_read_b32 v234, v237 offset:28288\n"
    "v_mfma_f32_16x16x32_bf16 v[140:143], v[208:211], v[124:127], v[140:143]\n"
    "s_add_u32 m0, s73, 0x12000\n"
    "s_nop 0\n"
    "buffer_load_dwordx4 v228, s[48:51], s89 offen lds\n"
    "ds_read_b64_tr_b16 v[206:207], v223 offset:0\n"
    "s_waitcnt lgkmcnt(13)\n"
    "v_mfma_f32_16x16x32_bf16 v[144:147], v[252:255], v[60:63], v[144:147]\n"
    "ds_read_b64_tr_b16 v[208:209], v220 offset:4096\n"
    "v_mfma_f32_16x16x32_bf16 v[148:151], v[252:255], v[124:127], v[148:151]\n"
    "ds_read_b64_tr_b16 v[210:211], v221 offset:4096\n"
    "s_waitcnt lgkmcnt(13)\n"
    "v_mfma_f32_16x16x32_bf16 v[152:155], v[192:195], v[60:63], v[152:155]\n"
    "s_add_u32 m0, s73, 0x14000\n"
    "s_add_u32 s89, s75, 0x38000\n"
    "buffer_load_dwordx4 v228, s[44:47], s89 offen lds\n"
    "ds_read_b64_tr_b16 v[252:253], v222 offset:4096\n"
    "v_mfma_f32_16x16x32_bf16 v[156:159], v[192:195], v[124:127], v[156:159]\n"
    "ds_read_b64_tr_b16 v[254:255], v223 offset:4096\n"
    "s_waitcnt lgkmcnt(12)\n"
    "v_mfma_f32_16x16x32_bf16 v[216:219], v[196:199], v[212:215], v[216:219]\n"
    "s_lshl_b32 s86, s70, 12\n"
    "v_subrev_u32 v224, s86, v224\n"
    "v_subrev_u32 v225, s86, v225\n"
    "v_xor_b32 v224, 0x10000, v224\n"
    "v_xor_b32 v225, 0x10000, v225\n"
    "ds_read_b64_tr_b16 v[192:193], v224 offset:0\n"
    "ds_read_b64_tr_b16 v[194:195], v225 offset:0\n"
    "s_lshl_b32 s86, s70, 6\n"
    "v_xor_b32 v227, s86, v226\n"
    "ds_read_b128 v[212:215], v227 offset:0\n"
    "s_waitcnt lgkmcnt(13)\n"
    "v_mfma_f32_16x16x32_bf16 v[160:163], v[0:3], v[200:203], 0\n"
    "s_add_u32 m0, s73, 0x16000\n"
    "s_nop 0\n"
    "buffer_load_dwordx4 v228, s[48:51], s89 offen lds\n"
    "ds_read_b64_tr_b16 v[196:197], v220 offset:8192\n"
    "s_lshl_b32 s87, s68, 7\n"
    "v_lshrrev_b32 v237, 4, v235\n"
    "v_mfma_f32_16x16x32_bf16 v[164:167], v[64:67], v[200:203], 0\n"
    "ds_read_b64_tr_b16 v[198:199], v221 offset:8192\n"
    "v_lshlrev_b32 v237, 4, v237\n"
    "v_add_u32 v237, s87, v237\n"
    "s_waitcnt lgkmcnt(9)\n"
    "v_mfma_f32_16x16x32_bf16 v[168:171], v[0:3], v[204:207], 0\n"
    "ds_read_b64_tr_b16 v[200:201], v222 offset:8192\n"
    "s_mov_b32 exec_lo, 0x10001\n"
    "s_mov_b32 exec_hi, 0x10001\n"
    "ds_write_b128 v237, v[216:219] offset:24576\n"
    "s_mov_b64 exec, -1\n"
    "v_and_b32 v237, 15, v235\n"
    "v_lshlrev_b32 v237, 2, v237\n"
    "s_nop 1\n"
    "v_mfma_f32_16x16x32_bf16 v[172:175], v[64:67], v[204:207], 0\n"
    "ds_read_b64_tr_b16 v[202:203], v223 offset:8192\n"
    "ds_read_b32 v238, v237 offset:28160\n"
    "ds_read_b32 v239, v237 offset:28736\n"
    "s_waitcnt vmcnt(8) lgkmcnt(3)\n"
    "s_barrier\n"
    "v_mfma_f32_16x16x32_bf16 v[160:163], v[4:7], v[208:211], v[160:163]\n"
    "s_add_u32 m0, s73, 0x18000\n"
    "s_add_u32 s89, s75, 0x70000\n"
    "buffer_load_dwordx4 v228, s[44:47], s89 offen lds\n"
    "ds_read_b64_tr_b16 v[204:205], v220 offset:12288\n"
    "v_mov_b32_dpp v176, v216 row_newbcast:0 row_mask:0xf bank_mask:0xf\n"
    "v_mfma_f32_16x16x32_bf16 v[164:167], v[68:71], v[208:211], v[164:167]\n"
    "ds_read_b64_tr_b16 v[206:207], v221 offset:12288\n"
    "v_mov_b32_dpp v177, v217 row_newbcast:0 row_mask:0xf bank_mask:0xf\n"
    "v_mov_b32_dpp v178, v218 row_newbcast:0 row_mask:0xf bank_mask:0xf\n"
    "v_mfma_f32_16x16x32_bf16 v[168:171], v[4:7], v[252:255], v[168:171]\n"
    "ds_read_b64_tr_b16 v[208:209], v222 offset:12288\n"
    "v_mov_b32_dpp v179, v219 row_newbcast:0 row_mask:0xf bank_mask:0xf\n"
    "s_waitcnt lgkmcnt(4)\n"
    "v_fma_f32 v176, -v238, v176, v216\n"
    "v_mfma_f32_16x16x32_bf16 v[172:175], v[68:71], v[252:255], v[172:175]\n"
    "s_add_u32 m0, s73, 0x1a000\n"
    "s_nop 0\n"
    "buffer_load_dwordx4 v228, s[48:51], s89 offen lds\n"
    "ds_read_b64_tr_b16 v[210:211], v223 offset:12288\n"
    "v_fma_f32 v177, -v238, v177, v217\n"
    "v_fma_f32 v178, -v238, v178, v218\n"
    "v_fma_f32 v179, -v238, v179, v219\n"
    "s_waitcnt lgkmcnt(4)\n"
    "v_mul_f32 v176, v176, v239\n"
    "v_mul_f32 v177, v177, v239\n"
    "v_mul_f32 v178, v178, v239\n"
    "v_mul_f32 v179, v179, v239\n"
    "v_cvt_pk_bf16_f32 v176, v176, v177\n"
    "v_cvt_pk_bf16_f32 v177, v178, v179\n"
    "s_lshl_b32 s87, s69, 9\n"
    "v_lshlrev_b32 v237, 3, v235\n"
    "v_add_u32 v237, s87, v237\n"
    "s_cmp_eq_u32 s70, 0\n"
    "s_cbranch_scc0 LA_kh_4\n"
    "ds_write_b64 v237, v[176:177] offset:16384\n"
    "LA_kh_4:\n"
    "v_mfma_f32_16x16x32_bf16 v[216:219], v[212:215], v[192:195], 0\n"
    "ds_read_b64_tr_b16 v[252:253], v224 offset:8192\n"
    "ds_read_b64_tr_b16 v[254:255], v225 offset:8192\n"
    "ds_read_b128 v[212:215], v227 offset:2048\n"
    "ds_read_b128 v[180:183], v236 offset:24576\n"
    "ds_read_b128 v[184:187], v236 offset:25088\n"
    "v_mfma_f32_16x16x32_bf16 v[160:163], v[8:11], v[196:199], v[160:163]\n"
    "ds_read_b64_tr_b16 v[192:193], v220 offset:16384\n"
    "s_waitcnt lgkmcnt(2)\n"
    "v_fma_f32 v132, -v232, v180, v132\n"
    "v_fma_f32 v133, -v232, v181, v133\n"
    "v_mfma_f32_16x16x32_bf16 v[164:167], v[72:75], v[196:199], v[164:167]\n"
    "s_add_u32 m0, s73, 0x1c000\n"
    "s_add_u32 s89, s75, 0xa8000\n"
    "buffer_load_dwordx4 v228, s[44:47], s89 offen lds\n"
    "ds_read_b64_tr_b16 v[194:195], v221 offset:16384\n"
    "v_fma_f32 v134, -v232, v182, v134\n"
    "v_mfma_f32_16x16x32_bf16 v[168:171], v[8:11], v[200:203], v[168:171]\n"
    "ds_read_b64_tr_b16 v[196:197], v222 offset:16384\n"
    "v_fma_f32 v135, -v232, v183, v135\n"
    "s_waitcnt lgkmcnt(3)\n"
    "v_fma_f32 v148, -v232, v184, v148\n"
    "v_mfma_f32_16x16x32_bf16 v[172:175], v[72:75], v[200:203], v[172:175]\n"
    "ds_read_b64_tr_b16 v[198:199], v223 offset:16384\n"
    "v_fma_f32 v149, -v232, v185, v149\n"
    "v_fma_f32 v150, -v232, v186, v150\n"
    "v_mfma_f32_16x16x32_bf16 v[160:163], v[12:15], v[204:207], v[160:163]\n"
    "s_add_u32 m0, s73, 0x1e000\n"
    "s_nop 0\n"
    "buffer_load_dwordx4 v228, s[48:51], s89 offen lds\n"
    "ds_read_b64_tr_b16 v[200:201], v220 offset:20480\n"
    "v_fma_f32 v151, -v232, v187, v151\n"
    "v_mfma_f32_16x16x32_bf16 v[164:167], v[76:79], v[204:207], v[164:167]\n"
    "ds_read_b64_tr_b16 v[202:203], v221 offset:20480\n"
    "v_mul_f32 v132, v132, v234\n"
    "v_mul_f32 v133, v133, v234\n"
    "v_mfma_f32_16x16x32_bf16 v[168:171], v[12:15], v[208:211], v[168:171]\n"
    "ds_read_b64_tr_b16 v[204:205], v222 offset:20480\n"
    "v_mul_f32 v134, v134, v234\n"
    "v_mul_f32 v135, v135, v234\n"
    "v_mfma_f32_16x16x32_bf16 v[172:175], v[76:79], v[208:211], v[172:175]\n"
    "ds_read_b64_tr_b16 v[206:207], v223 offset:20480\n"
    "v_cvt_pk_bf16_f32 v151, v150, v151\n"
    "v_cvt_pk_bf16_f32 v150, v148, v149\n"
    "v_mfma_f32_16x16x32_bf16 v[216:219], v[212:215], v[252:255], v[216:219]\n"
    "ds_read_b64_tr_b16 v[208:209], v224 offset:16384\n"
    "ds_read_b64_tr_b16 v[210:211], v225 offset:16384\n"
    "ds_read_b128 v[212:215], v227 offset:4096\n"
    "v_cvt_pk_bf16_f32 v135, v134, v135\n"
    "v_cvt_pk_bf16_f32 v134, v132, v133\n"
    "s_waitcnt lgkmcnt(9)\n"
    "v_mfma_f32_16x16x32_bf16 v[160:163], v[16:19], v[192:195], v[160:163]\n"
    "ds_read_b64_tr_b16 v[252:253], v220 offset:24576\n"
    "s_lshl_b32 s87, s69, 11\n"
    "s_lshl_b32 s88, s70, 10\n"
    "s_add_u32 s87, s87, s88\n"
    "v_lshlrev_b32 v237, 3, v235\n"
    "v_add_u32 v237, s87, v237\n"
    "s_cmp_lt_u32 s69, 3\n"
    "s_cbranch_scc0 LA_kx_5\n"
    "ds_write_b64 v237, v[134:135] offset:18432\n"
    "LA_kx_5:\n"
    "v_mfma_f32_16x16x32_bf16 v[164:167], v[80:83], v[192:195], v[164:167]\n"
    "ds_read_b64_tr_b16 v[254:255], v221 offset:24576\n"
    "v_fma_f32 v128, -v231, v180, v128\n"
    "v_fma_f32 v129, -v231, v181, v129\n"
    "s_waitcnt lgkmcnt(9)\n"
    "v_mfma_f32_16x16x32_bf16 v[168:171], v[16:19], v[196:199], v[168:171]\n"
    "ds_read_b64_tr_b16 v[192:193], v222 offset:24576\n"
    "v_fma_f32 v130, -v231, v182, v130\n"
    "v_fma_f32 v131, -v231, v183, v131\n"
    "v_mfma_f32_16x16x32_bf16 v[172:175], v[80:83], v[196:199], v[172:175]\n"
    "ds_read_b64_tr_b16 v[194:195], v223 offset:24576\n"
    "v_fma_f32 v144, -v231, v184, v144\n"
    "v_fma_f32 v145, -v231, v185, v145\n"
    "s_waitcnt vmcnt(10) lgkmcnt(3)\n"
    "s_barrier\n"
    "v_mfma_f32_16x16x32_bf16 v[160:163], v[20:23], v[200:203], v[160:163]\n"
    "s_add_u32 m0, s73, 0\n"
    "s_add_u32 s89, s75, 0xe0000\n"
    "buffer_load_dwordx4 v228, s[44:47], s89 offen lds\n"
    "ds_read_b64_tr_b16 v[196:197], v220 offset:28672\n"
    "v_fma_f32 v146, -v231, v186, v146\n"
    "v_mfma_f32_16x16x32_bf16 v[164:167], v[84:87], v[200:203], v[164:167]\n"
    "ds_read_b64_tr_b16 v[198:199], v221 offset:28672\n"
    "v_fma_f32 v147, -v231, v187, v147\n"
    "v_mul_f32 v128, v128, v233\n"
    "v_mfma_f32_16x16x32_bf16 v[168:171], v[20:23], v[204:207], v[168:171]\n"
    "ds_read_b64_tr_b16 v[200:201], v222 offset:28672\n"
    "v_mul_f32 v129, v129, v233\n"
    "v_mul_f32 v130, v130, v233\n"
    "v_mfma_f32_16x16x32_bf16 v[172:175], v[84:87], v[204:207], v[172:175]\n"
    "s_add_u32 m0, s73, 0x2000\n"
    "s_nop 0\n"
    "buffer_load_dwordx4 v228, s[48:51], s89 offen lds\n"
    "ds_read_b64_tr_b16 v[202:203], v223 offset:28672\n"
    "v_mul_f32 v131, v131, v233\n"
    "v_mfma_f32_16x16x32_bf16 v[216:219], v[212:215], v[208:211], v[216:219]\n"
    "ds_read_b64_tr_b16 v[204:205], v224 offset:24576\n"
    "ds_read_b64_tr_b16 v[206:207], v225 offset:24576\n"
    "ds_read_b128 v[212:215], v227 offset:6144\n"
    "v_cvt_pk_bf16_f32 v148, v144, v145\n"
    "v_cvt_pk_bf16_f32 v149, v146, v147\n"
    "s_waitcnt lgkmcnt(9)\n"
    "v_mfma_f32_16x16x32_bf16 v[160:163], v[24:27], v[252:255], v[160:163]\n"
    "ds_read_b64_tr_b16 v[208:209], v220 offset:32768\n"
    "v_cvt_pk_bf16_f32 v132, v128, v129\n"
    "v_cvt_pk_bf16_f32 v133, v130, v131\n"
    "v_mfma_f32_16x16x32_bf16 v[164:167], v[88:91], v[252:255], v[164:167]\n"
    "ds_read_b64_tr_b16 v[210:211], v221 offset:32768\n"
    "ds_read_b128 v[180:183], v236 offset:24704\n"
    "ds_read_b128 v[184:187], v236 offset:25216\n"
    "s_waitcnt lgkmcnt(11)\n"
    "v_mfma_f32_16x16x32_bf16 v[168:171], v[24:27], v[192:195], v[168:171]\n"
    "ds_read_b64_tr_b16 v[252:253], v222 offset:32768\n"
    "s_waitcnt lgkmcnt(2)\n"
    "v_fma_f32 v140, -v232, v180, v140\n"
    "v_fma_f32 v141, -v232, v181, v141\n"
    "v_mfma_f32_16x16x32_bf16 v[172:175], v[88:91], v[192:195], v[172:175]\n"
    "ds_read_b64_tr_b16 v[254:255], v223 offset:32768\n"
    "v_fma_f32 v142, -v232, v182, v142\n"
    "v_fma_f32 v143, -v232, v183, v143\n"
    "v_mfma_f32_16x16x32_bf16 v[160:163], v[28:31], v[196:199], v[160:163]\n"
    "ds_read_b64_tr_b16 v[192:193], v220 offset:36864\n"
    "s_waitcnt lgkmcnt(3)\n"
    "v_fma_f32 v156, -v232, v184, v156\n"
    "v_fma_f32 v157, -v232, v185, v157\n"
    "v_mfma_f32_16x16x32_bf16 v[164:167], v[92:95], v[196:199], v[164:167]\n"
    "ds_read_b64_tr_b16 v[194:195], v221 offset:36864\n"
    "v_fma_f32 v158, -v232, v186, v158\n"
    "v_fma_f32 v159, -v232, v187, v159\n"
    "v_mfma_f32_16x16x32_bf16 v[168:171], v[28:31], v[200:203], v[168:171]\n"
    "ds_read_b64_tr_b16 v[196:197], v222 offset:36864\n"
    "v_mul_f32 v140, v140, v234\n"
    "v_mul_f32 v141, v141, v234\n"
    "v_mfma_f32_16x16x32_bf16 v[172:175], v[92:95], v[200:203], v[172:175]\n"
    "ds_read_b64_tr_b16 v[198:199], v223 offset:36864\n"
    "v_mul_f32 v142, v142, v234\n"
    "v_mul_f32 v143, v143, v234\n"
    "v_mfma_f32_16x16x32_bf16 v[216:219], v[212:215], v[204:207], v[216:219]\n"
    "ds_read_b64_tr_b16 v[200:201], v224 offset:32768\n"
    "ds_read_b64_tr_b16 v[202:203], v225 offset:32768\n"
    "ds_read_b128 v[212:215], v227 offset:8192\n"
    "v_cvt_pk_bf16_f32 v159, v158, v159\n"
    "v_cvt_pk_bf16_f32 v158, v156, v157\n"
    "v_mfma_f32_16x16x32_bf16 v[160:163], v[32:35], v[208:211], v[160:163]\n"
    "ds_read_b64_tr_b16 v[204:205], v220 offset:40960\n"
    "v_cvt_pk_bf16_f32 v143, v142, v143\n"
    "v_cvt_pk_bf16_f32 v142, v140, v141\n"
    "v_mfma_f32_16x16x32_bf16 v[164:167], v[96:99], v[208:211], v[164:167]\n"
    "ds_read_b64_tr_b16 v[206:207], v221 offset:40960\n"
    "s_lshl_b32 s87, s69, 11\n"
    "s_lshl_b32 s88, s70, 10\n"
    "s_add_u32 s87, s87, s88\n"
    "v_lshlrev_b32 v237, 3, v235\n"
    "v_add_u32 v237, s87, v237\n"
    "s_cmp_lt_u32 s69, 3\n"
    "s_cbranch_scc0 LA_kx_6\n"
    "ds_write_b64 v237, v[142:143] offset:18944\n"
    "LA_kx_6:\n"
    "s_waitcnt lgkmcnt(9)\n"
    "v_mfma_f32_16x16x32_bf16 v[168:171], v[32:35], v[252:255], v[168:171]\n"
    "ds_read_b64_tr_b16 v[208:209], v222 offset:40960\n"
    "v_fma_f32 v136, -v231, v180, v136\n"
    "v_fma_f32 v137, -v231, v181, v137\n"
    "v_mfma_f32_16x16x32_bf16 v[172:175], v[96:99], v[252:255], v[172:175]\n"
    "ds_read_b64_tr_b16 v[210:211], v223 offset:40960\n"
    "v_fma_f32 v138, -v231, v182, v138\n"
    "v_fma_f32 v139, -v231, v183, v139\n"
    "s_waitcnt vmcnt(10) lgkmcnt(2)\n"
    "s_barrier\n"
    "v_mfma_f32_16x16x32_bf16 v[160:163], v[36:39], v[192:195], v[160:163]\n"
    "s_add_u32 m0, s73, 0x4000\n"
    "s_add_u32 s89, s75, 0x118000\n"
    "buffer_load_dwordx4 v228, s[44:47], s89 offen lds\n"
    "ds_read_b64_tr_b16 v[252:253], v220 offset:45056\n"
    "v_fma_f32 v152, -v231, v184, v152\n"
    "v_mfma_f32_16x16x32_bf16 v[164:167], v[100:103], v[192:195], v[164:167]\n"
    "ds_read_b64_tr_b16 v[254:255], v221 offset:45056\n"
    "v_fma_f32 v153, -v231, v185, v153\n"
    "v_fma_f32 v154, -v231, v186, v154\n"
    "v_mfma_f32_16x16x32_bf16 v[168:171], v[36:39], v[196:199], v[168:171]\n"
    "ds_read_b64_tr_b16 v[192:193], v222 offset:45056\n"
    "v_fma_f32 v155, -v231, v187, v155\n"
    "v_mul_f32 v136, v136, v233\n"
    "v_mfma_f32_16x16x32_bf16 v[172:175], v[100:103], v[196:199], v[172:175]\n"
    "s_add_u32 m0, s73, 0x6000\n"
    "s_nop 0\n"
    "buffer_load_dwordx4 v228, s[48:51], s89 offen lds\n"
    "ds_read_b64_tr_b16 v[194:195], v223 offset:45056\n"
    "v_mul_f32 v137, v137, v233\n"
    "v_mfma_f32_16x16x32_bf16 v[216:219], v[212:215], v[200:203], v[216:219]\n"
    "ds_read_b64_tr_b16 v[196:197], v224 offset:40960\n"
    "ds_read_b64_tr_b16 v[198:199], v225 offset:40960\n"
    "ds_read_b128 v[212:215], v227 offset:10240\n"
    "v_mul_f32 v138, v138, v233\n"
    "v_mul_f32 v139, v139, v233\n"
    "v_mfma_f32_16x16x32_bf16 v[160:163], v[40:43], v[204:207], v[160:163]\n"
    "ds_read_b64_tr_b16 v[200:201], v220 offset:49152\n"
    "v_cvt_pk_bf16_f32 v156, v152, v153\n"
    "v_cvt_pk_bf16_f32 v157, v154, v155\n"
    "v_mfma_f32_16x16x32_bf16 v[164:167], v[104:107], v[204:207], v[164:167]\n"
    "ds_read_b64_tr_b16 v[202:203], v221 offset:49152\n"
    "v_cvt_pk_bf16_f32 v140, v136, v137\n"
    "v_cvt_pk_bf16_f32 v141, v138, v139\n"
    "s_waitcnt lgkmcnt(9)\n"
    "v_mfma_f32_16x16x32_bf16 v[168:171], v[40:43], v[208:211], v[168:171]\n"
    "ds_read_b64_tr_b16 v[204:205], v222 offset:49152\n"
    "v_and_b32 v237, 1, v235\n"
    "v_cmp_eq_u32 s[80:81], 1, v237\n"
    "v_mfma_f32_16x16x32_bf16 v[172:175], v[104:107], v[208:211], v[172:175]\n"
    "ds_read_b64_tr_b16 v[206:207], v223 offset:49152\n"
    "v_lshrrev_b32 v238, 4, v235\n"
    "v_lshl_add_u32 v238, v238, 2, v237\n"
    "s_waitcnt lgkmcnt(9)\n"
    "v_mfma_f32_16x16x32_bf16 v[160:163], v[44:47], v[252:255], v[160:163]\n"
    "ds_read_b64_tr_b16 v[208:209], v220 offset:53248\n"
    "v_and_b32 v239, 15, v235\n"
    "v_cmp_le_u32 s[82:83], v238, v239\n"
    "v_mfma_f32_16x16x32_bf16 v[164:167], v[108:111], v[252:255], v[164:167]\n"
    "ds_read_b64_tr_b16 v[210:211], v221 offset:53248\n"
    "v_add_u32 v238, 2, v238\n"
    "v_cmp_le_u32 s[84:85], v238, v239\n"
    "s_waitcnt lgkmcnt(9)\n"
    "v_mfma_f32_16x16x32_bf16 v[168:171], v[44:47], v[192:195], v[168:171]\n"
    "ds_read_b64_tr_b16 v[252:253], v222 offset:53248\n"
    "v_mov_b32 v237, 0x0c0c0100\n"
    "v_mov_b32 v238, 0x01000c0c\n"
    "v_mfma_f32_16x16x32_bf16 v[172:175], v[108:111], v[192:195], v[172:175]\n"
    "ds_read_b64_tr_b16 v[254:255], v223 offset:53248\n"
    "v_cndmask_b32 v237, v237, v238, s[80:81]\n"
    "v_mov_b32 v238, 0x0c0c0c0c\n"
    "s_waitcnt lgkmcnt(8)\n"
    "v_mfma_f32_16x16x32_bf16 v[216:219], v[212:215], v[196:199], v[216:219]\n"
    "ds_read_b64_tr_b16 v[192:193], v224 offset:49152\n"
    "ds_read_b64_tr_b16 v[194:195], v225 offset:49152\n"
    "ds_read_b128 v[212:215], v227 offset:12288\n"
    "v_cndmask_b32 v248, v237, v238, s[82:83]\n"
    "v_cndmask_b32 v250, v238, v237, s[82:83]\n"
    "s_waitcnt lgkmcnt(9)\n"
    "v_mfma_f32_16x16x32_bf16 v[160:163], v[48:51], v[200:203], v[160:163]\n"
    "ds_read_b64_tr_b16 v[196:197], v220 offset:57344\n"
    "v_mov_b32 v237, 0x0c0c0302\n"
    "v_mov_b32 v239, 0x03020c0c\n"
    "v_mfma_f32_16x16x32_bf16 v[164:167], v[112:115], v[200:203], v[164:167]\n"
    "ds_read_b64_tr_b16 v[198:199], v221 offset:57344\n"
    "v_cndmask_b32 v237, v237, v239, s[80:81]\n"
    "v_cndmask_b32 v249, v237, v238, s[84:85]\n"
    "s_waitcnt lgkmcnt(9)\n"
    "v_mfma_f32_16x16x32_bf16 v[168:171], v[48:51], v[204:207], v[168:171]\n"
    "ds_read_b64_tr_b16 v[200:201], v222 offset:57344\n"
    "v_cndmask_b32 v251, v238, v237, s[84:85]\n"
    "v_mfma_f32_16x16x32_bf16 v[172:175], v[112:115], v[204:207], v[172:175]\n"
    "ds_read_b64_tr_b16 v[202:203], v223 offset:57344\n"
    "s_waitcnt vmcnt(10)\n"
    "s_barrier\n"
    "s_waitcnt lgkmcnt(9)\n"
    "v_mfma_f32_16x16x32_bf16 v[160:163], v[52:55], v[208:211], v[160:163]\n"
    "s_add_u32 m0, s73, 0x8000\n"
    "s_add_u32 s89, s75, 0x150000\n"
    "buffer_load_dwordx4 v228, s[44:47], s89 offen lds\n"
    "ds_read_b64_tr_b16 v[204:205], v220 offset:61440\n"
    "s_lshl_b32 s87, s69, 11\n"
    "s_lshl_b32 s88, s70, 10\n"
    "s_add_u32 s87, s87, s88\n"
    "v_lshlrev_b32 v237, 3, v235\n"
    "v_add_u32 v237, s87, v237\n"
    "v_mfma_f32_16x16x32_bf16 v[164:167], v[116:119], v[208:211], v[164:167]\n"
    "ds_read_b64_tr_b16 v[206:207], v221 offset:61440\n"
    "ds_read_b64 v[240:241], v237 offset:16384\n"
    "ds_read_b64 v[242:243], v237 offset:16896\n"
    "s_waitcnt lgkmcnt(11)\n"
    "v_mfma_f32_16x16x32_bf16 v[168:171], v[52:55], v[252:255], v[168:171]\n"
    "ds_read_b64_tr_b16 v[208:209], v222 offset:61440\n"
    "v_mfma_f32_16x16x16_bf16 v[144:147], v[132:133], v[150:151], 0\n"
    "v_mfma_f32_16x16x16_bf16 v[184:187], v[134:135], v[150:151], 0\n"
    "v_mfma_f32_16x16x16_bf16 v[152:155], v[140:141], v[158:159], 0\n"
    "v_mfma_f32_16x16x16_bf16 v[188:191], v[142:143], v[158:159], 0\n"
    "s_waitcnt lgkmcnt(2)\n"
    "v_mfma_f32_16x16x16_bf16 v[128:131], v[240:241], v[148:149], 0\n"
    "v_mfma_f32_16x16x16_bf16 v[176:179], v[132:133], v[148:149], 0\n"
    "s_waitcnt lgkmcnt(1)\n"
    "v_mfma_f32_16x16x16_bf16 v[136:139], v[242:243], v[156:157], 0\n"
    "v_mfma_f32_16x16x16_bf16 v[180:183], v[140:141], v[156:157], 0\n"
    "v_cndmask_b32 v144, v144, v145, s[80:81]\n"
    "v_cndmask_b32 v146, v146, v147, s[80:81]\n"
    "v_mfma_f32_16x16x32_bf16 v[172:175], v[116:119], v[252:255], v[172:175]\n"
    "s_add_u32 m0, s73, 0xa000\n"
    "s_nop 0\n"
    "buffer_load_dwordx4 v228, s[48:51], s89 offen lds\n"
    "ds_read_b64_tr_b16 v[210:211], v223 offset:61440\n"
    "v_cndmask_b32 v184, v184, v185, s[80:81]\n"
    "v_mfma_f32_16x16x32_bf16 v[216:219], v[212:215], v[192:195], v[216:219]\n"
    "ds_read_b64_tr_b16 v[252:253], v224 offset:57344\n"
    "ds_read_b64_tr_b16 v[254:255], v225 offset:57344\n"
    "ds_read_b128 v[212:215], v227 offset:14336\n"
    "v_cndmask_b32 v186, v186, v187, s[80:81]\n"
    "v_cndmask_b32 v148, v144, v184, s[82:83]\n"
    "v_mfma_f32_16x16x32_bf16 v[160:163], v[56:59], v[196:199], v[160:163]\n"
    "v_xor_b32 v220, 0x10000, v220\n"
    "v_xor_b32 v221, 0x10000, v221\n"
    "v_xor_b32 v222, 0x10000, v222\n"
    "v_xor_b32 v223, 0x10000, v223\n"
    "ds_read_b64_tr_b16 v[192:193], v220 offset:0\n"
    "v_mfma_f32_16x16x32_bf16 v[164:167], v[120:123], v[196:199], v[164:167]\n"
    "ds_read_b64_tr_b16 v[194:195], v221 offset:0\n"
    "v_cndmask_b32 v149, v146, v186, s[84:85]\n"
    "v_cndmask_b32 v152, v152, v153, s[80:81]\n"
    "v_mfma_f32_16x16x32_bf16 v[168:171], v[56:59], v[200:203], v[168:171]\n"
    "ds_read_b64_tr_b16 v[196:197], v222 offset:0\n"
    "v_cndmask_b32 v154, v154, v155, s[80:81]\n"
    "v_cndmask_b32 v188, v188, v189, s[80:81]\n"
    "v_mfma_f32_16x16x32_bf16 v[172:175], v[120:123], v[200:203], v[172:175]\n"
    "ds_read_b64_tr_b16 v[198:199], v223 offset:0\n"
    "v_cndmask_b32 v190, v190, v191, s[80:81]\n"
    "v_cndmask_b32 v156, v152, v188, s[82:83]\n"
    "v_mfma_f32_16x16x32_bf16 v[160:163], v[60:63], v[204:207], v[160:163]\n"
    "ds_read_b64_tr_b16 v[200:201], v220 offset:8192\n"
    "v_cndmask_b32 v157, v154, v190, s[84:85]\n"
    "v_cndmask_b32 v128, v128, v129, s[80:81]\n"
    "v_mfma_f32_16x16x32_bf16 v[164:167], v[124:127], v[204:207], v[164:167]\n"
    "ds_read_b64_tr_b16 v[202:203], v221 offset:8192\n"
    "v_cndmask_b32 v130, v130, v131, s[80:81]\n"
    "v_cndmask_b32 v176, v176, v177, s[80:81]\n"
    "s_waitcnt lgkmcnt(9)\n"
    "v_mfma_f32_16x16x32_bf16 v[168:171], v[60:63], v[208:211], v[168:171]\n"
    "ds_read_b64_tr_b16 v[204:205], v222 offset:8192\n"
    "v_cndmask_b32 v178, v178, v179, s[80:81]\n"
    "v_cndmask_b32 v132, v128, v176, s[82:83]\n"
    "v_mfma_f32_16x16x32_bf16 v[172:175], v[124:127], v[208:211], v[172:175]\n"
    "ds_read_b64_tr_b16 v[206:207], v223 offset:8192\n"
    "v_cndmask_b32 v133, v130, v178, s[84:85]\n"
    "v_cndmask_b32 v136, v136, v137, s[80:81]\n"
    "s_waitcnt lgkmcnt(8)\n"
    "v_mfma_f32_16x16x32_bf16 v[216:219], v[212:215], v[252:255], v[216:219]\n"
    "ds_read_b64_tr_b16 v[208:209], v220 offset:4096\n"
    "ds_read_b64_tr_b16 v[210:211], v221 offset:4096\n"
    "v_mov_b32 v212, 0x3f803f80\n"
    "v_mov_b32 v213, 0x3f803f80\n"
    "v_mov_b32 v214, 0x3f803f80\n"
    "v_mov_b32 v215, 0x3f803f80\n"
    "v_cndmask_b32 v138, v138, v139, s[80:81]\n"
    "v_cndmask_b32 v180, v180, v181, s[80:81]\n"
    "v_cndmask_b32 v182, v182, v183, s[80:81]\n"
    "v_cndmask_b32 v140, v136, v180, s[82:83]\n"
    "v_cndmask_b32 v141, v138, v182, s[84:85]\n"
    "s_cmp_eq_u32 s91, 1\n"
    "s_cbranch_scc0 LA_first_7\n"
    "v_mov_b32 v237, 0xf149f2ca\n"
    "v_cndmask_b32 v132, v237, v132, s[82:83]\n"
    "v_cndmask_b32 v133, v237, v133, s[84:85]\n"
    "v_cndmask_b32 v140, v237, v140, s[82:83]\n"
    "v_cndmask_b32 v141, v237, v141, s[84:85]\n"
    "LA_first_7:\n"
    "s_waitcnt lgkmcnt(8)\n"
    "v_mfma_f32_16x16x32_bf16 v[128:131], v[192:195], v[0:3], 0\n"
    "ds_read_b64_tr_b16 v[252:253], v222 offset:4096\n"
    "s_lshl_b32 s87, s69, 7\n"
    "s_lshl_b32 s88, s70, 6\n"
    "s_add_u32 s87, s87, s88\n"
    "v_lshlrev_b32 v226, 2, v235\n"
    "v_add_u32 v226, s87, v226\n"
    "v_mfma_f32_16x16x32_bf16 v[176:179], v[192:195], v[64:67], 0\n"
    "ds_read_b64_tr_b16 v[254:255], v223 offset:4096\n"
    "s_mov_b64 exec, 0xffff\n"
    "ds_write_b32 v226, v216 offset:26112\n"
    "s_mov_b64 exec, -1\n"
    "v_max_f32 v237, v132, v133\n"
    "v_mul_f32 v239, 0x3eb8aa3b, v233\n"
    "s_waitcnt lgkmcnt(9)\n"
    "s_nop 0\n"
    "v_mfma_f32_16x16x32_bf16 v[136:139], v[196:199], v[0:3], 0\n"
    "ds_read_b64_tr_b16 v[192:193], v220 offset:12288\n"
    "v_mov_b32 v238, v237\n"
    "s_nop 1\n"
    "v_permlane16_swap_b32 v237, v238\n"
    "v_mfma_f32_16x16x32_bf16 v[180:183], v[196:199], v[64:67], 0\n"
    "ds_read_b64_tr_b16 v[194:195], v221 offset:12288\n"
    "v_max_f32 v237, v237, v238\n"
    "v_mov_b32 v238, v237\n"
    "s_waitcnt lgkmcnt(9)\n"
    "v_mfma_f32_16x16x32_bf16 v[144:147], v[200:203], v[0:3], 0\n"
    "ds_read_b64_tr_b16 v[196:197], v222 offset:12288\n"
    "v_permlane32_swap_b32 v237, v238\n"
    "s_nop 0\n"
    "v_max_f32 v237, v237, v238\n"
    "v_mfma_f32_16x16x32_bf16 v[184:187], v[200:203], v[64:67], 0\n"
    "ds_read_b64_tr_b16 v[198:199], v223 offset:12288\n"
    "v_mul_f32 v237, -v237, v239\n"
    "v_fma_f32 v132, v132, v239, v237\n"
    "s_waitcnt lgkmcnt(9)\n"
    "v_mfma_f32_16x16x32_bf16 v[152:155], v[204:207], v[0:3], 0\n"
    "s_lshl_b32 s86, s70, 12\n"
    "v_add_u32 v224, s86, v224\n"
    "v_add_u32 v225, s86, v225\n"
    "v_xor_b32 v224, 0x10000, v224\n"
    "v_xor_b32 v225, 0x10000, v225\n"
    "ds_read_b64_tr_b16 v[200:201], v224 offset:0\n"
    "v_mfma_f32_16x16x32_bf16 v[188:191], v[204:207], v[64:67], 0\n"
    "ds_read_b64_tr_b16 v[202:203], v225 offset:0\n"
    "v_fma_f32 v133, v133, v239, v237\n"
    "s_waitcnt vmcnt(10) lgkmcnt(6)\n"
    "s_barrier\n"
    "v_mfma_f32_16x16x32_bf16 v[128:131], v[208:211], v[4:7], v[128:131]\n"
    "s_add_u32 m0, s73, 0xc000\n"
    "s_add_u32 s89, s75, 0x188000\n"
    "buffer_load_dwordx4 v228, s[44:47], s89 offen lds\n"
    "ds_read_b64_tr_b16 v[204:205], v224 offset:4096\n"
    "s_lshl_b32 s87, s69, 11\n"
    "s_lshl_b32 s88, s70, 10\n"
    "s_add_u32 s87, s87, s88\n"
    "v_lshlrev_b32 v227, 4, v235\n"
    "v_add_u32 v227, s87, v227\n"
    "v_mfma_f32_16x16x32_bf16 v[176:179], v[208:211], v[68:71], v[176:179]\n"
    "ds_read_b64_tr_b16 v[206:207], v225 offset:4096\n"
    "ds_write_b128 v227, v[216:219] offset:0\n"
    "v_exp_f32 v132, v132\n"
    "v_mfma_f32_16x16x32_bf16 v[136:139], v[252:255], v[4:7], v[136:139]\n"
    "ds_read_b64_tr_b16 v[208:209], v220 offset:16384\n"
    "v_exp_f32 v133, v133\n"
    "v_mfma_f32_16x16x32_bf16 v[180:183], v[252:255], v[68:71], v[180:183]\n"
    "s_add_u32 m0, s73, 0xe000\n"
    "s_nop 0\n"
    "buffer_load_dwordx4 v228, s[48:51], s89 offen lds\n"
    "ds_read_b64_tr_b16 v[210:211], v221 offset:16384\n"
    "v_add_f32 v244, v132, v133\n"
    "s_waitcnt lgkmcnt(9)\n"
    "v_mfma_f32_16x16x32_bf16 v[144:147], v[192:195], v[4:7], v[144:147]\n"
    "ds_read_b64_tr_b16 v[252:253], v222 offset:16384\n"
    "v_cvt_pk_bf16_f32 v237, v132, v133\n"
    "v_mov_b32 v238, v244\n"
    "v_mfma_f32_16x16x32_bf16 v[184:187], v[192:195], v[68:71], v[184:187]\n"
    "ds_read_b64_tr_b16 v[254:255], v223 offset:16384\n"
    "v_permlane16_swap_b32 v244, v238\n"
    "s_nop 0\n"
    "v_add_f32 v244, v244, v238\n"
    "s_waitcnt lgkmcnt(9)\n"
    "v_mfma_f32_16x16x32_bf16 v[152:155], v[196:199], v[4:7], v[152:155]\n"
    "ds_read_b64_tr_b16 v[192:193], v220 offset:24576\n"
    "v_mov_b32 v238, v244\n"
    "s_nop 1\n"
    "v_permlane32_swap_b32 v244, v238\n"
    "v_mfma_f32_16x16x32_bf16 v[188:191], v[196:199], v[68:71], v[188:191]\n"
    "ds_read_b64_tr_b16 v[194:195], v221 offset:24576\n"
    "v_add_f32 v244, v244, v238\n"
    "s_waitcnt lgkmcnt(9)\n"
    "v_mfma_f32_16x16x32_bf16 v[216:219], v[200:203], v[212:215], 0\n"
    "ds_read_b64_tr_b16 v[196:197], v222 offset:24576\n"
    "ds_read_b64_tr_b16 v[198:199], v223 offset:24576\n"
    "v_rcp_f32 v244, v244\n"
    "s_waitcnt lgkmcnt(9)\n"
    "v_mfma_f32_16x16x32_bf16 v[216:219], v[204:207], v[212:215], v[216:219]\n"
    "ds_read_b64_tr_b16 v[200:201], v224 offset:16384\n"
    "ds_read_b64_tr_b16 v[202:203], v225 offset:16384\n"
    "v_perm_b32 v132, v237, v237, v248\n"
    "v_perm_b32 v133, v237, v237, v249\n"
    "s_waitcnt lgkmcnt(8)\n"
    "v_mfma_f32_16x16x32_bf16 v[128:131], v[208:211], v[8:11], v[128:131]\n"
    "ds_read_b64_tr_b16 v[204:205], v220 offset:20480\n"
    "v_perm_b32 v134, v237, v237, v250\n"
    "v_perm_b32 v135, v237, v237, v251\n"
    "v_mfma_f32_16x16x32_bf16 v[176:179], v[208:211], v[72:75], v[176:179]\n"
    "ds_read_b64_tr_b16 v[206:207], v221 offset:20480\n"
    "v_max_f32 v237, v140, v141\n"
    "v_mul_f32 v239, 0x3eb8aa3b, v233\n"
    "s_waitcnt lgkmcnt(8)\n"
    "v_mfma_f32_16x16x32_bf16 v[136:139], v[252:255], v[8:11], v[136:139]\n"
    "ds_read_b64_tr_b16 v[208:209], v222 offset:20480\n"
    "v_mov_b32 v238, v237\n"
    "s_nop 1\n"
    "v_permlane16_swap_b32 v237, v238\n"
    "v_mfma_f32_16x16x32_bf16 v[180:183], v[252:255], v[72:75], v[180:183]\n"
    "ds_read_b64_tr_b16 v[210:211], v223 offset:20480\n"
    "v_max_f32 v237, v237, v238\n"
    "v_mov_b32 v238, v237\n"
    "s_waitcnt lgkmcnt(8)\n"
    "v_mfma_f32_16x16x32_bf16 v[144:147], v[192:195], v[8:11], v[144:147]\n"
    "ds_read_b64_tr_b16 v[252:253], v220 offset:28672\n"
    "v_permlane32_swap_b32 v237, v238\n"
    "s_nop 0\n"
    "v_max_f32 v237, v237, v238\n"
    "v_mfma_f32_16x16x32_bf16 v[184:187], v[192:195], v[72:75], v[184:187]\n"
    "ds_read_b64_tr_b16 v[254:255], v221 offset:28672\n"
    "v_mul_f32 v237, -v237, v239\n"
    "v_fma_f32 v140, v140, v239, v237\n"
    "s_waitcnt lgkmcnt(8)\n"
    "v_mfma_f32_16x16x32_bf16 v[152:155], v[196:199], v[8:11], v[152:155]\n"
    "ds_read_b64_tr_b16 v[192:193], v222 offset:28672\n"
    "v_fma_f32 v141, v141, v239, v237\n"
    "v_mfma_f32_16x16x32_bf16 v[188:191], v[196:199], v[72:75], v[188:191]\n"
    "ds_read_b64_tr_b16 v[194:195], v223 offset:28672\n"
    "v_exp_f32 v140, v140\n"
    "s_waitcnt lgkmcnt(8)\n"
    "v_mfma_f32_16x16x32_bf16 v[216:219], v[200:203], v[212:215], v[216:219]\n"
    "ds_read_b64_tr_b16 v[196:197], v224 offset:20480\n"
    "ds_read_b64_tr_b16 v[198:199], v225 offset:20480\n"
    "v_exp_f32 v141, v141\n"
    "s_waitcnt vmcnt(10)\n"
    "s_barrier\n"
    "s_waitcnt lgkmcnt(8)\n"
    "v_mfma_f32_16x16x32_bf16 v[128:131], v[204:207], v[12:15], v[128:131]\n"
    "s_add_u32 m0, s93, 0x10000\n"
    "s_mul_i32 s89, s70, 0xa0000\n"
    "s_add_u32 s89, s89, 0x80000\n"
    "buffer_load_dwordx4 v229, s[60:63], s89 offen lds\n"
    "ds_read_b64_tr_b16 v[200:201], v220 offset:32768\n"
    "v_add_f32 v245, v140, v141\n"
    "v_mfma_f32_16x16x32_bf16 v[176:179], v[204:207], v[76:79], v[176:179]\n"
    "ds_read_b64_tr_b16 v[202:203], v221 offset:32768\n"
    "v_cvt_pk_bf16_f32 v237, v140, v141\n"
    "v_mov_b32 v238, v245\n"
    "s_waitcnt lgkmcnt(8)\n"
    "v_mfma_f32_16x16x32_bf16 v[136:139], v[208:211], v[12:15], v[136:139]\n"
    "ds_read_b64_tr_b16 v[204:205], v222 offset:32768\n"
    "v_permlane16_swap_b32 v245, v238\n"
    "s_nop 0\n"
    "v_add_f32 v245, v245, v238\n"
    "v_mfma_f32_16x16x32_bf16 v[180:183], v[208:211], v[76:79], v[180:183]\n"
    "s_add_u32 m0, s93, 0x11000\n"
    "s_mul_i32 s89, s70, 0xa0000\n"
    "s_add_u32 s89, s89, 0x88000\n"
    "buffer_load_dwordx4 v229, s[60:63], s89 offen lds\n"
    "ds_read_b64_tr_b16 v[206:207], v223 offset:32768\n"
    "v_mov_b32 v238, v245\n"
    "s_waitcnt lgkmcnt(8)\n"
    "v_mfma_f32_16x16x32_bf16 v[144:147], v[252:255], v[12:15], v[144:147]\n"
    "ds_read_b64_tr_b16 v[208:209], v220 offset:40960\n"
    "v_permlane32_swap_b32 v245, v238\n"
    "s_nop 0\n"
    "v_add_f32 v245, v245, v238\n"
    "v_mfma_f32_16x16x32_bf16 v[184:187], v[252:255], v[76:79], v[184:187]\n"
    "ds_read_b64_tr_b16 v[210:211], v221 offset:40960\n"
    "v_rcp_f32 v245, v245\n"
    "s_waitcnt lgkmcnt(8)\n"
    "v_mfma_f32_16x16x32_bf16 v[152:155], v[192:195], v[12:15], v[152:155]\n"
    "ds_read_b64_tr_b16 v[252:253], v222 offset:40960\n"
    "v_perm_b32 v140, v237, v237, v248\n"
    "v_perm_b32 v141, v237, v237, v249\n"
    "v_mfma_f32_16x16x32_bf16 v[188:191], v[192:195], v[76:79], v[188:191]\n"
    "ds_read_b64_tr_b16 v[254:255], v223 offset:40960\n"
    "v_perm_b32 v142, v237, v237, v250\n"
    "v_perm_b32 v143, v237, v237, v251\n"
    "s_waitcnt lgkmcnt(8)\n"
    "v_mfma_f32_16x16x32_bf16 v[216:219], v[196:199], v[212:215], v[216:219]\n"
    "ds_read_b64_tr_b16 v[192:193], v224 offset:32768\n"
    "ds_read_b64_tr_b16 v[194:195], v225 offset:32768\n"
    "v_and_b32 v237, 15, v235\n"
    "v_lshlrev_b32 v237, 2, v237\n"
    "s_lshl_b32 s87, s70, 8\n"
    "v_add_u32 v237, s87, v237\n"
    "s_waitcnt lgkmcnt(8)\n"
    "v_mfma_f32_16x16x32_bf16 v[128:131], v[200:203], v[16:19], v[128:131]\n"
    "ds_read_b64_tr_b16 v[196:197], v220 offset:36864\n"
    "ds_read_b32 v64, v237 offset:26112\n"
    "ds_read_b32 v66, v237 offset:26176\n"
    "ds_read_b32 v65, v237 offset:26240\n"
    "ds_read_b32 v67, v237 offset:26304\n"
    "v_lshrrev_b32 v238, 4, v235\n"
    "v_lshlrev_b32 v238, 4, v238\n"
    "s_lshl_b32 s87, s69, 7\n"
    "v_add_u32 v238, s87, v238\n"
    "v_mfma_f32_16x16x32_bf16 v[176:179], v[200:203], v[80:83], v[176:179]\n"
    "ds_read_b64_tr_b16 v[198:199], v221 offset:36864\n"
    "ds_read_b128 v[0:3], v238 offset:27712\n"
    "ds_read_b128 v[4:7], v238 offset:28288\n"
    "s_waitcnt lgkmcnt(14)\n"
    "v_mfma_f32_16x16x32_bf16 v[136:139], v[204:207], v[16:19], v[136:139]\n"
    "ds_read_b64_tr_b16 v[200:201], v222 offset:36864\n"
    "s_waitcnt lgkmcnt(6)\n"
    "v_add_f32 v64, v64, v66\n"
    "s_waitcnt lgkmcnt(4)\n"
    "v_add_f32 v65, v65, v67\n"
    "v_mfma_f32_16x16x32_bf16 v[180:183], v[204:207], v[80:83], v[180:183]\n"
    "ds_read_b64_tr_b16 v[202:203], v223 offset:36864\n"
    "s_waitcnt lgkmcnt(3)\n"
    "v_fma_f32 v164, -v0, v64, v164\n"
    "v_fma_f32 v165, -v1, v64, v165\n"
    "v_mfma_f32_16x16x32_bf16 v[144:147], v[208:211], v[16:19], v[144:147]\n"
    "ds_read_b64_tr_b16 v[204:205], v220 offset:45056\n"
    "v_fma_f32 v166, -v2, v64, v166\n"
    "v_fma_f32 v167, -v3, v64, v167\n"
    "v_mfma_f32_16x16x32_bf16 v[184:187], v[208:211], v[80:83], v[184:187]\n"
    "ds_read_b64_tr_b16 v[206:207], v221 offset:45056\n"
    "s_waitcnt lgkmcnt(4)\n"
    "v_mul_f32 v164, v164, v4\n"
    "v_mul_f32 v165, v165, v5\n"
    "v_mfma_f32_16x16x32_bf16 v[152:155], v[252:255], v[16:19], v[152:155]\n"
    "ds_read_b64_tr_b16 v[208:209], v222 offset:45056\n"
    "v_mul_f32 v166, v166, v6\n"
    "v_mul_f32 v167, v167, v7\n"
    "v_mfma_f32_16x16x32_bf16 v[188:191], v[252:255], v[80:83], v[188:191]\n"
    "ds_read_b64_tr_b16 v[210:211], v223 offset:45056\n"
    "v_cvt_pk_bf16_f32 v164, v164, v165\n"
    "v_cvt_pk_bf16_f32 v165, v166, v167\n"
    "v_mfma_f32_16x16x32_bf16 v[216:219], v[192:195], v[212:215], v[216:219]\n"
    "ds_read_b64_tr_b16 v[252:253], v224 offset:36864\n"
    "ds_read_b64_tr_b16 v[254:255], v225 offset:36864\n"
    "v_fma_f32 v172, -v0, v65, v172\n"
    "v_fma_f32 v173, -v1, v65, v173\n"
    "s_waitcnt vmcnt(10)\n"
    "s_barrier\n"
    "v_mfma_f32_16x16x32_bf16 v[128:131], v[196:199], v[20:23], v[128:131]\n"
    "s_add_u32 m0, s93, 0x14000\n"
    "s_mul_i32 s89, s70, 0xa0000\n"
    "s_add_u32 s89, s89, 0xb0000\n"
    "buffer_load_dwordx4 v229, s[60:63], s89 offen lds\n"
    "ds_read_b64_tr_b16 v[192:193], v220 offset:49152\n"
    "v_fma_f32 v174, -v2, v65, v174\n"
    "v_mfma_f32_16x16x32_bf16 v[176:179], v[196:199], v[84:87], v[176:179]\n"
    "ds_read_b64_tr_b16 v[194:195], v221 offset:49152\n"
    "v_fma_f32 v175, -v3, v65, v175\n"
    "v_mul_f32 v172, v172, v4\n"
    "s_waitcnt lgkmcnt(8)\n"
    "v_mfma_f32_16x16x32_bf16 v[136:139], v[200:203], v[20:23], v[136:139]\n"
    "ds_read_b64_tr_b16 v[196:197], v222 offset:49152\n"
    "v_mul_f32 v173, v173, v5\n"
    "v_mul_f32 v174, v174, v6\n"
    "v_mfma_f32_16x16x32_bf16 v[180:183], v[200:203], v[84:87], v[180:183]\n"
    "s_add_u32 m0, s93, 0x15000\n"
    "s_mul_i32 s89, s70, 0xa0000\n"
    "s_add_u32 s89, s89, 0xb8000\n"
    "buffer_load_dwordx4 v229, s[60:63], s89 offen lds\n"
    "ds_read_b64_tr_b16 v[198:199], v223 offset:49152\n"
    "v_mul_f32 v175, v175, v7\n"
    "s_waitcnt lgkmcnt(8)\n"
    "v_mfma_f32_16x16x32_bf16 v[144:147], v[204:207], v[20:23], v[144:147]\n"
    "ds_read_b64_tr_b16 v[200:201], v220 offset:57344\n"
    "v_cvt_pk_bf16_f32 v172, v172, v173\n"
    "v_cvt_pk_bf16_f32 v173, v174, v175\n"
    "v_mfma_f32_16x16x32_bf16 v[184:187], v[204:207], v[84:87], v[184:187]\n"
    "ds_read_b64_tr_b16 v[202:203], v221 offset:57344\n"
    "ds_read_b128 v[0:3], v238 offset:27648\n"
    "ds_read_b128 v[4:7], v238 offset:28224\n"
    "s_lshl_b32 s87, s69, 11\n"
    "s_lshl_b32 s88, s70, 10\n"
    "s_add_u32 s87, s87, s88\n"
    "v_lshlrev_b32 v239, 3, v235\n"
    "v_add_u32 v239, s87, v239\n"
    "s_cmp_lt_u32 s69, 3\n"
    "s_cbranch_scc0 LA_vx_8\n"
    "ds_write_b64 v239, v[164:165] offset:18432\n"
    "ds_write_b64 v239, v[172:173] offset:18944\n"
    "LA_vx_8:\n"
    "s_waitcnt lgkmcnt(10)\n"
    "v_mfma_f32_16x16x32_bf16 v[152:155], v[208:211], v[20:23], v[152:155]\n"
    "ds_read_b64_tr_b16 v[204:205], v222 offset:57344\n"
    "s_waitcnt lgkmcnt(2)\n"
    "v_fma_f32 v160, -v0, v64, v160\n"
    "v_fma_f32 v161, -v1, v64, v161\n"
    "v_mfma_f32_16x16x32_bf16 v[188:191], v[208:211], v[84:87], v[188:191]\n"
    "ds_read_b64_tr_b16 v[206:207], v223 offset:57344\n"
    "v_fma_f32 v162, -v2, v64, v162\n"
    "v_fma_f32 v163, -v3, v64, v163\n"
    "v_mfma_f32_16x16x32_bf16 v[216:219], v[252:255], v[212:215], v[216:219]\n"
    "ds_read_b64_tr_b16 v[208:209], v224 offset:49152\n"
    "ds_read_b64_tr_b16 v[210:211], v225 offset:49152\n"
    "s_waitcnt lgkmcnt(4)\n"
    "v_mul_f32 v160, v160, v4\n"
    "v_mul_f32 v161, v161, v5\n"
    "v_mfma_f32_16x16x32_bf16 v[128:131], v[192:195], v[24:27], v[128:131]\n"
    "ds_read_b64_tr_b16 v[252:253], v220 offset:53248\n"
    "v_mul_f32 v162, v162, v6\n"
    "v_mul_f32 v163, v163, v7\n"
    "v_mfma_f32_16x16x32_bf16 v[176:179], v[192:195], v[88:91], v[176:179]\n"
    "ds_read_b64_tr_b16 v[254:255], v221 offset:53248\n"
    "v_cvt_pk_bf16_f32 v163, v162, v163\n"
    "v_cvt_pk_bf16_f32 v162, v160, v161\n"
    "v_mfma_f32_16x16x32_bf16 v[136:139], v[196:199], v[24:27], v[136:139]\n"
    "ds_read_b64_tr_b16 v[192:193], v222 offset:53248\n"
    "v_fma_f32 v168, -v0, v65, v168\n"
    "v_fma_f32 v169, -v1, v65, v169\n"
    "v_mfma_f32_16x16x32_bf16 v[180:183], v[196:199], v[88:91], v[180:183]\n"
    "ds_read_b64_tr_b16 v[194:195], v223 offset:53248\n"
    "v_fma_f32 v170, -v2, v65, v170\n"
    "v_fma_f32 v171, -v3, v65, v171\n"
    "v_mfma_f32_16x16x32_bf16 v[144:147], v[200:203], v[24:27], v[144:147]\n"
    "ds_read_b64_tr_b16 v[196:197], v220 offset:61440\n"
    "v_mul_f32 v168, v168, v4\n"
    "v_mul_f32 v169, v169, v5\n"
    "v_mfma_f32_16x16x32_bf16 v[184:187], v[200:203], v[88:91], v[184:187]\n"
    "ds_read_b64_tr_b16 v[198:199], v221 offset:61440\n"
    "v_mul_f32 v170, v170, v6\n"
    "v_mul_f32 v171, v171, v7\n"
    "s_waitcnt lgkmcnt(8)\n"
    "v_mfma_f32_16x16x32_bf16 v[152:155], v[204:207], v[24:27], v[152:155]\n"
    "ds_read_b64_tr_b16 v[200:201], v222 offset:61440\n"
    "v_cvt_pk_bf16_f32 v171, v170, v171\n"
    "v_cvt_pk_bf16_f32 v170, v168, v169\n"
    "v_mfma_f32_16x16x32_bf16 v[188:191], v[204:207], v[88:91], v[188:191]\n"
    "ds_read_b64_tr_b16 v[202:203], v223 offset:61440\n"
    "v_max_f32 v237, v148, v149\n"
    "v_mul_f32 v239, 0x3eb8aa3b, v234\n"
    "s_waitcnt lgkmcnt(8)\n"
    "v_mfma_f32_16x16x32_bf16 v[216:219], v[208:211], v[212:215], v[216:219]\n"
    "ds_read_b64_tr_b16 v[204:205], v224 offset:53248\n"
    "ds_read_b64_tr_b16 v[206:207], v225 offset:53248\n"
    "v_mov_b32 v238, v237\n"
    "s_nop 1\n"
    "v_permlane16_swap_b32 v237, v238\n"
    "s_waitcnt vmcnt(10)\n"
    "s_barrier\n"
    "s_waitcnt lgkmcnt(8)\n"
    "v_mfma_f32_16x16x32_bf16 v[128:131], v[252:255], v[28:31], v[128:131]\n"
    "s_add_u32 m0, s93, 0x18000\n"
    "s_mul_i32 s89, s70, 0xa0000\n"
    "s_add_u32 s89, s89, 0xd0000\n"
    "buffer_load_dwordx4 v229, s[60:63], s89 offen lds\n"
    "v_xor_b32 v220, 0x10000, v220\n"
    "v_xor_b32 v221, 0x10000, v221\n"
    "v_xor_b32 v222, 0x10000, v222\n"
    "v_xor_b32 v223, 0x10000, v223\n"
    "ds_read_b64_tr_b16 v[208:209], v220 offset:0\n"
    "v_mfma_f32_16x16x32_bf16 v[176:179], v[252:255], v[92:95], v[176:179]\n"
    "ds_read_b64_tr_b16 v[210:211], v221 offset:0\n"
    "v_max_f32 v237, v237, v238\n"
    "v_mov_b32 v238, v237\n"
    "s_waitcnt lgkmcnt(8)\n"
    "v_mfma_f32_16x16x32_bf16 v[136:139], v[192:195], v[28:31], v[136:139]\n"
    "ds_read_b64_tr_b16 v[252:253], v222 offset:0\n"
    "v_permlane32_swap_b32 v237, v238\n"
    "s_nop 0\n"
    "v_max_f32 v237, v237, v238\n"
    "v_mfma_f32_16x16x32_bf16 v[180:183], v[192:195], v[92:95], v[180:183]\n"
    "s_add_u32 m0, s93, 0x19000\n"
    "s_mul_i32 s89, s70, 0xa0000\n"
    "s_add_u32 s89, s89, 0xd8000\n"
    "buffer_load_dwordx4 v229, s[60:63], s89 offen lds\n"
    "ds_read_b64_tr_b16 v[254:255], v223 offset:0\n"
    "v_mul_f32 v237, -v237, v239\n"
    "s_waitcnt lgkmcnt(8)\n"
    "v_mfma_f32_16x16x32_bf16 v[144:147], v[196:199], v[28:31], v[144:147]\n"
    "ds_read_b64_tr_b16 v[192:193], v220 offset:8192\n"
    "v_fma_f32 v148, v148, v239, v237\n"
    "v_fma_f32 v149, v149, v239, v237\n"
    "v_mfma_f32_16x16x32_bf16 v[184:187], v[196:199], v[92:95], v[184:187]\n"
    "ds_read_b64_tr_b16 v[194:195], v221 offset:8192\n"
    "v_exp_f32 v148, v148\n"
    "s_waitcnt lgkmcnt(8)\n"
    "v_mfma_f32_16x16x32_bf16 v[152:155], v[200:203], v[28:31], v[152:155]\n"
    "ds_read_b64_tr_b16 v[196:197], v222 offset:8192\n"
    "v_exp_f32 v149, v149\n"
    "v_mfma_f32_16x16x32_bf16 v[188:191], v[200:203], v[92:95], v[188:191]\n"
    "ds_read_b64_tr_b16 v[198:199], v223 offset:8192\n"
    "v_add_f32 v246, v148, v149\n"
    "v_cvt_pk_bf16_f32 v237, v148, v149\n"
    "s_waitcnt lgkmcnt(8)\n"
    "v_mfma_f32_16x16x32_bf16 v[216:219], v[204:207], v[212:215], v[216:219]\n"
    "v_xor_b32 v224, 0x10000, v224\n"
    "v_xor_b32 v225, 0x10000, v225\n"
    "ds_read_b64_tr_b16 v[200:201], v224 offset:0\n"
    "ds_read_b64_tr_b16 v[202:203], v225 offset:0\n"
    "s_waitcnt lgkmcnt(8)\n"
    "v_mfma_f32_16x16x32_bf16 v[128:131], v[208:211], v[32:35], v[128:131]\n"
    "ds_read_b64_tr_b16 v[204:205], v220 offset:4096\n"
    "v_mov_b32 v238, v246\n"
    "s_nop 1\n"
    "v_permlane16_swap_b32 v246, v238\n"
    "v_mfma_f32_16x16x32_bf16 v[176:179], v[208:211], v[96:99], v[176:179]\n"
    "ds_read_b64_tr_b16 v[206:207], v221 offset:4096\n"
    "v_add_f32 v246, v246, v238\n"
    "v_mov_b32 v238, v246\n"
    "s_waitcnt lgkmcnt(8)\n"
    "v_mfma_f32_16x16x32_bf16 v[136:139], v[252:255], v[32:35], v[136:139]\n"
    "ds_read_b64_tr_b16 v[208:209], v222 offset:4096\n"
    "v_permlane32_swap_b32 v246, v238\n"
    "s_nop 0\n"
    "v_add_f32 v246, v246, v238\n"
    "v_mfma_f32_16x16x32_bf16 v[180:183], v[252:255], v[96:99], v[180:183]\n"
    "ds_read_b64_tr_b16 v[210:211], v223 offset:4096\n"
    "v_rcp_f32 v246, v246\n"
    "s_waitcnt lgkmcnt(8)\n"
    "v_mfma_f32_16x16x32_bf16 v[144:147], v[192:195], v[32:35], v[144:147]\n"
    "ds_read_b64_tr_b16 v[252:253], v220 offset:12288\n"
    "v_perm_b32 v148, v237, v237, v248\n"
    "v_perm_b32 v149, v237, v237, v249\n"
    "v_mfma_f32_16x16x32_bf16 v[184:187], v[192:195], v[96:99], v[184:187]\n"
    "ds_read_b64_tr_b16 v[254:255], v221 offset:12288\n"
    "v_perm_b32 v150, v237, v237, v250\n"
    "v_perm_b32 v151, v237, v237, v251\n"
    "s_waitcnt lgkmcnt(8)\n"
    "v_mfma_f32_16x16x32_bf16 v[152:155], v[196:199], v[32:35], v[152:155]\n"
    "ds_read_b64_tr_b16 v[192:193], v222 offset:12288\n"
    "v_max_f32 v237, v156, v157\n"
    "v_mul_f32 v239, 0x3eb8aa3b, v234\n"
    "v_mfma_f32_16x16x32_bf16 v[188:191], v[196:199], v[96:99], v[188:191]\n"
    "ds_read_b64_tr_b16 v[194:195], v223 offset:12288\n"
    "v_mov_b32 v238, v237\n"
    "s_nop 1\n"
    "v_permlane16_swap_b32 v237, v238\n"
    "s_waitcnt lgkmcnt(8)\n"
    "v_mfma_f32_16x16x32_bf16 v[216:219], v[200:203], v[212:215], v[216:219]\n"
    "ds_read_b64_tr_b16 v[196:197], v224 offset:4096\n"
    "ds_read_b64_tr_b16 v[198:199], v225 offset:4096\n"
    "v_max_f32 v237, v237, v238\n"
    "v_mov_b32 v238, v237\n"
    "s_waitcnt vmcnt(10)\n"
    "s_barrier\n"
    "s_waitcnt lgkmcnt(8)\n"
    "v_mfma_f32_16x16x32_bf16 v[128:131], v[204:207], v[36:39], v[128:131]\n"
    "s_add_u32 m0, s93, 0x1c000\n"
    "s_mul_i32 s89, s70, 0xa0000\n"
    "s_add_u32 s89, s89, 0x100000\n"
    "buffer_load_dwordx4 v229, s[60:63], s89 offen lds\n"
    "ds_read_b64_tr_b16 v[200:201], v220 offset:16384\n"
    "v_permlane32_swap_b32 v237, v238\n"
    "v_mfma_f32_16x16x32_bf16 v[176:179], v[204:207], v[100:103], v[176:179]\n"
    "ds_read_b64_tr_b16 v[202:203], v221 offset:16384\n"
    "v_max_f32 v237, v237, v238\n"
    "v_mul_f32 v237, -v237, v239\n"
    "s_waitcnt lgkmcnt(8)\n"
    "v_mfma_f32_16x16x32_bf16 v[136:139], v[208:211], v[36:39], v[136:139]\n"
    "ds_read_b64_tr_b16 v[204:205], v222 offset:16384\n"
    "v_fma_f32 v156, v156, v239, v237\n"
    "v_fma_f32 v157, v157, v239, v237\n"
    "v_mfma_f32_16x16x32_bf16 v[180:183], v[208:211], v[100:103], v[180:183]\n"
    "s_add_u32 m0, s93, 0x1d000\n"
    "s_mul_i32 s89, s70, 0xa0000\n"
    "s_add_u32 s89, s89, 0x108000\n"
    "buffer_load_dwordx4 v229, s[60:63], s89 offen lds\n"
    "ds_read_b64_tr_b16 v[206:207], v223 offset:16384\n"
    "s_waitcnt lgkmcnt(8)\n"
    "v_mfma_f32_16x16x32_bf16 v[144:147], v[252:255], v[36:39], v[144:147]\n"
    "ds_read_b64_tr_b16 v[208:209], v220 offset:24576\n"
    "v_exp_f32 v156, v156\n"
    "v_mfma_f32_16x16x32_bf16 v[184:187], v[252:255], v[100:103], v[184:187]\n"
    "ds_read_b64_tr_b16 v[210:211], v221 offset:24576\n"
    "v_exp_f32 v157, v157\n"
    "s_waitcnt lgkmcnt(8)\n"
    "v_mfma_f32_16x16x32_bf16 v[152:155], v[192:195], v[36:39], v[152:155]\n"
    "ds_read_b64_tr_b16 v[252:253], v222 offset:24576\n"
    "v_add_f32 v247, v156, v157\n"
    "v_cvt_pk_bf16_f32 v237, v156, v157\n"
    "v_mfma_f32_16x16x32_bf16 v[188:191], v[192:195], v[100:103], v[188:191]\n"
    "ds_read_b64_tr_b16 v[254:255], v223 offset:24576\n"
    "v_mov_b32 v238, v247\n"
    "s_nop 1\n"
    "v_permlane16_swap_b32 v247, v238\n"
    "s_waitcnt lgkmcnt(8)\n"
    "v_mfma_f32_16x16x32_bf16 v[216:219], v[196:199], v[212:215], v[216:219]\n"
    "ds_read_b64_tr_b16 v[192:193], v224 offset:16384\n"
    "ds_read_b64_tr_b16 v[194:195], v225 offset:16384\n"
    "v_add_f32 v247, v247, v238\n"
    "v_mov_b32 v238, v247\n"
    "s_waitcnt lgkmcnt(8)\n"
    "v_mfma_f32_16x16x32_bf16 v[128:131], v[200:203], v[40:43], v[128:131]\n"
    "ds_read_b64_tr_b16 v[196:197], v220 offset:20480\n"
    "v_permlane32_swap_b32 v247, v238\n"
    "s_nop 0\n"
    "v_add_f32 v247, v247, v238\n"
    "v_mfma_f32_16x16x32_bf16 v[176:179], v[200:203], v[104:107], v[176:179]\n"
    "ds_read_b64_tr_b16 v[198:199], v221 offset:20480\n"
    "v_rcp_f32 v247, v247\n"
    "s_waitcnt lgkmcnt(8)\n"
    "v_mfma_f32_16x16x32_bf16 v[136:139], v[204:207], v[40:43], v[136:139]\n"
    "ds_read_b64_tr_b16 v[200:201], v222 offset:20480\n"
    "v_perm_b32 v156, v237, v237, v248\n"
    "v_perm_b32 v157, v237, v237, v249\n"
    "v_mfma_f32_16x16x32_bf16 v[180:183], v[204:207], v[104:107], v[180:183]\n"
    "ds_read_b64_tr_b16 v[202:203], v223 offset:20480\n"
    "v_perm_b32 v158, v237, v237, v250\n"
    "v_perm_b32 v159, v237, v237, v251\n"
    "s_waitcnt lgkmcnt(8)\n"
    "v_mfma_f32_16x16x32_bf16 v[144:147], v[208:211], v[40:43], v[144:147]\n"
    "ds_read_b64_tr_b16 v[204:205], v220 offset:28672\n"
    "s_cmp_eq_u32 s69, 0\n"
    "s_cbranch_scc0 LA_vh_9\n"
    "v_lshrrev_b32 v238, 4, v235\n"
    "v_lshlrev_b32 v238, 4, v238\n"
    "ds_read_b128 v[0:3], v238 offset:28160\n"
    "ds_read_b128 v[4:7], v238 offset:28736\n"
    "s_lshl_b32 s87, s70, 12\n"
    "v_lshlrev_b32 v239, 4, v235\n"
    "v_add_u32 v239, s87, v239\n"
    "ds_read_b128 v[8:11], v239 offset:0\n"
    "ds_read_b128 v[12:15], v239 offset:1024\n"
    "s_waitcnt lgkmcnt(0)\n"
    "v_add_f32 v8, v8, v12\n"
    "v_add_f32 v9, v9, v13\n"
    "v_add_f32 v10, v10, v14\n"
    "v_add_f32 v11, v11, v15\n"
    "v_fma_f32 v8, -v0, v64, v8\n"
    "v_fma_f32 v9, -v1, v64, v9\n"
    "v_fma_f32 v10, -v2, v64, v10\n"
    "v_fma_f32 v11, -v3, v64, v11\n"
    "v_mul_f32 v8, v8, v4\n"
    "v_mul_f32 v9, v9, v5\n"
    "v_mul_f32 v10, v10, v6\n"
    "v_mul_f32 v11, v11, v7\n"
    "v_cvt_pk_bf16_f32 v160, v8, v9\n"
    "v_cvt_pk_bf16_f32 v161, v10, v11\n"
    "ds_read_b128 v[8:11], v239 offset:2048\n"
    "ds_read_b128 v[12:15], v239 offset:3072\n"
    "s_waitcnt lgkmcnt(0)\n"
    "v_add_f32 v8, v8, v12\n"
    "v_add_f32 v9, v9, v13\n"
    "v_add_f32 v10, v10, v14\n"
    "v_add_f32 v11, v11, v15\n"
    "v_fma_f32 v8, -v0, v65, v8\n"
    "v_fma_f32 v9, -v1, v65, v9\n"
    "v_fma_f32 v10, -v2, v65, v10\n"
    "v_fma_f32 v11, -v3, v65, v11\n"
    "v_mul_f32 v8, v8, v4\n"
    "v_mul_f32 v9, v9, v5\n"
    "v_mul_f32 v10, v10, v6\n"
    "v_mul_f32 v11, v11, v7\n"
    "v_cvt_pk_bf16_f32 v168, v8, v9\n"
    "v_cvt_pk_bf16_f32 v169, v10, v11\n"
    "s_branch LA_vhe_10\n"
    "LA_vh_9:\n"
    "s_lshl_b32 s87, s69, 11\n"
    "s_lshl_b32 s88, s70, 10\n"
    "s_add_u32 s87, s87, s88\n"
    "v_lshlrev_b32 v239, 3, v235\n"
    "v_add_u32 v239, s87, v239\n"
    "ds_read_b64 v[160:161], v239 offset:16384\n"
    "ds_read_b64 v[168:169], v239 offset:16896\n"
    "LA_vhe_10:\n"
    "v_mfma_f32_16x16x32_bf16 v[184:187], v[208:211], v[104:107], v[184:187]\n"
    "ds_read_b64_tr_b16 v[206:207], v221 offset:28672\n"
    "s_waitcnt lgkmcnt(1)\n"
    "v_mfma_f32_16x16x32_bf16 v[16:19], v[160:163], v[132:135], 0\n"
    "s_waitcnt lgkmcnt(1)\n"
    "v_mfma_f32_16x16x32_bf16 v[20:23], v[168:171], v[140:143], 0\n"
    "s_nop 5\n"
    "v_mul_f32 v16, v16, v244\n"
    "v_mul_f32 v17, v17, v244\n"
    "v_mfma_f32_16x16x32_bf16 v[152:155], v[252:255], v[40:43], v[152:155]\n"
    "ds_read_b64_tr_b16 v[208:209], v222 offset:28672\n"
    "v_mul_f32 v18, v18, v244\n"
    "v_mul_f32 v19, v19, v244\n"
    "v_mfma_f32_16x16x32_bf16 v[188:191], v[252:255], v[104:107], v[188:191]\n"
    "ds_read_b64_tr_b16 v[210:211], v223 offset:28672\n"
    "v_mul_f32 v20, v20, v245\n"
    "v_mul_f32 v21, v21, v245\n"
    "v_mfma_f32_16x16x32_bf16 v[216:219], v[192:195], v[212:215], v[216:219]\n"
    "ds_read_b64_tr_b16 v[252:253], v224 offset:20480\n"
    "ds_read_b64_tr_b16 v[254:255], v225 offset:20480\n"
    "v_mul_f32 v22, v22, v245\n"
    "v_mul_f32 v23, v23, v245\n"
    "s_waitcnt vmcnt(10)\n"
    "s_barrier\n"
    "v_mfma_f32_16x16x32_bf16 v[128:131], v[196:199], v[44:47], v[128:131]\n"
    "s_add_u32 m0, s93, 0\n"
    "s_mul_i32 s89, s70, 0xa0000\n"
    "s_add_u32 s89, s89, 0xa0000\n"
    "buffer_load_dwordx4 v229, s[60:63], s89 offen lds\n"
    "ds_read_b64_tr_b16 v[192:193], v220 offset:32768\n"
    "s_cmp_eq_u32 s68, 0\n"
    "s_cbranch_scc0 LA_skip_12\n"
    "v_and_b32 v26, 15, v235\n"
    "v_lshlrev_b32 v26, 3, v26\n"
    "s_add_u32 s88, s90, 0x80\n"
    "buffer_load_dwordx2 v[24:25], v26, s[52:55], s88 offen sc1\n"
    "LA_skip_12:\n"
    "v_mfma_f32_16x16x32_bf16 v[176:179], v[196:199], v[108:111], v[176:179]\n"
    "ds_read_b64_tr_b16 v[194:195], v221 offset:32768\n"
    "v_cvt_pk_bf16_f32 v68, v16, v17\n"
    "v_cvt_pk_bf16_f32 v69, v18, v19\n"
    "v_mfma_f32_16x16x32_bf16 v[136:139], v[200:203], v[44:47], v[136:139]\n"
    "ds_read_b64_tr_b16 v[196:197], v222 offset:32768\n"
    "v_cvt_pk_bf16_f32 v70, v20, v21\n"
    "v_cvt_pk_bf16_f32 v71, v22, v23\n"
    "v_mfma_f32_16x16x32_bf16 v[180:183], v[200:203], v[108:111], v[180:183]\n"
    "s_add_u32 m0, s93, 0x1000\n"
    "s_mul_i32 s89, s70, 0xa0000\n"
    "s_add_u32 s89, s89, 0xa8000\n"
    "buffer_load_dwordx4 v229, s[60:63], s89 offen lds\n"
    "ds_read_b64_tr_b16 v[198:199], v223 offset:32768\n"
    "s_add_u32 s87, s98, 0\n"
    "buffer_store_dwordx4 v[68:71], v230, s[52:55], s87 offen sc1\n"
    "v_mfma_f32_16x16x32_bf16 v[16:19], v[162:165], v[148:151], 0\n"
    "v_mfma_f32_16x16x32_bf16 v[20:23], v[170:173], v[156:159], 0\n"
    "s_nop 6\n"
    "v_mul_f32 v16, v16, v246\n"
    "v_mul_f32 v17, v17, v246\n"
    "s_waitcnt lgkmcnt(8)\n"
    "v_mfma_f32_16x16x32_bf16 v[144:147], v[204:207], v[44:47], v[144:147]\n"
    "ds_read_b64_tr_b16 v[200:201], v220 offset:40960\n"
    "v_mul_f32 v18, v18, v246\n"
    "v_mul_f32 v19, v19, v246\n"
    "v_mfma_f32_16x16x32_bf16 v[184:187], v[204:207], v[108:111], v[184:187]\n"
    "ds_read_b64_tr_b16 v[202:203], v221 offset:40960\n"
    "v_mul_f32 v20, v20, v247\n"
    "v_mul_f32 v21, v21, v247\n"
    "s_waitcnt lgkmcnt(8)\n"
    "v_mfma_f32_16x16x32_bf16 v[152:155], v[208:211], v[44:47], v[152:155]\n"
    "ds_read_b64_tr_b16 v[204:205], v222 offset:40960\n"
    "v_mul_f32 v22, v22, v247\n"
    "v_mul_f32 v23, v23, v247\n"
    "v_mfma_f32_16x16x32_bf16 v[188:191], v[208:211], v[108:111], v[188:191]\n"
    "ds_read_b64_tr_b16 v[206:207], v223 offset:40960\n"
    "v_cvt_pk_bf16_f32 v72, v16, v17\n"
    "v_cvt_pk_bf16_f32 v73, v18, v19\n"
    "s_waitcnt lgkmcnt(8)\n"
    "v_mfma_f32_16x16x32_bf16 v[216:219], v[252:255], v[212:215], v[216:219]\n"
    "ds_read_b64_tr_b16 v[208:209], v224 offset:32768\n"
    "ds_read_b64_tr_b16 v[210:211], v225 offset:32768\n"
    "v_cvt_pk_bf16_f32 v74, v20, v21\n"
    "v_cvt_pk_bf16_f32 v75, v22, v23\n"
    "s_waitcnt lgkmcnt(8)\n"
    "v_mfma_f32_16x16x32_bf16 v[128:131], v[192:195], v[48:51], v[128:131]\n"
    "ds_read_b64_tr_b16 v[252:253], v220 offset:36864\n"
    "s_add_u32 s87, s98, 0x12000\n"
    "buffer_store_dwordx4 v[72:75], v230, s[52:55], s87 offen sc1\n"
    "v_and_b32 v102, 15, v235\n"
    "v_lshrrev_b32 v103, 4, v235\n"
    "v_mfma_f32_16x16x32_bf16 v[176:179], v[192:195], v[112:115], v[176:179]\n"
    "ds_read_b64_tr_b16 v[254:255], v221 offset:36864\n"
    "v_lshrrev_b32 v108, 2, v102\n"
    "v_and_b32 v109, 3, v102\n"
    "s_waitcnt lgkmcnt(8)\n"
    "v_mfma_f32_16x16x32_bf16 v[136:139], v[196:199], v[48:51], v[136:139]\n"
    "ds_read_b64_tr_b16 v[192:193], v222 offset:36864\n"
    "v_lshrrev_b32 v110, 1, v103\n"
    "v_xor_b32 v110, v110, v103\n"
    "v_mfma_f32_16x16x32_bf16 v[180:183], v[196:199], v[112:115], v[180:183]\n"
    "ds_read_b64_tr_b16 v[194:195], v223 offset:36864\n"
    "v_and_b32 v110, 1, v110\n"
    "v_lshrrev_b32 v111, 1, v108\n"
    "s_waitcnt lgkmcnt(8)\n"
    "v_mfma_f32_16x16x32_bf16 v[144:147], v[200:203], v[48:51], v[144:147]\n"
    "ds_read_b64_tr_b16 v[196:197], v220 offset:45056\n"
    "v_lshl_or_b32 v111, v110, 1, v111\n"
    "v_lshl_add_u32 v110, v103, 2, v108\n"
    "v_mfma_f32_16x16x32_bf16 v[184:187], v[200:203], v[112:115], v[184:187]\n"
    "ds_read_b64_tr_b16 v[198:199], v221 offset:45056\n"
    "v_lshlrev_b32 v110, 7, v110\n"
    "v_lshl_add_u32 v110, v109, 3, v110\n"
    "s_waitcnt lgkmcnt(8)\n"
    "v_mfma_f32_16x16x32_bf16 v[152:155], v[204:207], v[48:51], v[152:155]\n"
    "ds_read_b64_tr_b16 v[200:201], v222 offset:45056\n"
    "s_lshl_b32 s87, s70, 13\n"
    "s_add_u32 s87, s87, 0x8000\n"
    "v_add_u32 v110, s87, v110\n"
    "v_lshl_add_u32 v96, v111, 5, v110\n"
    "v_mfma_f32_16x16x32_bf16 v[188:191], v[204:207], v[112:115], v[188:191]\n"
    "ds_read_b64_tr_b16 v[202:203], v223 offset:45056\n"
    "v_xor_b32 v97, 32, v96\n"
    "v_xor_b32 v98, 64, v96\n"
    "s_waitcnt lgkmcnt(8)\n"
    "v_mfma_f32_16x16x32_bf16 v[216:219], v[208:211], v[212:215], v[216:219]\n"
    "ds_read_b64_tr_b16 v[204:205], v224 offset:36864\n"
    "ds_read_b64_tr_b16 v[206:207], v225 offset:36864\n"
    "v_xor_b32 v99, 0x60, v96\n"
    "v_lshrrev_b32 v110, 3, v235\n"
    "s_cmp_eq_u32 s68, 0\n"
    "s_cbranch_scc0 LA_skip_16\n"
    "s_add_u32 s88, s90, 0x80\n"
    "s_mov_b32 s96, 0xff00ff00\n"
    "s_mov_b32 s97, 0xff00ff00\n"
    "s_waitcnt vmcnt(3)\n"
    "v_cmp_eq_u64 vcc, s[78:79], v[24:25]\n"
    "s_nop 4\n"
    "s_and_b64 vcc, vcc, s[96:97]\n"
    "s_cmp_eq_u64 vcc, s[96:97]\n"
    "s_cbranch_scc0 LA_slowE1_13\n"
    "LA_backE1_14:\n"
    "LA_skip_16:\n"
    "s_waitcnt vmcnt(12)\n"
    "s_barrier\n"
    "s_mul_i32 s40, s70, 0x3000\n"
    "s_mul_i32 s41, s70, 0x2000\n"
    "s_mul_i32 s42, s70, 0x4000\n"
    "s_lshl_b32 s86, s71, 3\n"
    "s_add_u32 s86, s86, 0\n"
    "s_add_u32 s86, s86, s69\n"
    "s_mul_i32 s86, s86, 0x12000\n"
    "v_lshlrev_b32 v100, 4, v235\n"
    "v_add_u32 v100, s86, v100\n"
    "s_add_u32 s89, s40, 0x4000\n"
    "buffer_load_dwordx4 v[0:3], v100, s[52:55], s89 offen sc1\n"
    "s_add_u32 s89, s40, 0x4400\n"
    "buffer_load_dwordx4 v[4:7], v100, s[52:55], s89 offen sc1\n"
    "s_add_u32 s89, s40, 0x4c00\n"
    "buffer_load_dwordx4 v[8:11], v100, s[52:55], s89 offen sc1\n"
    "s_add_u32 s89, s40, 0x5000\n"
    "buffer_load_dwordx4 v[12:15], v100, s[52:55], s89 offen sc1\n"
    "s_add_u32 s89, s40, 0x5800\n"
    "buffer_load_dwordx4 v[16:19], v100, s[52:55], s89 offen sc1\n"
    "s_add_u32 s89, s40, 0x5c00\n"
    "buffer_load_dwordx4 v[20:23], v100, s[52:55], s89 offen sc1\n"
    "s_waitcnt lgkmcnt(8)\n"
    "v_mfma_f32_16x16x32_bf16 v[128:131], v[252:255], v[52:55], v[128:131]\n"
    "s_add_u32 m0, s93, 0x4000\n"
    "s_mul_i32 s89, s70, 0xa0000\n"
    "s_add_u32 s89, s89, 0xf0000\n"
    "buffer_load_dwordx4 v229, s[60:63], s89 offen lds\n"
    "ds_read_b64_tr_b16 v[208:209], v220 offset:49152\n"
    "s_cmp_eq_u32 s68, 0\n"
    "s_cbranch_scc0 LA_skip_18\n"
    "v_and_b32 v166, 15, v235\n"
    "v_lshlrev_b32 v166, 3, v166\n"
    "s_add_u32 s88, s90, 0x180\n"
    "buffer_load_dwordx2 v[164:165], v166, s[52:55], s88 offen sc1\n"
    "LA_skip_18:\n"
    "v_mfma_f32_16x16x32_bf16 v[176:179], v[252:255], v[116:119], v[176:179]\n"
    "ds_read_b64_tr_b16 v[210:211], v221 offset:49152\n"
    "v_lshlrev_b32 v110, 10, v110\n"
    "v_and_b32 v111, 7, v235\n"
    "s_waitcnt lgkmcnt(8)\n"
    "v_mfma_f32_16x16x32_bf16 v[136:139], v[192:195], v[52:55], v[136:139]\n"
    "ds_read_b64_tr_b16 v[252:253], v222 offset:49152\n"
    "v_lshl_add_u32 v110, v111, 4, v110\n"
    "s_lshl_b32 s87, s68, 3\n"
    "s_add_u32 s87, s87, s77\n"
    "s_add_u32 s87, s87, 0\n"
    "s_lshl_b32 s87, s87, 10\n"
    "s_lshl_b32 s88, s72, 7\n"
    "s_add_u32 s87, s87, s88\n"
    "v_add_u32 v101, s87, v110\n"
    "buffer_load_dwordx4 v[104:107], v101, s[36:39], 0 offen\n"
    "v_mfma_f32_16x16x32_bf16 v[180:183], v[192:195], v[116:119], v[180:183]\n"
    "s_add_u32 m0, s93, 0x5000\n"
    "s_mul_i32 s89, s70, 0xa0000\n"
    "s_add_u32 s89, s89, 0xf8000\n"
    "buffer_load_dwordx4 v229, s[60:63], s89 offen lds\n"
    "ds_read_b64_tr_b16 v[254:255], v223 offset:49152\n"
    "s_waitcnt lgkmcnt(8)\n"
    "v_mfma_f32_16x16x32_bf16 v[144:147], v[196:199], v[52:55], v[144:147]\n"
    "ds_read_b64_tr_b16 v[192:193], v220 offset:57344\n"
    "v_mfma_f32_16x16x32_bf16 v[184:187], v[196:199], v[116:119], v[184:187]\n"
    "ds_read_b64_tr_b16 v[194:195], v221 offset:57344\n"
    "s_waitcnt lgkmcnt(8)\n"
    "v_mfma_f32_16x16x32_bf16 v[152:155], v[200:203], v[52:55], v[152:155]\n"
    "ds_read_b64_tr_b16 v[196:197], v222 offset:57344\n"
    "v_mfma_f32_16x16x32_bf16 v[188:191], v[200:203], v[116:119], v[188:191]\n"
    "ds_read_b64_tr_b16 v[198:199], v223 offset:57344\n"
    "s_waitcnt lgkmcnt(8)\n"
    "v_mfma_f32_16x16x32_bf16 v[216:219], v[204:207], v[212:215], v[216:219]\n"
    "ds_read_b64_tr_b16 v[200:201], v224 offset:49152\n"
    "ds_read_b64_tr_b16 v[202:203], v225 offset:49152\n"
    "s_waitcnt lgkmcnt(8)\n"
    "v_mfma_f32_16x16x32_bf16 v[128:131], v[208:211], v[56:59], v[128:131]\n"
    "ds_read_b64_tr_b16 v[204:205], v220 offset:53248\n"
    "v_mfma_f32_16x16x32_bf16 v[176:179], v[208:211], v[120:123], v[176:179]\n"
    "ds_read_b64_tr_b16 v[206:207], v221 offset:53248\n"
    "s_waitcnt lgkmcnt(8)\n"
    "v_mfma_f32_16x16x32_bf16 v[136:139], v[252:255], v[56:59], v[136:139]\n"
    "ds_read_b64_tr_b16 v[208:209], v222 offset:53248\n"
    "v_mfma_f32_16x16x32_bf16 v[180:183], v[252:255], v[120:123], v[180:183]\n"
    "ds_read_b64_tr_b16 v[210:211], v223 offset:53248\n"
    "s_waitcnt lgkmcnt(8)\n"
    "v_mfma_f32_16x16x32_bf16 v[144:147], v[192:195], v[56:59], v[144:147]\n"
    "ds_read_b64_tr_b16 v[252:253], v220 offset:61440\n"
    "v_mfma_f32_16x16x32_bf16 v[184:187], v[192:195], v[120:123], v[184:187]\n"
    "ds_read_b64_tr_b16 v[254:255], v221 offset:61440\n"
    "s_waitcnt lgkmcnt(8)\n"
    "v_mfma_f32_16x16x32_bf16 v[152:155], v[196:199], v[56:59], v[152:155]\n"
    "ds_read_b64_tr_b16 v[192:193], v222 offset:61440\n"
    "v_mfma_f32_16x16x32_bf16 v[188:191], v[196:199], v[120:123], v[188:191]\n"
    "ds_read_b64_tr_b16 v[194:195], v223 offset:61440\n"
    "s_waitcnt lgkmcnt(8)\n"
    "v_mfma_f32_16x16x32_bf16 v[216:219], v[200:203], v[212:215], v[216:219]\n"
    "ds_read_b64_tr_b16 v[196:197], v224 offset:53248\n"
    "ds_read_b64_tr_b16 v[198:199], v225 offset:53248\n"
    "s_waitcnt vmcnt(9)\n"
    "s_barrier\n"
    "s_waitcnt lgkmcnt(8)\n"
    "v_mfma_f32_16x16x32_bf16 v[128:131], v[204:207], v[60:63], v[128:131]\n"
    "s_add_u32 m0, s93, 0x8000\n"
    "s_mul_i32 s89, s70, 0x40000\n"
    "s_add_u32 s89, s89, 0\n"
    "buffer_load_dwordx4 v229, s[56:59], s89 offen lds\n"
    "v_xor_b32 v96, 0x10000, v96\n"
    "v_xor_b32 v97, 0x10000, v97\n"
    "v_xor_b32 v98, 0x10000, v98\n"
    "v_xor_b32 v99, 0x10000, v99\n"
    "ds_read_b64_tr_b16 v[64:65], v96 offset:0\n"
    "v_mfma_f32_16x16x32_bf16 v[176:179], v[204:207], v[124:127], v[176:179]\n"
    "ds_read_b64_tr_b16 v[66:67], v96 offset:2048\n"
    "s_cmp_eq_u32 s68, 0\n"
    "s_cbranch_scc0 LA_skip_11\n"
    "s_mul_i32 s87, s72, 8\n"
    "s_add_u32 s87, s87, 0x80\n"
    "s_add_u32 s87, s87, s90\n"
    "v_mov_b32 v226, s78\n"
    "v_mov_b32 v227, s79\n"
    "s_mov_b64 exec, 1\n"
    "buffer_store_dwordx2 v[226:227], off, s[52:55], s87 sc1\n"
    "s_mov_b64 exec, -1\n"
    "LA_skip_11:\n"
    "s_waitcnt lgkmcnt(8)\n"
    "s_nop 2\n"
    "v_mfma_f32_16x16x32_bf16 v[136:139], v[208:211], v[60:63], v[136:139]\n"
    "ds_read_b64_tr_b16 v[68:69], v97 offset:0\n"
    "v_mfma_f32_16x16x32_bf16 v[180:183], v[208:211], v[124:127], v[180:183]\n"
    "s_add_u32 m0, s93, 0x9000\n"
    "s_mul_i32 s89, s70, 0x40000\n"
    "s_add_u32 s89, s89, 0x8000\n"
    "buffer_load_dwordx4 v229, s[56:59], s89 offen lds\n"
    "ds_read_b64_tr_b16 v[70:71], v97 offset:2048\n"
    "s_waitcnt lgkmcnt(8)\n"
    "v_mfma_f32_16x16x32_bf16 v[144:147], v[252:255], v[60:63], v[144:147]\n"
    "ds_read_b64_tr_b16 v[72:73], v98 offset:0\n"
    "v_mfma_f32_16x16x32_bf16 v[184:187], v[252:255], v[124:127], v[184:187]\n"
    "ds_read_b64_tr_b16 v[74:75], v98 offset:2048\n"
    "s_waitcnt lgkmcnt(8)\n"
    "v_mfma_f32_16x16x32_bf16 v[152:155], v[192:195], v[60:63], v[152:155]\n"
    "ds_read_b64_tr_b16 v[76:77], v99 offset:0\n"
    "v_mfma_f32_16x16x32_bf16 v[188:191], v[192:195], v[124:127], v[188:191]\n"
    "ds_read_b64_tr_b16 v[78:79], v99 offset:2048\n"
    "s_waitcnt lgkmcnt(8)\n"
    "v_mfma_f32_16x16x32_bf16 v[216:219], v[196:199], v[212:215], v[216:219]\n"
    "ds_read_b64_tr_b16 v[80:81], v96 offset:4096\n"
    "ds_read_b64_tr_b16 v[82:83], v96 offset:6144\n"
    "ds_read_b64_tr_b16 v[84:85], v97 offset:4096\n"
    "ds_read_b64_tr_b16 v[86:87], v97 offset:6144\n"
    "ds_read_b64_tr_b16 v[88:89], v98 offset:4096\n"
    "ds_read_b64_tr_b16 v[90:91], v98 offset:6144\n"
    "ds_read_b64_tr_b16 v[92:93], v99 offset:4096\n"
    "ds_read_b64_tr_b16 v[94:95], v99 offset:6144\n"
    "s_lshl_b32 s87, s68, 7\n"
    "v_lshrrev_b32 v172, 4, v235\n"
    "v_lshlrev_b32 v172, 4, v172\n"
    "v_add_u32 v172, s87, v172\n"
    "s_mov_b32 exec_lo, 0x10001\n"
    "s_mov_b32 exec_hi, 0x10001\n"
    "ds_write_b128 v172, v[216:219] offset:26624\n"
    "s_mov_b64 exec, -1\n"
    "s_waitcnt vmcnt(9) lgkmcnt(0)\n"
    "s_barrier\n"
    "s_add_u32 s89, s40, 0x6400\n"
    "buffer_load_dwordx4 v[24:27], v100, s[52:55], s89 offen sc1\n"
    "s_add_u32 s89, s40, 0x6800\n"
    "buffer_load_dwordx4 v[28:31], v100, s[52:55], s89 offen sc1\n"
    "ds_read_b128 v[238:241], v236 offset:26624\n"
    "ds_read_b128 v[242:245], v236 offset:27136\n"
    "v_mul_f32 v250, 0xbfb8aa3b, v233\n"
    "v_mul_f32 v251, v233, v233\n"
    "v_rcp_f32 v251, v251\n"
    "s_waitcnt lgkmcnt(1)\n"
    "v_fma_f32 v128, -v231, v238, v128\n"
    "v_fma_f32 v129, -v231, v239, v129\n"
    "v_fma_f32 v130, -v231, v240, v130\n"
    "v_fma_f32 v131, -v231, v241, v131\n"
    "s_waitcnt lgkmcnt(0)\n"
    "v_fma_f32 v144, -v231, v242, v144\n"
    "v_fma_f32 v145, -v231, v243, v145\n"
    "v_fma_f32 v146, -v231, v244, v146\n"
    "v_fma_f32 v147, -v231, v245, v147\n"
    "ds_read_b128 v[238:241], v236 offset:26752\n"
    "ds_read_b128 v[242:245], v236 offset:27264\n"
    "v_mul_f32 v246, v128, v250\n"
    "v_mul_f32 v247, v129, v250\n"
    "v_mul_f32 v248, v130, v250\n"
    "v_mul_f32 v249, v131, v250\n"
    "v_exp_f32 v246, v246\n"
    "v_exp_f32 v247, v247\n"
    "v_exp_f32 v248, v248\n"
    "v_exp_f32 v249, v249\n"
    "v_mul_f32 v128, v128, v144\n"
    "v_mul_f32 v129, v129, v145\n"
    "v_mul_f32 v130, v130, v146\n"
    "v_mul_f32 v131, v131, v147\n"
    "v_fma_f32 v246, v246, v251, v251\n"
    "v_fma_f32 v247, v247, v251, v251\n"
    "v_fma_f32 v248, v248, v251, v251\n"
    "v_fma_f32 v249, v249, v251, v251\n"
    "v_rcp_f32 v246, v246\n"
    "v_rcp_f32 v247, v247\n"
    "v_rcp_f32 v248, v248\n"
    "v_rcp_f32 v249, v249\n"
    "v_mul_f32 v128, v128, v246\n"
    "v_mul_f32 v129, v129, v247\n"
    "v_mul_f32 v130, v130, v248\n"
    "v_mul_f32 v131, v131, v249\n"
    "v_cvt_pk_bf16_f32 v144, v128, v129\n"
    "v_cvt_pk_bf16_f32 v145, v130, v131\n"
    "s_waitcnt lgkmcnt(1)\n"
    "v_fma_f32 v136, -v231, v238, v136\n"
    "v_fma_f32 v137, -v231, v239, v137\n"
    "v_fma_f32 v138, -v231, v240, v138\n"
    "v_fma_f32 v139, -v231, v241, v139\n"
    "s_waitcnt lgkmcnt(0)\n"
    "v_fma_f32 v152, -v231, v242, v152\n"
    "v_fma_f32 v153, -v231, v243, v153\n"
    "v_mfma_f32_16x16x32_bf16 v[32:35], v[64:67], v[0:3], 0\n"
    "ds_read_b64_tr_b16 v[48:49], v96 offset:16384\n"
    "ds_read_b64_tr_b16 v[50:51], v96 offset:18432\n"
    "v_fma_f32 v154, -v231, v244, v154\n"
    "v_fma_f32 v155, -v231, v245, v155\n"
    "v_mfma_f32_16x16x32_bf16 v[36:39], v[68:71], v[0:3], 0\n"
    "ds_read_b64_tr_b16 v[52:53], v97 offset:16384\n"
    "ds_read_b64_tr_b16 v[54:55], v97 offset:18432\n"
    "v_mul_f32 v246, v136, v250\n"
    "v_mul_f32 v247, v137, v250\n"
    "v_mfma_f32_16x16x32_bf16 v[40:43], v[72:75], v[0:3], 0\n"
    "ds_read_b64_tr_b16 v[56:57], v98 offset:16384\n"
    "ds_read_b64_tr_b16 v[58:59], v98 offset:18432\n"
    "v_mul_f32 v248, v138, v250\n"
    "v_mul_f32 v249, v139, v250\n"
    "v_mfma_f32_16x16x32_bf16 v[44:47], v[76:79], v[0:3], 0\n"
    "ds_read_b64_tr_b16 v[60:61], v99 offset:16384\n"
    "ds_read_b64_tr_b16 v[62:63], v99 offset:18432\n"
    "v_exp_f32 v246, v246\n"
    "v_mfma_f32_16x16x32_bf16 v[32:35], v[80:83], v[4:7], v[32:35]\n"
    "ds_read_b64_tr_b16 v[112:113], v96 offset:20480\n"
    "ds_read_b64_tr_b16 v[114:115], v96 offset:22528\n"
    "v_exp_f32 v247, v247\n"
    "v_mfma_f32_16x16x32_bf16 v[36:39], v[84:87], v[4:7], v[36:39]\n"
    "ds_read_b64_tr_b16 v[116:117], v97 offset:20480\n"
    "ds_read_b64_tr_b16 v[118:119], v97 offset:22528\n"
    "v_exp_f32 v248, v248\n"
    "v_mfma_f32_16x16x32_bf16 v[40:43], v[88:91], v[4:7], v[40:43]\n"
    "ds_read_b64_tr_b16 v[120:121], v98 offset:20480\n"
    "ds_read_b64_tr_b16 v[122:123], v98 offset:22528\n"
    "v_exp_f32 v249, v249\n"
    "v_mfma_f32_16x16x32_bf16 v[44:47], v[92:95], v[4:7], v[44:47]\n"
    "ds_read_b64_tr_b16 v[124:125], v99 offset:20480\n"
    "ds_read_b64_tr_b16 v[126:127], v99 offset:22528\n"
    "v_mul_f32 v136, v136, v152\n"
    "v_mul_f32 v137, v137, v153\n"
    "s_cmp_eq_u32 s68, 0\n"
    "s_cbranch_scc0 LA_skip_22\n"
    "s_add_u32 s88, s90, 0x180\n"
    "s_mov_b32 s96, 0xff00ff00\n"
    "s_mov_b32 s97, 0xff00ff00\n"
    "s_waitcnt vmcnt(6)\n"
    "v_cmp_eq_u64 vcc, s[78:79], v[164:165]\n"
    "s_nop 4\n"
    "s_and_b64 vcc, vcc, s[96:97]\n"
    "s_cmp_eq_u64 vcc, s[96:97]\n"
    "s_cbranch_scc0 LA_slowE2_19\n"
    "LA_backE2_20:\n"
    "LA_skip_22:\n"
    "s_waitcnt vmcnt(9)\n"
    "s_barrier\n"
    "s_add_u32 s89, s40, 0x4800\n"
    "buffer_load_dwordx4 v[0:3], v100, s[52:55], s89 offen sc1\n"
    "s_add_u32 s89, s40, 0x5400\n"
    "buffer_load_dwordx4 v[4:7], v100, s[52:55], s89 offen sc1\n"
    "s_cmp_eq_u32 s68, 0\n"
    "s_cbranch_scc0 LA_skip_23\n"
    "v_and_b32 v162, 15, v235\n"
    "v_lshlrev_b32 v162, 3, v162\n"
    "s_add_u32 s88, s90, 0x80\n"
    "buffer_load_dwordx2 v[160:161], v162, s[52:55], s88 offen sc1\n"
    "LA_skip_23:\n"
    "v_mul_f32 v138, v138, v154\n"
    "v_mul_f32 v139, v139, v155\n"
    "v_fma_f32 v246, v246, v251, v251\n"
    "v_fma_f32 v247, v247, v251, v251\n"
    "v_fma_f32 v248, v248, v251, v251\n"
    "v_fma_f32 v249, v249, v251, v251\n"
    "v_rcp_f32 v246, v246\n"
    "v_rcp_f32 v247, v247\n"
    "v_rcp_f32 v248, v248\n"
    "v_rcp_f32 v249, v249\n"
    "v_mul_f32 v136, v136, v246\n"
    "v_mul_f32 v137, v137, v247\n"
    "v_mul_f32 v138, v138, v248\n"
    "v_mul_f32 v139, v139, v249\n"
    "v_cvt_pk_bf16_f32 v146, v136, v137\n"
    "v_cvt_pk_bf16_f32 v147, v138, v139\n"
    "s_add_u32 s87, s99, 0\n"
    "buffer_store_dwordx4 v[144:147], v230, s[52:55], s87 offen sc1\n"
    "ds_read_b128 v[238:241], v236 offset:26624\n"
    "ds_read_b128 v[242:245], v236 offset:27136\n"
    "v_mul_f32 v250, 0xbfb8aa3b, v234\n"
    "v_mul_f32 v251, v234, v234\n"
    "v_rcp_f32 v251, v251\n"
    "s_waitcnt lgkmcnt(1)\n"
    "v_fma_f32 v176, -v232, v238, v176\n"
    "v_fma_f32 v177, -v232, v239, v177\n"
    "v_fma_f32 v178, -v232, v240, v178\n"
    "v_fma_f32 v179, -v232, v241, v179\n"
    "s_waitcnt lgkmcnt(0)\n"
    "v_fma_f32 v184, -v232, v242, v184\n"
    "v_fma_f32 v185, -v232, v243, v185\n"
    "v_fma_f32 v186, -v232, v244, v186\n"
    "v_fma_f32 v187, -v232, v245, v187\n"
    "ds_read_b128 v[238:241], v236 offset:26752\n"
    "ds_read_b128 v[242:245], v236 offset:27264\n"
    "v_mul_f32 v246, v176, v250\n"
    "v_mul_f32 v247, v177, v250\n"
    "v_mul_f32 v248, v178, v250\n"
    "v_mul_f32 v249, v179, v250\n"
    "v_exp_f32 v246, v246\n"
    "v_exp_f32 v247, v247\n"
    "v_exp_f32 v248, v248\n"
    "v_exp_f32 v249, v249\n"
    "v_mul_f32 v176, v176, v184\n"
    "v_mul_f32 v177, v177, v185\n"
    "v_mul_f32 v178, v178, v186\n"
    "v_mul_f32 v179, v179, v187\n"
    "v_fma_f32 v246, v246, v251, v251\n"
    "v_fma_f32 v247, v247, v251, v251\n"
    "v_mfma_f32_16x16x32_bf16 v[32:35], v[48:51], v[8:11], v[32:35]\n"
    "ds_read_b64_tr_b16 v[64:65], v96 offset:32768\n"
    "ds_read_b64_tr_b16 v[66:67], v96 offset:34816\n"
    "v_fma_f32 v248, v248, v251, v251\n"
    "v_fma_f32 v249, v249, v251, v251\n"
    "v_mfma_f32_16x16x32_bf16 v[36:39], v[52:55], v[8:11], v[36:39]\n"
    "ds_read_b64_tr_b16 v[68:69], v97 offset:32768\n"
    "ds_read_b64_tr_b16 v[70:71], v97 offset:34816\n"
    "v_rcp_f32 v246, v246\n"
    "v_mfma_f32_16x16x32_bf16 v[40:43], v[56:59], v[8:11], v[40:43]\n"
    "ds_read_b64_tr_b16 v[72:73], v98 offset:32768\n"
    "ds_read_b64_tr_b16 v[74:75], v98 offset:34816\n"
    "v_rcp_f32 v247, v247\n"
    "v_mfma_f32_16x16x32_bf16 v[44:47], v[60:63], v[8:11], v[44:47]\n"
    "ds_read_b64_tr_b16 v[76:77], v99 offset:32768\n"
    "ds_read_b64_tr_b16 v[78:79], v99 offset:34816\n"
    "v_rcp_f32 v248, v248\n"
    "v_mfma_f32_16x16x32_bf16 v[32:35], v[112:115], v[12:15], v[32:35]\n"
    "ds_read_b64_tr_b16 v[80:81], v96 offset:36864\n"
    "ds_read_b64_tr_b16 v[82:83], v96 offset:38912\n"
    "v_rcp_f32 v249, v249\n"
    "v_mfma_f32_16x16x32_bf16 v[36:39], v[116:119], v[12:15], v[36:39]\n"
    "ds_read_b64_tr_b16 v[84:85], v97 offset:36864\n"
    "ds_read_b64_tr_b16 v[86:87], v97 offset:38912\n"
    "v_mul_f32 v176, v176, v246\n"
    "v_mul_f32 v177, v177, v247\n"
    "v_mfma_f32_16x16x32_bf16 v[40:43], v[120:123], v[12:15], v[40:43]\n"
    "ds_read_b64_tr_b16 v[88:89], v98 offset:36864\n"
    "ds_read_b64_tr_b16 v[90:91], v98 offset:38912\n"
    "v_mul_f32 v178, v178, v248\n"
    "v_mul_f32 v179, v179, v249\n"
    "v_mfma_f32_16x16x32_bf16 v[44:47], v[124:127], v[12:15], v[44:47]\n"
    "ds_read_b64_tr_b16 v[92:93], v99 offset:36864\n"
    "ds_read_b64_tr_b16 v[94:95], v99 offset:38912\n"
    "v_cvt_pk_bf16_f32 v184, v176, v177\n"
    "v_cvt_pk_bf16_f32 v185, v178, v179\n"
    "s_waitcnt vmcnt(10)\n"
    "s_barrier\n"
    "s_add_u32 s89, s40, 0x6000\n"
    "buffer_load_dwordx4 v[8:11], v100, s[52:55], s89 offen sc1\n"
    "s_add_u32 s89, s40, 0x6c00\n"
    "buffer_load_dwordx4 v[12:15], v100, s[52:55], s89 offen sc1\n"
    "s_waitcnt lgkmcnt(14)\n"
    "v_mfma_f32_16x16x32_bf16 v[32:35], v[64:67], v[16:19], v[32:35]\n"
    "ds_read_b64_tr_b16 v[48:49], v96 offset:49152\n"
    "ds_read_b64_tr_b16 v[50:51], v96 offset:51200\n"
    "v_fma_f32 v180, -v232, v238, v180\n"
    "v_fma_f32 v181, -v232, v239, v181\n"
    "s_waitcnt lgkmcnt(14)\n"
    "v_mfma_f32_16x16x32_bf16 v[36:39], v[68:71], v[16:19], v[36:39]\n"
    "ds_read_b64_tr_b16 v[52:53], v97 offset:49152\n"
    "ds_read_b64_tr_b16 v[54:55], v97 offset:51200\n"
    "v_fma_f32 v182, -v232, v240, v182\n"
    "v_fma_f32 v183, -v232, v241, v183\n"
    "s_waitcnt lgkmcnt(14)\n"
    "v_mfma_f32_16x16x32_bf16 v[40:43], v[72:75], v[16:19], v[40:43]\n"
    "ds_read_b64_tr_b16 v[56:57], v98 offset:49152\n"
    "ds_read_b64_tr_b16 v[58:59], v98 offset:51200\n"
    "v_fma_f32 v188, -v232, v242, v188\n"
    "v_fma_f32 v189, -v232, v243, v189\n"
    "s_waitcnt lgkmcnt(14)\n"
    "v_mfma_f32_16x16x32_bf16 v[44:47], v[76:79], v[16:19], v[44:47]\n"
    "ds_read_b64_tr_b16 v[60:61], v99 offset:49152\n"
    "ds_read_b64_tr_b16 v[62:63], v99 offset:51200\n"
    "v_fma_f32 v190, -v232, v244, v190\n"
    "v_fma_f32 v191, -v232, v245, v191\n"
    "s_waitcnt lgkmcnt(14)\n"
    "v_mfma_f32_16x16x32_bf16 v[32:35], v[80:83], v[20:23], v[32:35]\n"
    "ds_read_b64_tr_b16 v[112:113], v96 offset:53248\n"
    "ds_read_b64_tr_b16 v[114:115], v96 offset:55296\n"
    "v_mul_f32 v246, v180, v250\n"
    "v_mul_f32 v247, v181, v250\n"
    "s_waitcnt lgkmcnt(14)\n"
    "v_mfma_f32_16x16x32_bf16 v[36:39], v[84:87], v[20:23], v[36:39]\n"
    "ds_read_b64_tr_b16 v[116:117], v97 offset:53248\n"
    "ds_read_b64_tr_b16 v[118:119], v97 offset:55296\n"
    "v_mul_f32 v248, v182, v250\n"
    "v_mul_f32 v249, v183, v250\n"
    "s_waitcnt lgkmcnt(14)\n"
    "v_mfma_f32_16x16x32_bf16 v[40:43], v[88:91], v[20:23], v[40:43]\n"
    "ds_read_b64_tr_b16 v[120:121], v98 offset:53248\n"
    "ds_read_b64_tr_b16 v[122:123], v98 offset:55296\n"
    "v_exp_f32 v246, v246\n"
    "s_waitcnt lgkmcnt(14)\n"
    "v_mfma_f32_16x16x32_bf16 v[44:47], v[92:95], v[20:23], v[44:47]\n"
    "ds_read_b64_tr_b16 v[124:125], v99 offset:53248\n"
    "ds_read_b64_tr_b16 v[126:127], v99 offset:55296\n"
    "v_exp_f32 v247, v247\n"
    "v_exp_f32 v248, v248\n"
    "v_exp_f32 v249, v249\n"
    "v_mul_f32 v180, v180, v188\n"
    "v_mul_f32 v181, v181, v189\n"
    "v_mul_f32 v182, v182, v190\n"
    "v_mul_f32 v183, v183, v191\n"
    "v_fma_f32 v246, v246, v251, v251\n"
    "v_fma_f32 v247, v247, v251, v251\n"
    "v_fma_f32 v248, v248, v251, v251\n"
    "v_fma_f32 v249, v249, v251, v251\n"
    "v_rcp_f32 v246, v246\n"
    "v_rcp_f32 v247, v247\n"
    "v_rcp_f32 v248, v248\n"
    "v_rcp_f32 v249, v249\n"
    "v_mul_f32 v180, v180, v246\n"
    "v_mul_f32 v181, v181, v247\n"
    "v_mul_f32 v182, v182, v248\n"
    "v_mul_f32 v183, v183, v249\n"
    "v_cvt_pk_bf16_f32 v186, v180, v181\n"
    "v_cvt_pk_bf16_f32 v187, v182, v183\n"
    "s_add_u32 s87, s99, 0x12000\n"
    "buffer_store_dwordx4 v[184:187], v230, s[52:55], s87 offen sc1\n"
    "s_cmp_eq_u32 s68, 0\n"
    "s_cbranch_scc0 LA_skip_27\n"
    "s_add_u32 s88, s90, 0x80\n"
    "s_mov_b32 s96, 0xff00ff\n"
    "s_mov_b32 s97, 0xff00ff\n"
    "s_waitcnt vmcnt(4)\n"
    "v_cmp_eq_u64 vcc, s[78:79], v[160:161]\n"
    "s_nop 4\n"
    "s_and_b64 vcc, vcc, s[96:97]\n"
    "s_cmp_eq_u64 vcc, s[96:97]\n"
    "s_cbranch_scc0 LA_slowE3_24\n"
    "LA_backE3_25:\n"
    "LA_skip_27:\n"
    "s_waitcnt vmcnt(6)\n"
    "s_barrier\n"
    "s_add_u32 s89, s41, 0\n"
    "buffer_load_dwordx4 v[16:19], v100, s[52:55], s89 offen sc1\n"
    "s_add_u32 s89, s41, 0x400\n"
    "buffer_load_dwordx4 v[20:23], v100, s[52:55], s89 offen sc1\n"
    "s_waitcnt lgkmcnt(14)\n"
    "v_mfma_f32_16x16x32_bf16 v[32:35], v[48:51], v[24:27], v[32:35]\n"
    "v_xor_b32 v96, 0x10000, v96\n"
    "v_xor_b32 v97, 0x10000, v97\n"
    "v_xor_b32 v98, 0x10000, v98\n"
    "v_xor_b32 v99, 0x10000, v99\n"
    "ds_read_b64_tr_b16 v[64:65], v96 offset:0\n"
    "ds_read_b64_tr_b16 v[66:67], v96 offset:2048\n"
    "s_waitcnt lgkmcnt(14)\n"
    "v_mfma_f32_16x16x32_bf16 v[36:39], v[52:55], v[24:27], v[36:39]\n"
    "ds_read_b64_tr_b16 v[68:69], v97 offset:0\n"
    "ds_read_b64_tr_b16 v[70:71], v97 offset:2048\n"
    "s_add_u32 m0, s93, 0xc000\n"
    "s_mul_i32 s89, s70, 0x40000\n"
    "s_add_u32 s89, s89, 0x10000\n"
    "buffer_load_dwordx4 v229, s[56:59], s89 offen lds\n"
    "s_waitcnt lgkmcnt(14)\n"
    "v_mfma_f32_16x16x32_bf16 v[40:43], v[56:59], v[24:27], v[40:43]\n"
    "ds_read_b64_tr_b16 v[72:73], v98 offset:0\n"
    "ds_read_b64_tr_b16 v[74:75], v98 offset:2048\n"
    "s_waitcnt lgkmcnt(14)\n"
    "v_mfma_f32_16x16x32_bf16 v[44:47], v[60:63], v[24:27], v[44:47]\n"
    "ds_read_b64_tr_b16 v[76:77], v99 offset:0\n"
    "ds_read_b64_tr_b16 v[78:79], v99 offset:2048\n"
    "s_add_u32 m0, s93, 0xd000\n"
    "s_mul_i32 s89, s70, 0x40000\n"
    "s_add_u32 s89, s89, 0x18000\n"
    "buffer_load_dwordx4 v229, s[56:59], s89 offen lds\n"
    "s_waitcnt lgkmcnt(14)\n"
    "v_mfma_f32_16x16x32_bf16 v[32:35], v[112:115], v[28:31], v[32:35]\n"
    "ds_read_b64_tr_b16 v[80:81], v96 offset:4096\n"
    "ds_read_b64_tr_b16 v[82:83], v96 offset:6144\n"
    "s_waitcnt lgkmcnt(14)\n"
    "v_mfma_f32_16x16x32_bf16 v[36:39], v[116:119], v[28:31], v[36:39]\n"
    "ds_read_b64_tr_b16 v[84:85], v97 offset:4096\n"
    "ds_read_b64_tr_b16 v[86:87], v97 offset:6144\n"
    "s_waitcnt lgkmcnt(14)\n"
    "v_mfma_f32_16x16x32_bf16 v[40:43], v[120:123], v[28:31], v[40:43]\n"
    "ds_read_b64_tr_b16 v[88:89], v98 offset:4096\n"
    "ds_read_b64_tr_b16 v[90:91], v98 offset:6144\n"
    "s_waitcnt lgkmcnt(14)\n"
    "v_mfma_f32_16x16x32_bf16 v[44:47], v[124:127], v[28:31], v[44:47]\n"
    "ds_read_b64_tr_b16 v[92:93], v99 offset:4096\n"
    "ds_read_b64_tr_b16 v[94:95], v99 offset:6144\n"
    "s_waitcnt vmcnt(4)\n"
    "s_barrier\n"
    "s_add_u32 s89, s41, 0x800\n"
    "buffer_load_dwordx4 v[24:27], v100, s[52:55], s89 offen sc1\n"
    "s_add_u32 s89, s41, 0xc00\n"
    "buffer_load_dwordx4 v[28:31], v100, s[52:55], s89 offen sc1\n"
    "s_waitcnt lgkmcnt(14)\n"
    "v_mfma_f32_16x16x32_bf16 v[32:35], v[64:67], v[0:3], v[32:35]\n"
    "ds_read_b64_tr_b16 v[48:49], v96 offset:16384\n"
    "ds_read_b64_tr_b16 v[50:51], v96 offset:18432\n"
    "s_cmp_eq_u32 s68, 0\n"
    "s_cbranch_scc0 LA_skip_17\n"
    "s_mul_i32 s87, s72, 8\n"
    "s_add_u32 s87, s87, 0x100\n"
    "s_add_u32 s87, s87, s90\n"
    "v_mov_b32 v226, s78\n"
    "v_mov_b32 v227, s79\n"
    "s_mov_b64 exec, 1\n"
    "buffer_store_dwordx2 v[226:227], off, s[52:55], s87 sc1\n"
    "s_mov_b64 exec, -1\n"
    "LA_skip_17:\n"
    "s_waitcnt lgkmcnt(14)\n"
    "s_nop 2\n"
    "v_mfma_f32_16x16x32_bf16 v[36:39], v[68:71], v[0:3], v[36:39]\n"
    "ds_read_b64_tr_b16 v[52:53], v97 offset:16384\n"
    "ds_read_b64_tr_b16 v[54:55], v97 offset:18432\n"
    "s_add_u32 m0, s93, 0x10000\n"
    "s_mul_i32 s89, s70, 0x40000\n"
    "s_add_u32 s89, s89, 0x20000\n"
    "buffer_load_dwordx4 v229, s[56:59], s89 offen lds\n"
    "s_waitcnt lgkmcnt(14)\n"
    "v_mfma_f32_16x16x32_bf16 v[40:43], v[72:75], v[0:3], v[40:43]\n"
    "ds_read_b64_tr_b16 v[56:57], v98 offset:16384\n"
    "ds_read_b64_tr_b16 v[58:59], v98 offset:18432\n"
    "s_waitcnt lgkmcnt(14)\n"
    "v_mfma_f32_16x16x32_bf16 v[44:47], v[76:79], v[0:3], v[44:47]\n"
    "ds_read_b64_tr_b16 v[60:61], v99 offset:16384\n"
    "ds_read_b64_tr_b16 v[62:63], v99 offset:18432\n"
    "s_add_u32 m0, s93, 0x11000\n"
    "s_mul_i32 s89, s70, 0x40000\n"
    "s_add_u32 s89, s89, 0x28000\n"
    "buffer_load_dwordx4 v229, s[56:59], s89 offen lds\n"
    "s_waitcnt lgkmcnt(14)\n"
    "v_mfma_f32_16x16x32_bf16 v[32:35], v[80:83], v[4:7], v[32:35]\n"
    "ds_read_b64_tr_b16 v[112:113], v96 offset:20480\n"
    "ds_read_b64_tr_b16 v[114:115], v96 offset:22528\n"
    "s_waitcnt lgkmcnt(14)\n"
    "v_mfma_f32_16x16x32_bf16 v[36:39], v[84:87], v[4:7], v[36:39]\n"
    "ds_read_b64_tr_b16 v[116:117], v97 offset:20480\n"
    "ds_read_b64_tr_b16 v[118:119], v97 offset:22528\n"
    "s_waitcnt lgkmcnt(14)\n"
    "v_mfma_f32_16x16x32_bf16 v[40:43], v[88:91], v[4:7], v[40:43]\n"
    "ds_read_b64_tr_b16 v[120:121], v98 offset:20480\n"
    "ds_read_b64_tr_b16 v[122:123], v98 offset:22528\n"
    "s_waitcnt lgkmcnt(14)\n"
    "v_mfma_f32_16x16x32_bf16 v[44:47], v[92:95], v[4:7], v[44:47]\n"
    "ds_read_b64_tr_b16 v[124:125], v99 offset:20480\n"
    "ds_read_b64_tr_b16 v[126:127], v99 offset:22528\n"
    "s_barrier\n"
    "s_add_u32 s89, s41, 0x1000\n"
    "buffer_load_dwordx4 v[0:3], v100, s[52:55], s89 offen sc1\n"
    "s_add_u32 s89, s41, 0x1400\n"
    "buffer_load_dwordx4 v[4:7], v100, s[52:55], s89 offen sc1\n"
    "s_waitcnt lgkmcnt(14)\n"
    "v_mfma_f32_16x16x32_bf16 v[32:35], v[48:51], v[8:11], v[32:35]\n"
    "ds_read_b64_tr_b16 v[64:65], v96 offset:32768\n"
    "ds_read_b64_tr_b16 v[66:67], v96 offset:34816\n"
    "s_cmp_eq_u32 s68, 0\n"
    "s_cbranch_scc0 LA_skip_29\n"
    "v_and_b32 v170, 15, v235\n"
    "v_lshlrev_b32 v170, 3, v170\n"
    "s_add_u32 s88, s90, 0x100\n"
    "buffer_load_dwordx2 v[168:169], v170, s[52:55], s88 offen sc1\n"
    "LA_skip_29:\n"
    "s_waitcnt lgkmcnt(14)\n"
    "v_mfma_f32_16x16x32_bf16 v[36:39], v[52:55], v[8:11], v[36:39]\n"
    "ds_read_b64_tr_b16 v[68:69], v97 offset:32768\n"
    "ds_read_b64_tr_b16 v[70:71], v97 offset:34816\n"
    "s_add_u32 m0, s93, 0x14000\n"
    "s_mul_i32 s89, s70, 0x40000\n"
    "s_add_u32 s89, s89, 0x30000\n"
    "buffer_load_dwordx4 v229, s[56:59], s89 offen lds\n"
    "s_waitcnt lgkmcnt(14)\n"
    "v_mfma_f32_16x16x32_bf16 v[40:43], v[56:59], v[8:11], v[40:43]\n"
    "ds_read_b64_tr_b16 v[72:73], v98 offset:32768\n"
    "ds_read_b64_tr_b16 v[74:75], v98 offset:34816\n"
    "s_waitcnt lgkmcnt(14)\n"
    "v_mfma_f32_16x16x32_bf16 v[44:47], v[60:63], v[8:11], v[44:47]\n"
    "ds_read_b64_tr_b16 v[76:77], v99 offset:32768\n"
    "ds_read_b64_tr_b16 v[78:79], v99 offset:34816\n"
    "s_add_u32 m0, s93, 0x15000\n"
    "s_mul_i32 s89, s70, 0x40000\n"
    "s_add_u32 s89, s89, 0x38000\n"
    "buffer_load_dwordx4 v229, s[56:59], s89 offen lds\n"
    "s_waitcnt lgkmcnt(14)\n"
    "v_mfma_f32_16x16x32_bf16 v[32:35], v[112:115], v[12:15], v[32:35]\n"
    "ds_read_b64_tr_b16 v[80:81], v96 offset:36864\n"
    "ds_read_b64_tr_b16 v[82:83], v96 offset:38912\n"
    "s_waitcnt lgkmcnt(14)\n"
    "v_mfma_f32_16x16x32_bf16 v[36:39], v[116:119], v[12:15], v[36:39]\n"
    "ds_read_b64_tr_b16 v[84:85], v97 offset:36864\n"
    "ds_read_b64_tr_b16 v[86:87], v97 offset:38912\n"
    "s_waitcnt lgkmcnt(14)\n"
    "v_mfma_f32_16x16x32_bf16 v[40:43], v[120:123], v[12:15], v[40:43]\n"
    "ds_read_b64_tr_b16 v[88:89], v98 offset:36864\n"
    "ds_read_b64_tr_b16 v[90:91], v98 offset:38912\n"
    "s_waitcnt lgkmcnt(14)\n"
    "v_mfma_f32_16x16x32_bf16 v[44:47], v[124:127], v[12:15], v[44:47]\n"
    "ds_read_b64_tr_b16 v[92:93], v99 offset:36864\n"
    "ds_read_b64_tr_b16 v[94:95], v99 offset:38912\n"
    "s_waitcnt vmcnt(8)\n"
    "s_barrier\n"
    "s_add_u32 s89, s41, 0x1800\n"
    "buffer_load_dwordx4 v[8:11], v100, s[52:55], s89 offen sc1\n"
    "s_add_u32 s89, s41, 0x1c00\n"
    "buffer_load_dwordx4 v[12:15], v100, s[52:55], s89 offen sc1\n"
    "s_waitcnt lgkmcnt(14)\n"
    "v_mfma_f32_16x16x32_bf16 v[32:35], v[64:67], v[16:19], v[32:35]\n"
    "ds_read_b64_tr_b16 v[48:49], v96 offset:49152\n"
    "ds_read_b64_tr_b16 v[50:51], v96 offset:51200\n"
    "s_waitcnt lgkmcnt(14)\n"
    "v_mfma_f32_16x16x32_bf16 v[36:39], v[68:71], v[16:19], v[36:39]\n"
    "ds_read_b64_tr_b16 v[52:53], v97 offset:49152\n"
    "ds_read_b64_tr_b16 v[54:55], v97 offset:51200\n"
    "s_add_u32 m0, s93, 0x18000\n"
    "s_mul_i32 s89, s70, 0x90000\n"
    "s_add_u32 s89, s89, 0\n"
    "buffer_load_dwordx4 v229, s[60:63], s89 offen lds\n"
    "s_waitcnt lgkmcnt(14)\n"
    "v_mfma_f32_16x16x32_bf16 v[40:43], v[72:75], v[16:19], v[40:43]\n"
    "ds_read_b64_tr_b16 v[56:57], v98 offset:49152\n"
    "ds_read_b64_tr_b16 v[58:59], v98 offset:51200\n"
    "s_waitcnt lgkmcnt(14)\n"
    "v_mfma_f32_16x16x32_bf16 v[44:47], v[76:79], v[16:19], v[44:47]\n"
    "ds_read_b64_tr_b16 v[60:61], v99 offset:49152\n"
    "ds_read_b64_tr_b16 v[62:63], v99 offset:51200\n"
    "s_add_u32 m0, s93, 0x19000\n"
    "s_mul_i32 s89, s70, 0x90000\n"
    "s_add_u32 s89, s89, 0x8000\n"
    "buffer_load_dwordx4 v229, s[60:63], s89 offen lds\n"
    "s_waitcnt lgkmcnt(14)\n"
    "v_mfma_f32_16x16x32_bf16 v[32:35], v[80:83], v[20:23], v[32:35]\n"
    "ds_read_b64_tr_b16 v[112:113], v96 offset:53248\n"
    "ds_read_b64_tr_b16 v[114:115], v96 offset:55296\n"
    "s_waitcnt lgkmcnt(14)\n"
    "v_mfma_f32_16x16x32_bf16 v[36:39], v[84:87], v[20:23], v[36:39]\n"
    "ds_read_b64_tr_b16 v[116:117], v97 offset:53248\n"
    "ds_read_b64_tr_b16 v[118:119], v97 offset:55296\n"
    "s_waitcnt lgkmcnt(14)\n"
    "v_mfma_f32_16x16x32_bf16 v[40:43], v[88:91], v[20:23], v[40:43]\n"
    "ds_read_b64_tr_b16 v[120:121], v98 offset:53248\n"
    "ds_read_b64_tr_b16 v[122:123], v98 offset:55296\n"
    "s_waitcnt lgkmcnt(14)\n"
    "v_mfma_f32_16x16x32_bf16 v[44:47], v[92:95], v[20:23], v[44:47]\n"
    "ds_read_b64_tr_b16 v[124:125], v99 offset:53248\n"
    "ds_read_b64_tr_b16 v[126:127], v99 offset:55296\n"
    "s_cmp_eq_u32 s68, 0\n"
    "s_cbranch_scc0 LA_skip_33\n"
    "s_add_u32 s88, s90, 0x100\n"
    "s_mov_b32 s96, 0xffffffff\n"
    "s_mov_b32 s97, 0xffffffff\n"
    "s_waitcnt vmcnt(6)\n"
    "v_cmp_eq_u64 vcc, s[78:79], v[168:169]\n"
    "s_nop 4\n"
    "s_and_b64 vcc, vcc, s[96:97]\n"
    "s_cmp_eq_u64 vcc, s[96:97]\n"
    "s_cbranch_scc0 LA_slowL_30\n"
    "LA_backL_31:\n"
    "s_cmp_eq_u32 s72, 0\n"
    "s_cbranch_scc0 LA_eupd_28\n"
    "v_mov_b32 v226, s78\n"
    "v_mov_b32 v227, s79\n"
    "s_mov_b64 exec, 1\n"
    "buffer_store_dwordx2 v[226:227], off, s[52:55], s90 sc1\n"
    "s_mov_b64 exec, -1\n"
    "LA_eupd_28:\n"
    "LA_skip_33:\n"
    "s_waitcnt vmcnt(8)\n"
    "s_barrier\n"
    "s_add_u32 s89, s42, 0xa000\n"
    "buffer_load_dwordx4 v[16:19], v100, s[52:55], s89 offen sc1\n"
    "s_add_u32 s89, s42, 0xa400\n"
    "buffer_load_dwordx4 v[20:23], v100, s[52:55], s89 offen sc1\n"
    "s_waitcnt lgkmcnt(14)\n"
    "v_mfma_f32_16x16x32_bf16 v[32:35], v[48:51], v[24:27], v[32:35]\n"
    "v_xor_b32 v96, 0x10000, v96\n"
    "v_xor_b32 v97, 0x10000, v97\n"
    "v_xor_b32 v98, 0x10000, v98\n"
    "v_xor_b32 v99, 0x10000, v99\n"
    "ds_read_b64_tr_b16 v[64:65], v96 offset:0\n"
    "ds_read_b64_tr_b16 v[66:67], v96 offset:2048\n"
    "s_waitcnt lgkmcnt(14)\n"
    "v_mfma_f32_16x16x32_bf16 v[36:39], v[52:55], v[24:27], v[36:39]\n"
    "ds_read_b64_tr_b16 v[68:69], v97 offset:0\n"
    "ds_read_b64_tr_b16 v[70:71], v97 offset:2048\n"
    "s_add_u32 m0, s93, 0x1c000\n"
    "s_mul_i32 s89, s70, 0xb0000\n"
    "s_add_u32 s89, s89, 0x10000\n"
    "buffer_load_dwordx4 v229, s[60:63], s89 offen lds\n"
    "s_waitcnt lgkmcnt(14)\n"
    "v_mfma_f32_16x16x32_bf16 v[40:43], v[56:59], v[24:27], v[40:43]\n"
    "ds_read_b64_tr_b16 v[72:73], v98 offset:0\n"
    "ds_read_b64_tr_b16 v[74:75], v98 offset:2048\n"
    "s_waitcnt lgkmcnt(14)\n"
    "v_mfma_f32_16x16x32_bf16 v[44:47], v[60:63], v[24:27], v[44:47]\n"
    "ds_read_b64_tr_b16 v[76:77], v99 offset:0\n"
    "ds_read_b64_tr_b16 v[78:79], v99 offset:2048\n"
    "s_add_u32 m0, s93, 0x1d000\n"
    "s_mul_i32 s89, s70, 0xb0000\n"
    "s_add_u32 s89, s89, 0x18000\n"
    "buffer_load_dwordx4 v229, s[60:63], s89 offen lds\n"
    "s_waitcnt lgkmcnt(14)\n"
    "v_mfma_f32_16x16x32_bf16 v[32:35], v[112:115], v[28:31], v[32:35]\n"
    "ds_read_b64_tr_b16 v[80:81], v96 offset:4096\n"
    "ds_read_b64_tr_b16 v[82:83], v96 offset:6144\n"
    "s_waitcnt lgkmcnt(14)\n"
    "v_mfma_f32_16x16x32_bf16 v[36:39], v[116:119], v[28:31], v[36:39]\n"
    "ds_read_b64_tr_b16 v[84:85], v97 offset:4096\n"
    "ds_read_b64_tr_b16 v[86:87], v97 offset:6144\n"
    "s_waitcnt lgkmcnt(14)\n"
    "v_mfma_f32_16x16x32_bf16 v[40:43], v[120:123], v[28:31], v[40:43]\n"
    "ds_read_b64_tr_b16 v[88:89], v98 offset:4096\n"
    "ds_read_b64_tr_b16 v[90:91], v98 offset:6144\n"
    "s_waitcnt lgkmcnt(14)\n"
    "v_mfma_f32_16x16x32_bf16 v[44:47], v[124:127], v[28:31], v[44:47]\n"
    "ds_read_b64_tr_b16 v[92:93], v99 offset:4096\n"
    "ds_read_b64_tr_b16 v[94:95], v99 offset:6144\n"
    "s_waitcnt vmcnt(8)\n"
    "s_barrier\n"
    "s_add_u32 s89, s42, 0xa800\n"
    "buffer_load_dwordx4 v[24:27], v100, s[52:55], s89 offen sc1\n"
    "s_add_u32 s89, s42, 0xac00\n"
    "buffer_load_dwordx4 v[28:31], v100, s[52:55], s89 offen sc1\n"
    "s_waitcnt lgkmcnt(14)\n"
    "v_mfma_f32_16x16x32_bf16 v[32:35], v[64:67], v[0:3], v[32:35]\n"
    "ds_read_b64_tr_b16 v[48:49], v96 offset:16384\n"
    "ds_read_b64_tr_b16 v[50:51], v96 offset:18432\n"
    "s_waitcnt lgkmcnt(14)\n"
    "v_mfma_f32_16x16x32_bf16 v[36:39], v[68:71], v[0:3], v[36:39]\n"
    "ds_read_b64_tr_b16 v[52:53], v97 offset:16384\n"
    "ds_read_b64_tr_b16 v[54:55], v97 offset:18432\n"
    "s_add_u32 m0, s93, 0\n"
    "s_mul_i32 s89, s70, 0xc0000\n"
    "s_add_u32 s89, s89, 0x20000\n"
    "buffer_load_dwordx4 v229, s[60:63], s89 offen lds\n"
    "s_waitcnt lgkmcnt(14)\n"
    "v_mfma_f32_16x16x32_bf16 v[40:43], v[72:75], v[0:3], v[40:43]\n"
    "ds_read_b64_tr_b16 v[56:57], v98 offset:16384\n"
    "ds_read_b64_tr_b16 v[58:59], v98 offset:18432\n"
    "s_waitcnt lgkmcnt(14)\n"
    "v_mfma_f32_16x16x32_bf16 v[44:47], v[76:79], v[0:3], v[44:47]\n"
    "ds_read_b64_tr_b16 v[60:61], v99 offset:16384\n"
    "ds_read_b64_tr_b16 v[62:63], v99 offset:18432\n"
    "s_add_u32 m0, s93, 0x1000\n"
    "s_mul_i32 s89, s70, 0xc0000\n"
    "s_add_u32 s89, s89, 0x28000\n"
    "buffer_load_dwordx4 v229, s[60:63], s89 offen lds\n"
    "s_waitcnt lgkmcnt(14)\n"
    "v_mfma_f32_16x16x32_bf16 v[32:35], v[80:83], v[4:7], v[32:35]\n"
    "ds_read_b64_tr_b16 v[112:113], v96 offset:20480\n"
    "ds_read_b64_tr_b16 v[114:115], v96 offset:22528\n"
    "s_waitcnt lgkmcnt(14)\n"
    "v_mfma_f32_16x16x32_bf16 v[36:39], v[84:87], v[4:7], v[36:39]\n"
    "ds_read_b64_tr_b16 v[116:117], v97 offset:20480\n"
    "ds_read_b64_tr_b16 v[118:119], v97 offset:22528\n"
    "s_waitcnt lgkmcnt(14)\n"
    "v_mfma_f32_16x16x32_bf16 v[40:43], v[88:91], v[4:7], v[40:43]\n"
    "ds_read_b64_tr_b16 v[120:121], v98 offset:20480\n"
    "ds_read_b64_tr_b16 v[122:123], v98 offset:22528\n"
    "s_waitcnt lgkmcnt(14)\n"
    "v_mfma_f32_16x16x32_bf16 v[44:47], v[92:95], v[4:7], v[44:47]\n"
    "ds_read_b64_tr_b16 v[124:125], v99 offset:20480\n"
    "ds_read_b64_tr_b16 v[126:127], v99 offset:22528\n"
    "s_waitcnt vmcnt(8)\n"
    "s_barrier\n"
    "s_add_u32 s89, s42, 0xb000\n"
    "buffer_load_dwordx4 v[0:3], v100, s[52:55], s89 offen sc1\n"
    "s_add_u32 s89, s42, 0xb400\n"
    "buffer_load_dwordx4 v[4:7], v100, s[52:55], s89 offen sc1\n"
    "s_waitcnt lgkmcnt(14)\n"
    "v_mfma_f32_16x16x32_bf16 v[32:35], v[48:51], v[8:11], v[32:35]\n"
    "ds_read_b64_tr_b16 v[64:65], v96 offset:32768\n"
    "ds_read_b64_tr_b16 v[66:67], v96 offset:34816\n"
    "s_waitcnt lgkmcnt(14)\n"
    "v_mfma_f32_16x16x32_bf16 v[36:39], v[52:55], v[8:11], v[36:39]\n"
    "ds_read_b64_tr_b16 v[68:69], v97 offset:32768\n"
    "ds_read_b64_tr_b16 v[70:71], v97 offset:34816\n"
    "s_add_u32 m0, s93, 0x4000\n"
    "s_mul_i32 s89, s70, 0xe0000\n"
    "s_add_u32 s89, s89, 0x30000\n"
    "buffer_load_dwordx4 v229, s[60:63], s89 offen lds\n"
    "s_waitcnt lgkmcnt(14)\n"
    "v_mfma_f32_16x16x32_bf16 v[40:43], v[56:59], v[8:11], v[40:43]\n"
    "ds_read_b64_tr_b16 v[72:73], v98 offset:32768\n"
    "ds_read_b64_tr_b16 v[74:75], v98 offset:34816\n"
    "s_waitcnt lgkmcnt(14)\n"
    "v_mfma_f32_16x16x32_bf16 v[44:47], v[60:63], v[8:11], v[44:47]\n"
    "ds_read_b64_tr_b16 v[76:77], v99 offset:32768\n"
    "ds_read_b64_tr_b16 v[78:79], v99 offset:34816\n"
    "s_add_u32 m0, s93, 0x5000\n"
    "s_mul_i32 s89, s70, 0xe0000\n"
    "s_add_u32 s89, s89, 0x38000\n"
    "buffer_load_dwordx4 v229, s[60:63], s89 offen lds\n"
    "s_waitcnt lgkmcnt(14)\n"
    "v_mfma_f32_16x16x32_bf16 v[32:35], v[112:115], v[12:15], v[32:35]\n"
    "ds_read_b64_tr_b16 v[80:81], v96 offset:36864\n"
    "ds_read_b64_tr_b16 v[82:83], v96 offset:38912\n"
    "s_waitcnt lgkmcnt(14)\n"
    "v_mfma_f32_16x16x32_bf16 v[36:39], v[116:119], v[12:15], v[36:39]\n"
    "ds_read_b64_tr_b16 v[84:85], v97 offset:36864\n"
    "ds_read_b64_tr_b16 v[86:87], v97 offset:38912\n"
    "s_waitcnt lgkmcnt(14)\n"
    "v_mfma_f32_16x16x32_bf16 v[40:43], v[120:123], v[12:15], v[40:43]\n"
    "ds_read_b64_tr_b16 v[88:89], v98 offset:36864\n"
    "ds_read_b64_tr_b16 v[90:91], v98 offset:38912\n"
    "s_waitcnt lgkmcnt(14)\n"
    "v_mfma_f32_16x16x32_bf16 v[44:47], v[124:127], v[12:15], v[44:47]\n"
    "ds_read_b64_tr_b16 v[92:93], v99 offset:36864\n"
    "ds_read_b64_tr_b16 v[94:95], v99 offset:38912\n"
    "s_waitcnt vmcnt(8)\n"
    "s_barrier\n"
    "s_add_u32 s89, s42, 0xb800\n"
    "buffer_load_dwordx4 v[8:11], v100, s[52:55], s89 offen sc1\n"
    "s_add_u32 s89, s42, 0xbc00\n"
    "buffer_load_dwordx4 v[12:15], v100, s[52:55], s89 offen sc1\n"
    "s_waitcnt lgkmcnt(14)\n"
    "v_mfma_f32_16x16x32_bf16 v[32:35], v[64:67], v[16:19], v[32:35]\n"
    "ds_read_b64_tr_b16 v[48:49], v96 offset:49152\n"
    "ds_read_b64_tr_b16 v[50:51], v96 offset:51200\n"
    "s_waitcnt lgkmcnt(14)\n"
    "v_mfma_f32_16x16x32_bf16 v[36:39], v[68:71], v[16:19], v[36:39]\n"
    "ds_read_b64_tr_b16 v[52:53], v97 offset:49152\n"
    "ds_read_b64_tr_b16 v[54:55], v97 offset:51200\n"
    "s_add_u32 m0, s93, 0x8000\n"
    "s_mul_i32 s89, s70, 0xf0000\n"
    "s_add_u32 s89, s89, 0x40000\n"
    "buffer_load_dwordx4 v229, s[60:63], s89 offen lds\n"
    "s_waitcnt lgkmcnt(14)\n"
    "v_mfma_f32_16x16x32_bf16 v[40:43], v[72:75], v[16:19], v[40:43]\n"
    "ds_read_b64_tr_b16 v[56:57], v98 offset:49152\n"
    "ds_read_b64_tr_b16 v[58:59], v98 offset:51200\n"
    "s_waitcnt lgkmcnt(14)\n"
    "v_mfma_f32_16x16x32_bf16 v[44:47], v[76:79], v[16:19], v[44:47]\n"
    "ds_read_b64_tr_b16 v[60:61], v99 offset:49152\n"
    "ds_read_b64_tr_b16 v[62:63], v99 offset:51200\n"
    "s_add_u32 m0, s93, 0x9000\n"
    "s_mul_i32 s89, s70, 0xf0000\n"
    "s_add_u32 s89, s89, 0x48000\n"
    "buffer_load_dwordx4 v229, s[60:63], s89 offen lds\n"
    "s_waitcnt lgkmcnt(14)\n"
    "v_mfma_f32_16x16x32_bf16 v[32:35], v[80:83], v[20:23], v[32:35]\n"
    "ds_read_b64_tr_b16 v[112:113], v96 offset:53248\n"
    "ds_read_b64_tr_b16 v[114:115], v96 offset:55296\n"
    "s_waitcnt lgkmcnt(14)\n"
    "v_mfma_f32_16x16x32_bf16 v[36:39], v[84:87], v[20:23], v[36:39]\n"
    "ds_read_b64_tr_b16 v[116:117], v97 offset:53248\n"
    "ds_read_b64_tr_b16 v[118:119], v97 offset:55296\n"
    "s_waitcnt lgkmcnt(14)\n"
    "v_mfma_f32_16x16x32_bf16 v[40:43], v[88:91], v[20:23], v[40:43]\n"
    "ds_read_b64_tr_b16 v[120:121], v98 offset:53248\n"
    "ds_read_b64_tr_b16 v[122:123], v98 offset:55296\n"
    "s_waitcnt lgkmcnt(14)\n"
    "v_mfma_f32_16x16x32_bf16 v[44:47], v[92:95], v[20:23], v[44:47]\n"
    "ds_read_b64_tr_b16 v[124:125], v99 offset:53248\n"
    "ds_read_b64_tr_b16 v[126:127], v99 offset:55296\n"
    "s_waitcnt vmcnt(8)\n"
    "s_barrier\n"
    "s_add_u32 s89, s42, 0xc000\n"
    "buffer_load_dwordx4 v[16:19], v100, s[52:55], s89 offen sc1\n"
    "s_add_u32 s89, s42, 0xc400\n"
    "buffer_load_dwordx4 v[20:23], v100, s[52:55], s89 offen sc1\n"
    "s_waitcnt lgkmcnt(14)\n"
    "v_mfma_f32_16x16x32_bf16 v[32:35], v[48:51], v[24:27], v[32:35]\n"
    "v_xor_b32 v96, 0x10000, v96\n"
    "v_xor_b32 v97, 0x10000, v97\n"
    "v_xor_b32 v98, 0x10000, v98\n"
    "v_xor_b32 v99, 0x10000, v99\n"
    "ds_read_b64_tr_b16 v[64:65], v96 offset:0\n"
    "ds_read_b64_tr_b16 v[66:67], v96 offset:2048\n"
    "s_waitcnt lgkmcnt(14)\n"
    "v_mfma_f32_16x16x32_bf16 v[36:39], v[52:55], v[24:27], v[36:39]\n"
    "ds_read_b64_tr_b16 v[68:69], v97 offset:0\n"
    "ds_read_b64_tr_b16 v[70:71], v97 offset:2048\n"
    "s_add_u32 m0, s93, 0xc000\n"
    "s_mul_i32 s89, s70, 0x110000\n"
    "s_add_u32 s89, s89, 0x50000\n"
    "buffer_load_dwordx4 v229, s[60:63], s89 offen lds\n"
    "s_waitcnt lgkmcnt(14)\n"
    "v_mfma_f32_16x16x32_bf16 v[40:43], v[56:59], v[24:27], v[40:43]\n"
    "ds_read_b64_tr_b16 v[72:73], v98 offset:0\n"
    "ds_read_b64_tr_b16 v[74:75], v98 offset:2048\n"
    "s_waitcnt lgkmcnt(14)\n"
    "v_mfma_f32_16x16x32_bf16 v[44:47], v[60:63], v[24:27], v[44:47]\n"
    "ds_read_b64_tr_b16 v[76:77], v99 offset:0\n"
    "ds_read_b64_tr_b16 v[78:79], v99 offset:2048\n"
    "s_add_u32 m0, s93, 0xd000\n"
    "s_mul_i32 s89, s70, 0x110000\n"
    "s_add_u32 s89, s89, 0x58000\n"
    "buffer_load_dwordx4 v229, s[60:63], s89 offen lds\n"
    "s_waitcnt lgkmcnt(14)\n"
    "v_mfma_f32_16x16x32_bf16 v[32:35], v[112:115], v[28:31], v[32:35]\n"
    "ds_read_b64_tr_b16 v[80:81], v96 offset:4096\n"
    "ds_read_b64_tr_b16 v[82:83], v96 offset:6144\n"
    "s_waitcnt lgkmcnt(14)\n"
    "v_mfma_f32_16x16x32_bf16 v[36:39], v[116:119], v[28:31], v[36:39]\n"
    "ds_read_b64_tr_b16 v[84:85], v97 offset:4096\n"
    "ds_read_b64_tr_b16 v[86:87], v97 offset:6144\n"
    "s_waitcnt lgkmcnt(14)\n"
    "v_mfma_f32_16x16x32_bf16 v[40:43], v[120:123], v[28:31], v[40:43]\n"
    "ds_read_b64_tr_b16 v[88:89], v98 offset:4096\n"
    "ds_read_b64_tr_b16 v[90:91], v98 offset:6144\n"
    "s_waitcnt lgkmcnt(14)\n"
    "v_mfma_f32_16x16x32_bf16 v[44:47], v[124:127], v[28:31], v[44:47]\n"
    "ds_read_b64_tr_b16 v[92:93], v99 offset:4096\n"
    "ds_read_b64_tr_b16 v[94:95], v99 offset:6144\n"
    "s_waitcnt vmcnt(8)\n"
    "s_barrier\n"
    "s_add_u32 s89, s42, 0xc800\n"
    "buffer_load_dwordx4 v[24:27], v100, s[52:55], s89 offen sc1\n"
    "s_add_u32 s89, s42, 0xcc00\n"
    "buffer_load_dwordx4 v[28:31], v100, s[52:55], s89 offen sc1\n"
    "s_waitcnt lgkmcnt(14)\n"
    "v_mfma_f32_16x16x32_bf16 v[32:35], v[64:67], v[0:3], v[32:35]\n"
    "ds_read_b64_tr_b16 v[48:49], v96 offset:16384\n"
    "ds_read_b64_tr_b16 v[50:51], v96 offset:18432\n"
    "s_waitcnt lgkmcnt(14)\n"
    "v_mfma_f32_16x16x32_bf16 v[36:39], v[68:71], v[0:3], v[36:39]\n"
    "ds_read_b64_tr_b16 v[52:53], v97 offset:16384\n"
    "ds_read_b64_tr_b16 v[54:55], v97 offset:18432\n"
    "s_add_u32 m0, s93, 0x10000\n"
    "s_mul_i32 s89, s70, 0x120000\n"
    "s_add_u32 s89, s89, 0x60000\n"
    "buffer_load_dwordx4 v229, s[60:63], s89 offen lds\n"
    "s_waitcnt lgkmcnt(14)\n"
    "v_mfma_f32_16x16x32_bf16 v[40:43], v[72:75], v[0:3], v[40:43]\n"
    "ds_read_b64_tr_b16 v[56:57], v98 offset:16384\n"
    "ds_read_b64_tr_b16 v[58:59], v98 offset:18432\n"
    "s_waitcnt lgkmcnt(14)\n"
    "v_mfma_f32_16x16x32_bf16 v[44:47], v[76:79], v[0:3], v[44:47]\n"
    "ds_read_b64_tr_b16 v[60:61], v99 offset:16384\n"
    "ds_read_b64_tr_b16 v[62:63], v99 offset:18432\n"
    "s_add_u32 m0, s93, 0x11000\n"
    "s_mul_i32 s89, s70, 0x120000\n"
    "s_add_u32 s89, s89, 0x68000\n"
    "buffer_load_dwordx4 v229, s[60:63], s89 offen lds\n"
    "s_waitcnt lgkmcnt(14)\n"
    "v_mfma_f32_16x16x32_bf16 v[32:35], v[80:83], v[4:7], v[32:35]\n"
    "ds_read_b64_tr_b16 v[112:113], v96 offset:20480\n"
    "ds_read_b64_tr_b16 v[114:115], v96 offset:22528\n"
    "s_waitcnt lgkmcnt(14)\n"
    "v_mfma_f32_16x16x32_bf16 v[36:39], v[84:87], v[4:7], v[36:39]\n"
    "ds_read_b64_tr_b16 v[116:117], v97 offset:20480\n"
    "ds_read_b64_tr_b16 v[118:119], v97 offset:22528\n"
    "s_waitcnt lgkmcnt(14)\n"
    "v_mfma_f32_16x16x32_bf16 v[40:43], v[88:91], v[4:7], v[40:43]\n"
    "ds_read_b64_tr_b16 v[120:121], v98 offset:20480\n"
    "ds_read_b64_tr_b16 v[122:123], v98 offset:22528\n"
    "s_waitcnt lgkmcnt(14)\n"
    "v_mfma_f32_16x16x32_bf16 v[44:47], v[92:95], v[4:7], v[44:47]\n"
    "ds_read_b64_tr_b16 v[124:125], v99 offset:20480\n"
    "ds_read_b64_tr_b16 v[126:127], v99 offset:22528\n"
    "s_waitcnt vmcnt(8)\n"
    "s_barrier\n"
    "s_add_u32 s89, s42, 0xd000\n"
    "buffer_load_dwordx4 v[0:3], v100, s[52:55], s89 offen sc1\n"
    "s_add_u32 s89, s42, 0xd400\n"
    "buffer_load_dwordx4 v[4:7], v100, s[52:55], s89 offen sc1\n"
    "s_waitcnt lgkmcnt(14)\n"
    "v_mfma_f32_16x16x32_bf16 v[32:35], v[48:51], v[8:11], v[32:35]\n"
    "ds_read_b64_tr_b16 v[64:65], v96 offset:32768\n"
    "ds_read_b64_tr_b16 v[66:67], v96 offset:34816\n"
    "s_waitcnt lgkmcnt(14)\n"
    "v_mfma_f32_16x16x32_bf16 v[36:39], v[52:55], v[8:11], v[36:39]\n"
    "ds_read_b64_tr_b16 v[68:69], v97 offset:32768\n"
    "ds_read_b64_tr_b16 v[70:71], v97 offset:34816\n"
    "s_add_u32 m0, s93, 0x14000\n"
    "s_mul_i32 s89, s70, 0x140000\n"
    "s_add_u32 s89, s89, 0x70000\n"
    "buffer_load_dwordx4 v229, s[60:63], s89 offen lds\n"
    "s_waitcnt lgkmcnt(14)\n"
    "v_mfma_f32_16x16x32_bf16 v[40:43], v[56:59], v[8:11], v[40:43]\n"
    "ds_read_b64_tr_b16 v[72:73], v98 offset:32768\n"
    "ds_read_b64_tr_b16 v[74:75], v98 offset:34816\n"
    "s_waitcnt lgkmcnt(14)\n"
    "v_mfma_f32_16x16x32_bf16 v[44:47], v[60:63], v[8:11], v[44:47]\n"
    "ds_read_b64_tr_b16 v[76:77], v99 offset:32768\n"
    "ds_read_b64_tr_b16 v[78:79], v99 offset:34816\n"
    "s_add_u32 m0, s93, 0x15000\n"
    "s_mul_i32 s89, s70, 0x140000\n"
    "s_add_u32 s89, s89, 0x78000\n"
    "buffer_load_dwordx4 v229, s[60:63], s89 offen lds\n"
    "s_waitcnt lgkmcnt(14)\n"
    "v_mfma_f32_16x16x32_bf16 v[32:35], v[112:115], v[12:15], v[32:35]\n"
    "ds_read_b64_tr_b16 v[80:81], v96 offset:36864\n"
    "ds_read_b64_tr_b16 v[82:83], v96 offset:38912\n"
    "s_waitcnt lgkmcnt(14)\n"
    "v_mfma_f32_16x16x32_bf16 v[36:39], v[116:119], v[12:15], v[36:39]\n"
    "ds_read_b64_tr_b16 v[84:85], v97 offset:36864\n"
    "ds_read_b64_tr_b16 v[86:87], v97 offset:38912\n"
    "s_waitcnt lgkmcnt(14)\n"
    "v_mfma_f32_16x16x32_bf16 v[40:43], v[120:123], v[12:15], v[40:43]\n"
    "ds_read_b64_tr_b16 v[88:89], v98 offset:36864\n"
    "ds_read_b64_tr_b16 v[90:91], v98 offset:38912\n"
    "s_waitcnt lgkmcnt(14)\n"
    "v_mfma_f32_16x16x32_bf16 v[44:47], v[124:127], v[12:15], v[44:47]\n"
    "ds_read_b64_tr_b16 v[92:93], v99 offset:36864\n"
    "ds_read_b64_tr_b16 v[94:95], v99 offset:38912\n"
    "s_waitcnt vmcnt(8)\n"
    "s_barrier\n"
    "s_add_u32 s89, s42, 0xd800\n"
    "buffer_load_dwordx4 v[8:11], v100, s[52:55], s89 offen sc1\n"
    "s_add_u32 s89, s42, 0xdc00\n"
    "buffer_load_dwordx4 v[12:15], v100, s[52:55], s89 offen sc1\n"
    "s_waitcnt lgkmcnt(14)\n"
    "v_mfma_f32_16x16x32_bf16 v[32:35], v[64:67], v[16:19], v[32:35]\n"
    "ds_read_b64_tr_b16 v[48:49], v96 offset:49152\n"
    "ds_read_b64_tr_b16 v[50:51], v96 offset:51200\n"
    "s_waitcnt lgkmcnt(14)\n"
    "v_mfma_f32_16x16x32_bf16 v[36:39], v[68:71], v[16:19], v[36:39]\n"
    "ds_read_b64_tr_b16 v[52:53], v97 offset:49152\n"
    "ds_read_b64_tr_b16 v[54:55], v97 offset:51200\n"
    "s_waitcnt lgkmcnt(14)\n"
    "v_mfma_f32_16x16x32_bf16 v[40:43], v[72:75], v[16:19], v[40:43]\n"
    "ds_read_b64_tr_b16 v[56:57], v98 offset:49152\n"
    "ds_read_b64_tr_b16 v[58:59], v98 offset:51200\n"
    "s_waitcnt lgkmcnt(14)\n"
    "v_mfma_f32_16x16x32_bf16 v[44:47], v[76:79], v[16:19], v[44:47]\n"
    "ds_read_b64_tr_b16 v[60:61], v99 offset:49152\n"
    "ds_read_b64_tr_b16 v[62:63], v99 offset:51200\n"
    "s_waitcnt lgkmcnt(14)\n"
    "v_mfma_f32_16x16x32_bf16 v[32:35], v[80:83], v[20:23], v[32:35]\n"
    "ds_read_b64_tr_b16 v[112:113], v96 offset:53248\n"
    "ds_read_b64_tr_b16 v[114:115], v96 offset:55296\n"
    "s_waitcnt lgkmcnt(14)\n"
    "v_mfma_f32_16x16x32_bf16 v[36:39], v[84:87], v[20:23], v[36:39]\n"
    "ds_read_b64_tr_b16 v[116:117], v97 offset:53248\n"
    "ds_read_b64_tr_b16 v[118:119], v97 offset:55296\n"
    "s_waitcnt lgkmcnt(14)\n"
    "v_mfma_f32_16x16x32_bf16 v[40:43], v[88:91], v[20:23], v[40:43]\n"
    "ds_read_b64_tr_b16 v[120:121], v98 offset:53248\n"
    "ds_read_b64_tr_b16 v[122:123], v98 offset:55296\n"
    "s_waitcnt lgkmcnt(14)\n"
    "v_mfma_f32_16x16x32_bf16 v[44:47], v[92:95], v[20:23], v[44:47]\n"
    "ds_read_b64_tr_b16 v[124:125], v99 offset:53248\n"
    "ds_read_b64_tr_b16 v[126:127], v99 offset:55296\n"
    "s_waitcnt vmcnt(6)\n"
    "s_barrier\n"
    "s_waitcnt lgkmcnt(14)\n"
    "v_mfma_f32_16x16x32_bf16 v[32:35], v[48:51], v[24:27], v[32:35]\n"
    "v_xor_b32 v96, 0x10000, v96\n"
    "v_xor_b32 v97, 0x10000, v97\n"
    "v_xor_b32 v98, 0x10000, v98\n"
    "v_xor_b32 v99, 0x10000, v99\n"
    "ds_read_b64_tr_b16 v[64:65], v96 offset:0\n"
    "ds_read_b64_tr_b16 v[66:67], v96 offset:2048\n"
    "s_waitcnt lgkmcnt(14)\n"
    "v_mfma_f32_16x16x32_bf16 v[36:39], v[52:55], v[24:27], v[36:39]\n"
    "ds_read_b64_tr_b16 v[68:69], v97 offset:0\n"
    "ds_read_b64_tr_b16 v[70:71], v97 offset:2048\n"
    "s_waitcnt lgkmcnt(14)\n"
    "v_mfma_f32_16x16x32_bf16 v[40:43], v[56:59], v[24:27], v[40:43]\n"
    "ds_read_b64_tr_b16 v[72:73], v98 offset:0\n"
    "ds_read_b64_tr_b16 v[74:75], v98 offset:2048\n"
    "s_waitcnt lgkmcnt(14)\n"
    "v_mfma_f32_16x16x32_bf16 v[44:47], v[60:63], v[24:27], v[44:47]\n"
    "ds_read_b64_tr_b16 v[76:77], v99 offset:0\n"
    "ds_read_b64_tr_b16 v[78:79], v99 offset:2048\n"
    "s_waitcnt lgkmcnt(14)\n"
    "v_mfma_f32_16x16x32_bf16 v[32:35], v[112:115], v[28:31], v[32:35]\n"
    "ds_read_b64_tr_b16 v[80:81], v96 offset:4096\n"
    "ds_read_b64_tr_b16 v[82:83], v96 offset:6144\n"
    "s_waitcnt lgkmcnt(14)\n"
    "v_mfma_f32_16x16x32_bf16 v[36:39], v[116:119], v[28:31], v[36:39]\n"
    "ds_read_b64_tr_b16 v[84:85], v97 offset:4096\n"
    "ds_read_b64_tr_b16 v[86:87], v97 offset:6144\n"
    "s_waitcnt lgkmcnt(14)\n"
    "v_mfma_f32_16x16x32_bf16 v[40:43], v[120:123], v[28:31], v[40:43]\n"
    "ds_read_b64_tr_b16 v[88:89], v98 offset:4096\n"
    "ds_read_b64_tr_b16 v[90:91], v98 offset:6144\n"
    "s_waitcnt lgkmcnt(14)\n"
    "v_mfma_f32_16x16x32_bf16 v[44:47], v[124:127], v[28:31], v[44:47]\n"
    "ds_read_b64_tr_b16 v[92:93], v99 offset:4096\n"
    "ds_read_b64_tr_b16 v[94:95], v99 offset:6144\n"
    "s_waitcnt vmcnt(2)\n"
    "s_barrier\n"
    "s_waitcnt lgkmcnt(14)\n"
    "v_mfma_f32_16x16x32_bf16 v[32:35], v[64:67], v[0:3], v[32:35]\n"
    "ds_read_b64_tr_b16 v[48:49], v96 offset:16384\n"
    "ds_read_b64_tr_b16 v[50:51], v96 offset:18432\n"
    "s_waitcnt lgkmcnt(14)\n"
    "v_mfma_f32_16x16x32_bf16 v[36:39], v[68:71], v[0:3], v[36:39]\n"
    "ds_read_b64_tr_b16 v[52:53], v97 offset:16384\n"
    "ds_read_b64_tr_b16 v[54:55], v97 offset:18432\n"
    "s_waitcnt lgkmcnt(14)\n"
    "v_mfma_f32_16x16x32_bf16 v[40:43], v[72:75], v[0:3], v[40:43]\n"
    "ds_read_b64_tr_b16 v[56:57], v98 offset:16384\n"
    "ds_read_b64_tr_b16 v[58:59], v98 offset:18432\n"
    "s_waitcnt lgkmcnt(14)\n"
    "v_mfma_f32_16x16x32_bf16 v[44:47], v[76:79], v[0:3], v[44:47]\n"
    "ds_read_b64_tr_b16 v[60:61], v99 offset:16384\n"
    "ds_read_b64_tr_b16 v[62:63], v99 offset:18432\n"
    "s_waitcnt lgkmcnt(14)\n"
    "v_mfma_f32_16x16x32_bf16 v[32:35], v[80:83], v[4:7], v[32:35]\n"
    "ds_read_b64_tr_b16 v[112:113], v96 offset:20480\n"
    "ds_read_b64_tr_b16 v[114:115], v96 offset:22528\n"
    "s_waitcnt lgkmcnt(14)\n"
    "v_mfma_f32_16x16x32_bf16 v[36:39], v[84:87], v[4:7], v[36:39]\n"
    "ds_read_b64_tr_b16 v[116:117], v97 offset:20480\n"
    "ds_read_b64_tr_b16 v[118:119], v97 offset:22528\n"
    "s_waitcnt lgkmcnt(14)\n"
    "v_mfma_f32_16x16x32_bf16 v[40:43], v[88:91], v[4:7], v[40:43]\n"
    "ds_read_b64_tr_b16 v[120:121], v98 offset:20480\n"
    "ds_read_b64_tr_b16 v[122:123], v98 offset:22528\n"
    "s_waitcnt lgkmcnt(14)\n"
    "v_mfma_f32_16x16x32_bf16 v[44:47], v[92:95], v[4:7], v[44:47]\n"
    "ds_read_b64_tr_b16 v[124:125], v99 offset:20480\n"
    "ds_read_b64_tr_b16 v[126:127], v99 offset:22528\n"
    "s_waitcnt vmcnt(0)\n"
    "s_barrier\n"
    "s_waitcnt lgkmcnt(14)\n"
    "v_mfma_f32_16x16x32_bf16 v[32:35], v[48:51], v[8:11], v[32:35]\n"
    "s_waitcnt lgkmcnt(12)\n"
    "v_mfma_f32_16x16x32_bf16 v[36:39], v[52:55], v[8:11], v[36:39]\n"
    "s_waitcnt lgkmcnt(10)\n"
    "v_mfma_f32_16x16x32_bf16 v[40:43], v[56:59], v[8:11], v[40:43]\n"
    "s_waitcnt lgkmcnt(8)\n"
    "v_mfma_f32_16x16x32_bf16 v[44:47], v[60:63], v[8:11], v[44:47]\n"
    "s_waitcnt lgkmcnt(6)\n"
    "v_mfma_f32_16x16x32_bf16 v[32:35], v[112:115], v[12:15], v[32:35]\n"
    "s_waitcnt lgkmcnt(4)\n"
    "v_mfma_f32_16x16x32_bf16 v[36:39], v[116:119], v[12:15], v[36:39]\n"
    "s_waitcnt lgkmcnt(2)\n"
    "v_mfma_f32_16x16x32_bf16 v[40:43], v[120:123], v[12:15], v[40:43]\n"
    "s_waitcnt lgkmcnt(0)\n"
    "v_mfma_f32_16x16x32_bf16 v[44:47], v[124:127], v[12:15], v[44:47]\n"
    "s_barrier\n"
    "v_and_b32 v108, 15, v235\n"
    "v_lshrrev_b32 v109, 4, v235\n"
    "v_and_b32 v110, 7, v108\n"
    "v_xor_b32 v109, v109, v110\n"
    "v_lshlrev_b32 v109, 4, v109\n"
    "v_lshl_add_u32 v108, v108, 8, v109\n"
    "s_lshl_b32 s86, s69, 12\n"
    "s_lshl_b32 s87, s70, 14\n"
    "s_add_u32 s86, s86, s87\n"
    "s_add_u32 s86, s86, 0x8000\n"
    "v_add_u32 v108, s86, v108\n"
    "ds_write_b128 v108, v[32:35] offset:0\n"
    "v_xor_b32 v109, 64, v108\n"
    "ds_write_b128 v109, v[36:39] offset:0\n"
    "v_xor_b32 v109, 0x80, v108\n"
    "ds_write_b128 v109, v[40:43] offset:0\n"
    "v_xor_b32 v109, 0xc0, v108\n"
    "ds_write_b128 v109, v[44:47] offset:0\n"
    "s_waitcnt lgkmcnt(0)\n"
    "s_barrier\n"
    "v_lshrrev_b32 v108, 3, v235\n"
    "v_and_b32 v109, 7, v235\n"
    "v_lshlrev_b32 v109, 1, v109\n"
    "v_xor_b32 v109, v109, v108\n"
    "v_lshlrev_b32 v109, 4, v109\n"
    "v_lshl_add_u32 v108, v108, 8, v109\n"
    "s_lshl_b32 s86, s68, 11\n"
    "s_add_u32 s86, s86, 0x8000\n"
    "v_add_u32 v108, s86, v108\n"
    "v_xor_b32 v109, 16, v108\n"
    "ds_read_b128 v[0:3], v108 offset:0\n"
    "ds_read_b128 v[4:7], v108 offset:16384\n"
    "ds_read_b128 v[8:11], v109 offset:0\n"
    "ds_read_b128 v[12:15], v109 offset:16384\n"
    "s_waitcnt lgkmcnt(2)\n"
    "v_add_f32 v0, v0, v4\n"
    "v_add_f32 v1, v1, v5\n"
    "v_add_f32 v2, v2, v6\n"
    "v_add_f32 v3, v3, v7\n"
    "s_waitcnt lgkmcnt(0)\n"
    "v_add_f32 v8, v8, v12\n"
    "v_add_f32 v9, v9, v13\n"
    "v_add_f32 v10, v10, v14\n"
    "v_add_f32 v11, v11, v15\n"
    "v_lshlrev_b32 v16, 16, v104\n"
    "v_and_b32 v17, 0xffff0000, v104\n"
    "v_add_f32 v0, v0, v16\n"
    "v_add_f32 v1, v1, v17\n"
    "v_cvt_pk_bf16_f32 v20, v0, v1\n"
    "v_lshlrev_b32 v16, 16, v105\n"
    "v_and_b32 v17, 0xffff0000, v105\n"
    "v_add_f32 v2, v2, v16\n"
    "v_add_f32 v3, v3, v17\n"
    "v_cvt_pk_bf16_f32 v21, v2, v3\n"
    "v_lshlrev_b32 v16, 16, v106\n"
    "v_and_b32 v17, 0xffff0000, v106\n"
    "v_add_f32 v8, v8, v16\n"
    "v_add_f32 v9, v9, v17\n"
    "v_cvt_pk_bf16_f32 v22, v8, v9\n"
    "v_lshlrev_b32 v16, 16, v107\n"
    "v_and_b32 v17, 0xffff0000, v107\n"
    "v_add_f32 v10, v10, v16\n"
    "v_add_f32 v11, v11, v17\n"
    "v_cvt_pk_bf16_f32 v23, v10, v11\n"
    "buffer_store_dwordx4 v[20:23], v101, s[64:67], 0 offen\n"
    "s_endpgm\n"
    "LA_slowstat_1:\n"
    "s_mov_b32 s92, 0x40000\n"
    "LA_loopstat_3:\n"
    "s_sleep 1\n"
    "buffer_load_dwordx4 v[168:171], v237, s[52:55], s88 offen sc1\n"
    "s_waitcnt vmcnt(0)\n"
    "v_cmp_eq_u32 vcc, s78, v169\n"
    "s_nop 4\n"
    "v_cmp_eq_u32 s[94:95], s79, v171\n"
    "s_nop 4\n"
    "s_and_b64 vcc, vcc, s[94:95]\n"
    "s_and_b64 vcc, vcc, exec\n"
    "s_cmp_eq_u64 vcc, exec\n"
    "s_cbranch_scc1 LA_backstat_2\n"
    "s_sub_u32 s92, s92, 1\n"
    "s_cmp_lg_u32 s92, 0\n"
    "s_cbranch_scc1 LA_loopstat_3\n"
    "s_branch LA_backstat_2\n"
    "LA_slowE1_13:\n"
    "s_mov_b32 s92, 0x40000\n"
    "LA_loopE1_15:\n"
    "s_sleep 1\n"
    "buffer_load_dwordx2 v[24:25], v26, s[52:55], s88 offen sc1\n"
    "s_waitcnt vmcnt(0)\n"
    "v_cmp_eq_u64 vcc, s[78:79], v[24:25]\n"
    "s_nop 4\n"
    "s_and_b64 vcc, vcc, s[96:97]\n"
    "s_cmp_eq_u64 vcc, s[96:97]\n"
    "s_cbranch_scc1 LA_backE1_14\n"
    "s_sub_u32 s92, s92, 1\n"
    "s_cmp_lg_u32 s92, 0\n"
    "s_cbranch_scc1 LA_loopE1_15\n"
    "s_branch LA_backE1_14\n"
    "LA_slowE2_19:\n"
    "s_mov_b32 s92, 0x40000\n"
    "LA_loopE2_21:\n"
    "s_sleep 1\n"
    "buffer_load_dwordx2 v[164:165], v166, s[52:55], s88 offen sc1\n"
    "s_waitcnt vmcnt(0)\n"
    "v_cmp_eq_u64 vcc, s[78:79], v[164:165]\n"
    "s_nop 4\n"
    "s_and_b64 vcc, vcc, s[96:97]\n"
    "s_cmp_eq_u64 vcc, s[96:97]\n"
    "s_cbranch_scc1 LA_backE2_20\n"
    "s_sub_u32 s92, s92, 1\n"
    "s_cmp_lg_u32 s92, 0\n"
    "s_cbranch_scc1 LA_loopE2_21\n"
    "s_branch LA_backE2_20\n"
    "LA_slowE3_24:\n"
    "s_mov_b32 s92, 0x40000\n"
    "LA_loopE3_26:\n"
    "s_sleep 1\n"
    "buffer_load_dwordx2 v[160:161], v162, s[52:55], s88 offen sc1\n"
    "s_waitcnt vmcnt(0)\n"
    "v_cmp_eq_u64 vcc, s[78:79], v[160:161]\n"
    "s_nop 4\n"
    "s_and_b64 vcc, vcc, s[96:97]\n"
    "s_cmp_eq_u64 vcc, s[96:97]\n"
    "s_cbranch_scc1 LA_backE3_25\n"
    "s_sub_u32 s92, s92, 1\n"
    "s_cmp_lg_u32 s92, 0\n"
    "s_cbranch_scc1 LA_loopE3_26\n"
    "s_branch LA_backE3_25\n"
    "LA_slowL_30:\n"
    "s_mov_b32 s92, 0x40000\n"
    "LA_loopL_32:\n"
    "s_sleep 1\n"
    "buffer_load_dwordx2 v[168:169], v170, s[52:55], s88 offen sc1\n"
    "s_waitcnt vmcnt(0)\n"
    "v_cmp_eq_u64 vcc, s[78:79], v[168:169]\n"
    "s_nop 4\n"
    "s_and_b64 vcc, vcc, s[96:97]\n"
    "s_cmp_eq_u64 vcc, s[96:97]\n"
    "s_cbranch_scc1 LA_backL_31\n"
    "s_sub_u32 s92, s92, 1\n"
    "s_cmp_lg_u32 s92, 0\n"
    "s_cbranch_scc1 LA_loopL_32\n"
    "s_branch LA_backL_31\n"
    "LTYPE_B:\n"
    "v_and_b32 v235, 63, %0\n"
    "v_lshrrev_b32 v237, 6, %0\n"
    "s_nop 0\n"
    "v_readfirstlane_b32 s68, v237\n"
    "s_lshr_b32 s86, %1, 3\n"
    "s_and_b32 s72, s86, 7\n"
    "s_lshr_b32 s86, s86, 3\n"
    "s_lshr_b32 s87, %1, 1\n"
    "s_and_b32 s87, s87, 3\n"
    "s_lshl_b32 s87, s87, 2\n"
    "s_add_u32 s71, s86, s87\n"
    "s_lshl_b32 s77, s71, 7\n"
    "s_mov_b64 s[52:53], %9\n"
    "s_and_b32 s53, s53, 0xffff\n"
    "s_mov_b32 s54, 0x90a000\n"
    "s_mov_b32 s55, 0x20000\n"
    "s_mov_b64 s[36:37], %2\n"
    "s_and_b32 s37, s37, 0xffff\n"
    "s_mov_b32 s38, 0x200000\n"
    "s_mov_b32 s39, 0x20000\n"
    "s_mul_i32 s90, s71, 0x200\n"
    "s_add_u32 s90, s90, 0x900000\n"
    "s_and_b32 s69, s68, 3\n"
    "s_lshr_b32 s70, s68, 2\n"
    "s_lshl_b32 s73, s68, 10\n"
    "s_add_u32 s73, s73, 0x8000\n"
    "s_lshl_b32 s74, s68, 11\n"
    "s_add_u32 s74, s74, 0x8000\n"
    "v_mov_b32 v237, 0.0\n"
    "buffer_load_dwordx2 v[168:169], v237, s[52:55], s90 offen\n"
    "v_lshlrev_b32 v238, 4, v235\n"
    "s_lshl_b32 s86, s72, 3\n"
    "s_add_u32 s86, s86, s68\n"
    "s_add_u32 s86, s86, 64\n"
    "s_add_u32 s86, s86, s77\n"
    "s_lshl_b32 s86, s86, 10\n"
    "buffer_load_dwordx4 v[180:183], v238, s[36:39], s86 offen\n"
    "v_lshrrev_b32 v239, 3, v235\n"
    "v_and_b32 v240, 7, v235\n"
    "s_lshl_b32 s86, s68, 4\n"
    "s_add_u32 s86, s86, s77\n"
    "s_lshl_b32 s86, s86, 10\n"
    "v_lshrrev_b32 v241, 1, v239\n"
    "v_xor_b32 v241, v241, v240\n"
    "v_lshlrev_b32 v241, 4, v241\n"
    "v_lshl_add_u32 v242, v239, 10, v241\n"
    "v_add_u32 v160, s86, v242\n"
    "v_xor_b32 v242, 64, v242\n"
    "v_add_u32 v161, s86, v242\n"
    "v_add_u32 v161, 0x2000, v161\n"
    "s_lshl_b32 s86, s68, 3\n"
    "s_mov_b64 s[44:45], %5\n"
    "s_and_b32 s45, s45, 0xffff\n"
    "s_mov_b32 s46, 0x1c0000\n"
    "s_mov_b32 s47, 0x20000\n"
    "s_mov_b64 s[48:49], %6\n"
    "s_and_b32 s49, s49, 0xffff\n"
    "s_mov_b32 s50, 0x1c0000\n"
    "s_mov_b32 s51, 0x20000\n"
    "v_lshrrev_b32 v241, 2, v239\n"
    "v_xor_b32 v241, s68, v241\n"
    "v_and_b32 v241, 1, v241\n"
    "v_lshlrev_b32 v241, 2, v241\n"
    "v_and_b32 v242, 2, v239\n"
    "v_or_b32 v241, v241, v242\n"
    "v_xor_b32 v241, v241, v240\n"
    "v_lshlrev_b32 v241, 4, v241\n"
    "v_add_u32 v242, s86, v239\n"
    "v_mul_u32_u24 v242, 0xe00, v242\n"
    "v_add_u32 v228, v241, v242\n"
    "s_mul_i32 s75, s72, 0x140\n"
    "s_and_b32 s87, s72, 1\n"
    "s_mul_i32 s88, s87, 64\n"
    "s_add_u32 s76, s75, 0\n"
    "s_add_u32 s75, s75, s88\n"
    "s_add_u32 s75, s75, 0x400\n"
    "s_mul_i32 s88, s87, 0xffffff00\n"
    "s_add_u32 s76, s76, s88\n"
    "s_add_u32 s76, s76, 0x500\n"
    "s_add_u32 m0, s74, 0\n"
    "s_mov_b32 s89, 0\n"
    "buffer_load_dwordx4 v160, s[36:39], s89 offen lds\n"
    "s_add_u32 m0, s74, 0x400\n"
    "s_nop 0\n"
    "buffer_load_dwordx4 v161, s[36:39], s89 offen lds\n"
    "s_add_u32 m0, s73, 0x4000\n"
    "s_add_u32 s89, s75, 0\n"
    "buffer_load_dwordx4 v228, s[44:47], s89 offen lds\n"
    "s_add_u32 m0, s73, 0x6000\n"
    "s_nop 0\n"
    "buffer_load_dwordx4 v228, s[48:51], s89 offen lds\n"
    "s_add_u32 m0, s74, 0x8000\n"
    "s_mov_b32 s89, 0x80\n"
    "buffer_load_dwordx4 v160, s[36:39], s89 offen lds\n"
    "s_add_u32 m0, s74, 0x8400\n"
    "s_nop 0\n"
    "buffer_load_dwordx4 v161, s[36:39], s89 offen lds\n"
    "s_add_u32 m0, s73, 0xc000\n"
    "s_add_u32 s89, s75, 0x38000\n"
    "buffer_load_dwordx4 v228, s[44:47], s89 offen lds\n"
    "s_add_u32 m0, s73, 0xe000\n"
    "s_nop 0\n"
    "buffer_load_dwordx4 v228, s[48:51], s89 offen lds\n"
    "s_add_u32 m0, s74, 0x10000\n"
    "s_mov_b32 s89, 0x100\n"
    "buffer_load_dwordx4 v160, s[36:39], s89 offen lds\n"
    "s_add_u32 m0, s74, 0x10400\n"
    "s_nop 0\n"
    "buffer_load_dwordx4 v161, s[36:39], s89 offen lds\n"
    "s_add_u32 m0, s73, 0x14000\n"
    "s_add_u32 s89, s75, 0x70000\n"
    "buffer_load_dwordx4 v228, s[44:47], s89 offen lds\n"
    "s_add_u32 m0, s73, 0x16000\n"
    "s_nop 0\n"
    "buffer_load_dwordx4 v228, s[48:51], s89 offen lds\n"
    "s_add_u32 m0, s74, 0x18000\n"
    "s_mov_b32 s89, 0x180\n"
    "buffer_load_dwordx4 v160, s[36:39], s89 offen lds\n"
    "s_add_u32 m0, s74, 0x18400\n"
    "s_nop 0\n"
    "buffer_load_dwordx4 v161, s[36:39], s89 offen lds\n"
    "s_add_u32 m0, s73, 0x1c000\n"
    "s_add_u32 s89, s75, 0xa8000\n"
    "buffer_load_dwordx4 v228, s[44:47], s89 offen lds\n"
    "s_add_u32 m0, s73, 0x1e000\n"
    "s_nop 0\n"
    "buffer_load_dwordx4 v228, s[48:51], s89 offen lds\n"
    "v_lshrrev_b32 v241, 5, v235\n"
    "v_lshlrev_b32 v241, 1, v241\n"
    "v_and_b32 v242, 3, v235\n"
    "v_xor_b32 v241, v241, v242\n"
    "v_lshlrev_b32 v241, 4, v241\n"
    "s_lshl_b32 s86, s68, 4\n"
    "v_lshrrev_b32 v242, 2, v235\n"
    "v_add_u32 v242, s86, v242\n"
    "v_mul_u32_u24 v242, 0xe00, v242\n"
    "v_add_u32 v176, v241, v242\n"
    "s_mov_b64 s[56:57], %4\n"
    "s_and_b32 s57, s57, 0xffff\n"
    "s_mov_b32 s58, 0x80000\n"
    "s_mov_b32 s59, 0x20000\n"
    "s_mov_b64 s[60:61], %7\n"
    "s_and_b32 s61, s61, 0xffff\n"
    "s_mov_b32 s62, 0x1c0000\n"
    "s_mov_b32 s63, 0x20000\n"
    "s_mov_b64 s[64:65], %8\n"
    "s_and_b32 s65, s65, 0xffff\n"
    "s_mov_b32 s66, 0x200000\n"
    "s_mov_b32 s67, 0x20000\n"
    "s_and_b32 s93, s68, 3\n"
    "s_lshl_b32 s86, s93, 3\n"
    "v_lshrrev_b32 v241, 2, v239\n"
    "v_xor_b32 v241, s93, v241\n"
    "v_and_b32 v241, 1, v241\n"
    "v_lshlrev_b32 v241, 2, v241\n"
    "v_and_b32 v242, 2, v239\n"
    "v_or_b32 v241, v241, v242\n"
    "v_xor_b32 v241, v241, v240\n"
    "v_lshlrev_b32 v241, 4, v241\n"
    "v_add_u32 v242, s86, v239\n"
    "v_mul_u32_u24 v242, 0x400, v242\n"
    "v_add_u32 v229, v241, v242\n"
    "s_lshl_b32 s86, s72, 7\n"
    "v_add_u32 v229, s86, v229\n"
    "s_lshl_b32 s93, s70, 13\n"
    "s_lshl_b32 s86, s69, 10\n"
    "s_add_u32 s93, s93, s86\n"
    "s_add_u32 s93, s93, 0x8000\n"
    "v_and_b32 v239, 15, v235\n"
    "v_lshrrev_b32 v240, 4, v235\n"
    "v_lshrrev_b32 v244, 2, v239\n"
    "v_and_b32 v245, 3, v239\n"
    "v_lshrrev_b32 v241, 1, v239\n"
    "v_xor_b32 v241, v241, v240\n"
    "v_lshlrev_b32 v241, 4, v241\n"
    "v_lshl_add_u32 v241, v239, 7, v241\n"
    "s_lshl_b32 s86, s69, 12\n"
    "s_add_u32 s86, s86, 0x8000\n"
    "v_add_u32 v163, s86, v241\n"
    "v_xor_b32 v164, 64, v163\n"
    "v_lshrrev_b32 v246, 1, v244\n"
    "v_and_b32 v241, 1, v240\n"
    "v_lshl_or_b32 v246, v241, 1, v246\n"
    "v_lshl_add_u32 v241, v240, 3, v244\n"
    "v_lshlrev_b32 v241, 7, v241\n"
    "v_lshl_add_u32 v247, v245, 3, v241\n"
    "v_add_u32 v247, 0x8000, v247\n"
    "s_lshl_b32 s86, s70, 1\n"
    "v_xor_b32 v241, s86, v246\n"
    "v_lshl_add_u32 v220, v241, 5, v247\n"
    "v_xor_b32 v222, 32, v220\n"
    "v_xor_b32 v241, 64, v220\n"
    "v_add_u32 v221, 0x200, v241\n"
    "v_xor_b32 v223, 32, v221\n"
    "v_xor_b32 v241, s69, v246\n"
    "v_lshl_add_u32 v241, v241, 5, v247\n"
    "s_lshl_b32 s86, s70, 13\n"
    "v_add_u32 v224, s86, v241\n"
    "v_xor_b32 v241, 64, v224\n"
    "v_add_u32 v225, 0x200, v241\n"
    "v_lshl_add_u32 v241, v240, 3, v244\n"
    "v_lshlrev_b32 v241, 6, v241\n"
    "v_lshl_add_u32 v242, v245, 3, v241\n"
    "v_add_u32 v242, 0x8000, v242\n"
    "v_and_b32 v243, 1, v240\n"
    "v_xor_b32 v241, s70, v243\n"
    "v_lshl_add_u32 v226, v241, 5, v242\n"
    "s_and_b32 s86, s69, 1\n"
    "v_xor_b32 v241, s86, v243\n"
    "v_lshl_add_u32 v241, v241, 5, v242\n"
    "s_lshr_b32 s86, s69, 1\n"
    "s_lshl_b32 s86, s86, 13\n"
    "s_lshl_b32 s87, s70, 11\n"
    "s_add_u32 s86, s86, s87\n"
    "v_add_u32 v227, s86, v241\n"
    "v_mov_b32 v212, 0x3f803f80\n"
    "v_mov_b32 v213, 0x3f803f80\n"
    "v_mov_b32 v214, 0x3f803f80\n"
    "v_mov_b32 v215, 0x3f803f80\n"
    "s_lshl_b32 s86, s71, 3\n"
    "s_lshl_b32 s87, s69, 1\n"
    "s_add_u32 s86, s86, s87\n"
    "s_mul_i32 s86, s86, 0x12000\n"
    "v_lshlrev_b32 v241, 4, v235\n"
    "v_add_u32 v230, s86, v241\n"
    "s_lshl_b32 s86, s70, 8\n"
    "v_lshlrev_b32 v241, 4, v240\n"
    "v_add_u32 v236, s86, v241\n"
    "s_waitcnt vmcnt(17)\n"
    "v_readfirstlane_b32 s86, v168\n"
    "v_readfirstlane_b32 s87, v169\n"
    "s_nop 3\n"
    "s_mul_i32 s78, s86, 0x9e3779b1\n"
    "s_add_u32 s78, s78, s87\n"
    "s_add_u32 s78, s78, 0x7f4a7c15\n"
    "s_mul_i32 s79, s87, 0x85ebca77\n"
    "s_add_u32 s79, s79, s78\n"
    "s_xor_b32 s79, s79, 0xc2b2ae3d\n"
    "s_waitcnt vmcnt(12)\n"
    "s_barrier\n"
    "ds_read_b128 v[0:3], v163 offset:0\n"
    "ds_read_b128 v[64:67], v163 offset:2048\n"
    "ds_read_b128 v[4:7], v164 offset:0\n"
    "ds_read_b128 v[68:71], v164 offset:2048\n"
    "ds_read_b64_tr_b16 v[192:193], v220 offset:16384\n"
    "ds_read_b64_tr_b16 v[194:195], v221 offset:16384\n"
    "ds_read_b64_tr_b16 v[196:197], v222 offset:16384\n"
    "ds_read_b64_tr_b16 v[198:199], v223 offset:16384\n"
    "ds_read_b64_tr_b16 v[200:201], v220 offset:24576\n"
    "ds_read_b64_tr_b16 v[202:203], v221 offset:24576\n"
    "ds_read_b64_tr_b16 v[204:205], v222 offset:24576\n"
    "ds_read_b64_tr_b16 v[206:207], v223 offset:24576\n"
    "ds_read_b64_tr_b16 v[208:209], v224 offset:16384\n"
    "ds_read_b64_tr_b16 v[210:211], v225 offset:16384\n"
    "s_waitcnt lgkmcnt(8)\n"
    "v_mfma_f32_16x16x32_bf16 v[128:131], v[192:195], v[0:3], 0\n"
    "ds_read_b64_tr_b16 v[252:253], v220 offset:20480\n"
    "v_lshlrev_b32 v237, 16, v180\n"
    "v_and_b32 v238, 0xffff0000, v180\n"
    "v_mfma_f32_16x16x32_bf16 v[132:135], v[192:195], v[64:67], 0\n"
    "ds_read_b64_tr_b16 v[254:255], v221 offset:20480\n"
    "v_lshlrev_b32 v239, 16, v181\n"
    "v_and_b32 v240, 0xffff0000, v181\n"
    "s_waitcnt lgkmcnt(8)\n"
    "v_mfma_f32_16x16x32_bf16 v[136:139], v[196:199], v[0:3], 0\n"
    "ds_read_b64_tr_b16 v[192:193], v222 offset:20480\n"
    "v_lshlrev_b32 v241, 16, v182\n"
    "v_and_b32 v242, 0xffff0000, v182\n"
    "v_mfma_f32_16x16x32_bf16 v[140:143], v[196:199], v[64:67], 0\n"
    "ds_read_b64_tr_b16 v[194:195], v223 offset:20480\n"
    "v_lshlrev_b32 v243, 16, v183\n"
    "v_and_b32 v244, 0xffff0000, v183\n"
    "s_waitcnt lgkmcnt(8)\n"
    "v_mfma_f32_16x16x32_bf16 v[144:147], v[200:203], v[0:3], 0\n"
    "ds_read_b64_tr_b16 v[196:197], v220 offset:28672\n"
    "v_add_f32 v245, v237, v238\n"
    "v_add_f32 v246, v239, v240\n"
    "v_mfma_f32_16x16x32_bf16 v[148:151], v[200:203], v[64:67], 0\n"
    "ds_read_b64_tr_b16 v[198:199], v221 offset:28672\n"
    "v_add_f32 v245, v245, v241\n"
    "v_add_f32 v246, v246, v242\n"
    "s_waitcnt lgkmcnt(8)\n"
    "v_mfma_f32_16x16x32_bf16 v[152:155], v[204:207], v[0:3], 0\n"
    "ds_read_b64_tr_b16 v[200:201], v222 offset:28672\n"
    "v_add_f32 v245, v245, v243\n"
    "v_add_f32 v246, v246, v244\n"
    "v_mfma_f32_16x16x32_bf16 v[156:159], v[204:207], v[64:67], 0\n"
    "ds_read_b64_tr_b16 v[202:203], v223 offset:28672\n"
    "v_add_f32 v245, v245, v246\n"
    "s_nop 1\n"
    "v_add_f32_dpp v245, v245, v245 quad_perm:[1,0,3,2] row_mask:0xf bank_mask:0xf\n"
    "s_waitcnt lgkmcnt(8)\n"
    "v_mfma_f32_16x16x32_bf16 v[216:219], v[208:211], v[212:215], 0\n"
    "ds_read_b64_tr_b16 v[204:205], v224 offset:20480\n"
    "ds_read_b64_tr_b16 v[206:207], v225 offset:20480\n"
    "v_add_f32_dpp v245, v245, v245 quad_perm:[2,3,0,1] row_mask:0xf bank_mask:0xf\n"
    "s_nop 1\n"
    "v_add_f32_dpp v245, v245, v245 row_half_mirror row_mask:0xf bank_mask:0xf\n"
    "s_waitcnt vmcnt(8)\n"
    "s_barrier\n"
    "s_waitcnt lgkmcnt(8)\n"
    "v_mfma_f32_16x16x32_bf16 v[128:131], v[252:255], v[4:7], v[128:131]\n"
    "ds_read_b64_tr_b16 v[208:209], v220 offset:49152\n"
    "ds_read_b128 v[8:11], v163 offset:32768\n"
    "v_add_f32_dpp v245, v245, v245 row_mirror row_mask:0xf bank_mask:0xf\n"
    "s_nop 0\n"
    "v_mov_b32 v247, v245\n"
    "v_mfma_f32_16x16x32_bf16 v[132:135], v[252:255], v[68:71], v[132:135]\n"
    "ds_read_b64_tr_b16 v[210:211], v221 offset:49152\n"
    "v_permlane16_swap_b32 v245, v247\n"
    "s_nop 0\n"
    "v_add_f32 v245, v245, v247\n"
    "s_waitcnt lgkmcnt(9)\n"
    "v_mfma_f32_16x16x32_bf16 v[136:139], v[192:195], v[4:7], v[136:139]\n"
    "ds_read_b64_tr_b16 v[252:253], v222 offset:49152\n"
    "ds_read_b128 v[72:75], v163 offset:34816\n"
    "v_mov_b32 v247, v245\n"
    "s_nop 1\n"
    "v_permlane32_swap_b32 v245, v247\n"
    "v_mfma_f32_16x16x32_bf16 v[140:143], v[192:195], v[68:71], v[140:143]\n"
    "ds_read_b64_tr_b16 v[254:255], v223 offset:49152\n"
    "v_add_f32 v245, v245, v247\n"
    "v_mul_f32 v245, 0x3b000000, v245\n"
    "s_waitcnt lgkmcnt(10)\n"
    "v_mfma_f32_16x16x32_bf16 v[144:147], v[196:199], v[4:7], v[144:147]\n"
    "ds_read_b64_tr_b16 v[192:193], v220 offset:57344\n"
    "ds_read_b128 v[12:15], v164 offset:32768\n"
    "v_sub_f32 v237, v237, v245\n"
    "v_sub_f32 v238, v238, v245\n"
    "v_mfma_f32_16x16x32_bf16 v[148:151], v[196:199], v[68:71], v[148:151]\n"
    "ds_read_b64_tr_b16 v[194:195], v221 offset:57344\n"
    "v_sub_f32 v239, v239, v245\n"
    "v_sub_f32 v240, v240, v245\n"
    "s_waitcnt lgkmcnt(11)\n"
    "v_mfma_f32_16x16x32_bf16 v[152:155], v[200:203], v[4:7], v[152:155]\n"
    "ds_read_b64_tr_b16 v[196:197], v222 offset:57344\n"
    "ds_read_b128 v[76:79], v164 offset:34816\n"
    "v_sub_f32 v241, v241, v245\n"
    "v_sub_f32 v242, v242, v245\n"
    "v_mfma_f32_16x16x32_bf16 v[156:159], v[200:203], v[68:71], v[156:159]\n"
    "ds_read_b64_tr_b16 v[198:199], v223 offset:57344\n"
    "v_sub_f32 v243, v243, v245\n"
    "v_sub_f32 v244, v244, v245\n"
    "s_waitcnt lgkmcnt(12)\n"
    "v_mfma_f32_16x16x32_bf16 v[216:219], v[204:207], v[212:215], v[216:219]\n"
    "ds_read_b64_tr_b16 v[200:201], v224 offset:49152\n"
    "ds_read_b64_tr_b16 v[202:203], v225 offset:49152\n"
    "s_mul_i32 s98, s72, 0xc00\n"
    "s_lshl_b32 s87, s70, 10\n"
    "s_add_u32 s98, s98, s87\n"
    "s_add_u32 s98, s98, 0x4000\n"
    "s_mul_i32 s99, s72, 0x800\n"
    "s_add_u32 s99, s99, s87\n"
    "s_add_u32 s99, s99, 0xe000\n"
    "s_mul_i32 s91, s72, 0xc00\n"
    "s_mul_i32 s87, s70, 0x12000\n"
    "s_add_u32 s91, s91, s87\n"
    "s_add_u32 s91, s91, 0x4800\n"
    "v_mul_f32 v246, v237, v237\n"
    "v_fmac_f32 v246, v238, v238\n"
    "s_waitcnt lgkmcnt(11)\n"
    "v_mfma_f32_16x16x32_bf16 v[128:131], v[208:211], v[8:11], v[128:131]\n"
    "ds_read_b64_tr_b16 v[204:205], v220 offset:53248\n"
    "v_fmac_f32 v246, v239, v239\n"
    "v_fmac_f32 v246, v240, v240\n"
    "s_waitcnt lgkmcnt(10)\n"
    "v_mfma_f32_16x16x32_bf16 v[132:135], v[208:211], v[72:75], v[132:135]\n"
    "ds_read_b64_tr_b16 v[206:207], v221 offset:53248\n"
    "v_fmac_f32 v246, v241, v241\n"
    "v_fmac_f32 v246, v242, v242\n"
    "s_waitcnt lgkmcnt(10)\n"
    "v_mfma_f32_16x16x32_bf16 v[136:139], v[252:255], v[8:11], v[136:139]\n"
    "ds_read_b64_tr_b16 v[208:209], v222 offset:53248\n"
    "v_fmac_f32 v246, v243, v243\n"
    "v_fmac_f32 v246, v244, v244\n"
    "v_mfma_f32_16x16x32_bf16 v[140:143], v[252:255], v[72:75], v[140:143]\n"
    "ds_read_b64_tr_b16 v[210:211], v223 offset:53248\n"
    "v_add_f32_dpp v246, v246, v246 quad_perm:[1,0,3,2] row_mask:0xf bank_mask:0xf\n"
    "s_nop 1\n"
    "v_add_f32_dpp v246, v246, v246 quad_perm:[2,3,0,1] row_mask:0xf bank_mask:0xf\n"
    "s_waitcnt lgkmcnt(9)\n"
    "v_mfma_f32_16x16x32_bf16 v[144:147], v[192:195], v[8:11], v[144:147]\n"
    "ds_read_b64_tr_b16 v[252:253], v220 offset:61440\n"
    "v_add_f32_dpp v246, v246, v246 row_half_mirror row_mask:0xf bank_mask:0xf\n"
    "s_nop 1\n"
    "v_add_f32_dpp v246, v246, v246 row_mirror row_mask:0xf bank_mask:0xf\n"
    "v_mfma_f32_16x16x32_bf16 v[148:151], v[192:195], v[72:75], v[148:151]\n"
    "ds_read_b64_tr_b16 v[254:255], v221 offset:61440\n"
    "v_mov_b32 v247, v246\n"
    "s_nop 1\n"
    "v_permlane16_swap_b32 v246, v247\n"
    "s_waitcnt lgkmcnt(8)\n"
    "v_mfma_f32_16x16x32_bf16 v[152:155], v[196:199], v[8:11], v[152:155]\n"
    "ds_read_b64_tr_b16 v[192:193], v222 offset:61440\n"
    "v_add_f32 v246, v246, v247\n"
    "v_mov_b32 v247, v246\n"
    "v_mfma_f32_16x16x32_bf16 v[156:159], v[196:199], v[72:75], v[156:159]\n"
    "ds_read_b64_tr_b16 v[194:195], v223 offset:61440\n"
    "v_permlane32_swap_b32 v246, v247\n"
    "s_nop 0\n"
    "v_add_f32 v246, v246, v247\n"
    "s_waitcnt lgkmcnt(8)\n"
    "v_mfma_f32_16x16x32_bf16 v[216:219], v[200:203], v[212:215], v[216:219]\n"
    "ds_read_b64_tr_b16 v[196:197], v224 offset:53248\n"
    "ds_read_b64_tr_b16 v[198:199], v225 offset:53248\n"
    "v_mov_b32 v247, 0x3727c5ac\n"
    "v_fmac_f32 v247, 0x3b000000, v246\n"
    "s_waitcnt vmcnt(4)\n"
    "s_barrier\n"
    "s_waitcnt lgkmcnt(8)\n"
    "v_mfma_f32_16x16x32_bf16 v[128:131], v[204:207], v[12:15], v[128:131]\n"
    "s_add_u32 m0, s74, 0\n"
    "s_mov_b32 s89, 0x200\n"
    "buffer_load_dwordx4 v160, s[36:39], s89 offen lds\n"
    "v_xor_b32 v220, 0x10000, v220\n"
    "v_xor_b32 v221, 0x10000, v221\n"
    "v_xor_b32 v222, 0x10000, v222\n"
    "v_xor_b32 v223, 0x10000, v223\n"
    "ds_read_b64_tr_b16 v[200:201], v220 offset:16384\n"
    "v_xor_b32 v163, 0x10000, v163\n"
    "v_xor_b32 v164, 0x10000, v164\n"
    "ds_read_b128 v[16:19], v163 offset:0\n"
    "v_mfma_f32_16x16x32_bf16 v[132:135], v[204:207], v[76:79], v[132:135]\n"
    "ds_read_b64_tr_b16 v[202:203], v221 offset:16384\n"
    "v_rsq_f32 v246, v247\n"
    "s_waitcnt lgkmcnt(9)\n"
    "v_mfma_f32_16x16x32_bf16 v[136:139], v[208:211], v[12:15], v[136:139]\n"
    "ds_read_b64_tr_b16 v[204:205], v222 offset:16384\n"
    "ds_read_b128 v[80:83], v163 offset:2048\n"
    "v_mov_b32 v248, v245\n"
    "v_mov_b32 v249, s78\n"
    "v_mfma_f32_16x16x32_bf16 v[140:143], v[208:211], v[76:79], v[140:143]\n"
    "s_add_u32 m0, s74, 0x400\n"
    "s_nop 0\n"
    "buffer_load_dwordx4 v161, s[36:39], s89 offen lds\n"
    "ds_read_b64_tr_b16 v[206:207], v223 offset:16384\n"
    "v_mov_b32 v250, v246\n"
    "s_waitcnt lgkmcnt(10)\n"
    "v_mfma_f32_16x16x32_bf16 v[144:147], v[252:255], v[12:15], v[144:147]\n"
    "ds_read_b64_tr_b16 v[208:209], v220 offset:24576\n"
    "ds_read_b128 v[20:23], v164 offset:0\n"
    "v_mov_b32 v251, s79\n"
    "v_mov_b32 v237, 0.0\n"
    "v_mfma_f32_16x16x32_bf16 v[148:151], v[252:255], v[76:79], v[148:151]\n"
    "ds_read_b64_tr_b16 v[210:211], v221 offset:24576\n"
    "s_lshl_b32 s87, s72, 3\n"
    "s_add_u32 s87, s87, s68\n"
    "s_add_u32 s87, s87, 64\n"
    "s_add_u32 s87, s87, s77\n"
    "s_lshl_b32 s87, s87, 4\n"
    "s_add_u32 s87, s87, 0x902000\n"
    "s_mov_b64 exec, 1\n"
    "buffer_store_dwordx4 v[248:251], v237, s[52:55], s87 offen sc1\n"
    "s_mov_b64 exec, -1\n"
    "s_waitcnt lgkmcnt(11)\n"
    "s_nop 2\n"
    "v_mfma_f32_16x16x32_bf16 v[152:155], v[192:195], v[12:15], v[152:155]\n"
    "s_add_u32 m0, s73, 0x4000\n"
    "s_add_u32 s89, s75, 0xe0000\n"
    "buffer_load_dwordx4 v228, s[44:47], s89 offen lds\n"
    "ds_read_b64_tr_b16 v[252:253], v222 offset:24576\n"
    "ds_read_b128 v[84:87], v164 offset:2048\n"
    "v_mfma_f32_16x16x32_bf16 v[156:159], v[192:195], v[76:79], v[156:159]\n"
    "ds_read_b64_tr_b16 v[254:255], v223 offset:24576\n"
    "s_waitcnt lgkmcnt(12)\n"
    "v_mfma_f32_16x16x32_bf16 v[216:219], v[196:199], v[212:215], v[216:219]\n"
    "v_xor_b32 v224, 0x10000, v224\n"
    "v_xor_b32 v225, 0x10000, v225\n"
    "ds_read_b64_tr_b16 v[192:193], v224 offset:16384\n"
    "ds_read_b64_tr_b16 v[194:195], v225 offset:16384\n"
    "s_waitcnt lgkmcnt(11)\n"
    "v_mfma_f32_16x16x32_bf16 v[128:131], v[200:203], v[16:19], v[128:131]\n"
    "s_add_u32 m0, s73, 0x6000\n"
    "s_nop 0\n"
    "buffer_load_dwordx4 v228, s[48:51], s89 offen lds\n"
    "ds_read_b64_tr_b16 v[196:197], v220 offset:20480\n"
    "s_waitcnt lgkmcnt(10)\n"
    "v_mfma_f32_16x16x32_bf16 v[132:135], v[200:203], v[80:83], v[132:135]\n"
    "ds_read_b64_tr_b16 v[198:199], v221 offset:20480\n"
    "s_waitcnt lgkmcnt(10)\n"
    "v_mfma_f32_16x16x32_bf16 v[136:139], v[204:207], v[16:19], v[136:139]\n"
    "ds_read_b64_tr_b16 v[200:201], v222 offset:20480\n"
    "v_mfma_f32_16x16x32_bf16 v[140:143], v[204:207], v[80:83], v[140:143]\n"
    "ds_read_b64_tr_b16 v[202:203], v223 offset:20480\n"
    "s_waitcnt lgkmcnt(9)\n"
    "v_mfma_f32_16x16x32_bf16 v[144:147], v[208:211], v[16:19], v[144:147]\n"
    "ds_read_b64_tr_b16 v[204:205], v220 offset:28672\n"
    "v_mfma_f32_16x16x32_bf16 v[148:151], v[208:211], v[80:83], v[148:151]\n"
    "ds_read_b64_tr_b16 v[206:207], v221 offset:28672\n"
    "s_waitcnt lgkmcnt(8)\n"
    "v_mfma_f32_16x16x32_bf16 v[152:155], v[252:255], v[16:19], v[152:155]\n"
    "ds_read_b64_tr_b16 v[208:209], v222 offset:28672\n"
    "v_mfma_f32_16x16x32_bf16 v[156:159], v[252:255], v[80:83], v[156:159]\n"
    "ds_read_b64_tr_b16 v[210:211], v223 offset:28672\n"
    "s_waitcnt lgkmcnt(8)\n"
    "v_mfma_f32_16x16x32_bf16 v[216:219], v[192:195], v[212:215], v[216:219]\n"
    "ds_read_b64_tr_b16 v[252:253], v224 offset:20480\n"
    "ds_read_b64_tr_b16 v[254:255], v225 offset:20480\n"
    "s_waitcnt vmcnt(5)\n"
    "s_barrier\n"
    "s_waitcnt lgkmcnt(8)\n"
    "v_mfma_f32_16x16x32_bf16 v[128:131], v[196:199], v[20:23], v[128:131]\n"
    "s_add_u32 m0, s74, 0x8000\n"
    "s_mov_b32 s89, 0x280\n"
    "buffer_load_dwordx4 v160, s[36:39], s89 offen lds\n"
    "ds_read_b64_tr_b16 v[192:193], v220 offset:49152\n"
    "ds_read_b128 v[24:27], v163 offset:32768\n"
    "v_mfma_f32_16x16x32_bf16 v[132:135], v[196:199], v[84:87], v[132:135]\n"
    "ds_read_b64_tr_b16 v[194:195], v221 offset:49152\n"
    "s_waitcnt lgkmcnt(9)\n"
    "v_mfma_f32_16x16x32_bf16 v[136:139], v[200:203], v[20:23], v[136:139]\n"
    "ds_read_b64_tr_b16 v[196:197], v222 offset:49152\n"
    "ds_read_b128 v[88:91], v163 offset:34816\n"
    "v_mfma_f32_16x16x32_bf16 v[140:143], v[200:203], v[84:87], v[140:143]\n"
    "s_add_u32 m0, s74, 0x8400\n"
    "s_nop 0\n"
    "buffer_load_dwordx4 v161, s[36:39], s89 offen lds\n"
    "ds_read_b64_tr_b16 v[198:199], v223 offset:49152\n"
    "s_waitcnt lgkmcnt(10)\n"
    "v_mfma_f32_16x16x32_bf16 v[144:147], v[204:207], v[20:23], v[144:147]\n"
    "ds_read_b64_tr_b16 v[200:201], v220 offset:57344\n"
    "ds_read_b128 v[28:31], v164 offset:32768\n"
    "v_mfma_f32_16x16x32_bf16 v[148:151], v[204:207], v[84:87], v[148:151]\n"
    "ds_read_b64_tr_b16 v[202:203], v221 offset:57344\n"
    "s_waitcnt lgkmcnt(11)\n"
    "v_mfma_f32_16x16x32_bf16 v[152:155], v[208:211], v[20:23], v[152:155]\n"
    "s_add_u32 m0, s73, 0xc000\n"
    "s_add_u32 s89, s75, 0x118000\n"
    "buffer_load_dwordx4 v228, s[44:47], s89 offen lds\n"
    "ds_read_b64_tr_b16 v[204:205], v222 offset:57344\n"
    "ds_read_b128 v[92:95], v164 offset:34816\n"
    "v_mfma_f32_16x16x32_bf16 v[156:159], v[208:211], v[84:87], v[156:159]\n"
    "ds_read_b64_tr_b16 v[206:207], v223 offset:57344\n"
    "s_waitcnt lgkmcnt(12)\n"
    "v_mfma_f32_16x16x32_bf16 v[216:219], v[252:255], v[212:215], v[216:219]\n"
    "ds_read_b64_tr_b16 v[208:209], v224 offset:49152\n"
    "ds_read_b64_tr_b16 v[210:211], v225 offset:49152\n"
    "s_waitcnt lgkmcnt(11)\n"
    "v_mfma_f32_16x16x32_bf16 v[128:131], v[192:195], v[24:27], v[128:131]\n"
    "s_add_u32 m0, s73, 0xe000\n"
    "s_nop 0\n"
    "buffer_load_dwordx4 v228, s[48:51], s89 offen lds\n"
    "ds_read_b64_tr_b16 v[252:253], v220 offset:53248\n"
    "s_waitcnt lgkmcnt(10)\n"
    "v_mfma_f32_16x16x32_bf16 v[132:135], v[192:195], v[88:91], v[132:135]\n"
    "ds_read_b64_tr_b16 v[254:255], v221 offset:53248\n"
    "s_waitcnt lgkmcnt(10)\n"
    "v_mfma_f32_16x16x32_bf16 v[136:139], v[196:199], v[24:27], v[136:139]\n"
    "ds_read_b64_tr_b16 v[192:193], v222 offset:53248\n"
    "v_mfma_f32_16x16x32_bf16 v[140:143], v[196:199], v[88:91], v[140:143]\n"
    "ds_read_b64_tr_b16 v[194:195], v223 offset:53248\n"
    "s_waitcnt lgkmcnt(9)\n"
    "v_mfma_f32_16x16x32_bf16 v[144:147], v[200:203], v[24:27], v[144:147]\n"
    "ds_read_b64_tr_b16 v[196:197], v220 offset:61440\n"
    "v_mfma_f32_16x16x32_bf16 v[148:151], v[200:203], v[88:91], v[148:151]\n"
    "ds_read_b64_tr_b16 v[198:199], v221 offset:61440\n"
    "s_waitcnt lgkmcnt(8)\n"
    "v_mfma_f32_16x16x32_bf16 v[152:155], v[204:207], v[24:27], v[152:155]\n"
    "ds_read_b64_tr_b16 v[200:201], v222 offset:61440\n"
    "v_mfma_f32_16x16x32_bf16 v[156:159], v[204:207], v[88:91], v[156:159]\n"
    "ds_read_b64_tr_b16 v[202:203], v223 offset:61440\n"
    "s_waitcnt lgkmcnt(8)\n"
    "v_mfma_f32_16x16x32_bf16 v[216:219], v[208:211], v[212:215], v[216:219]\n"
    "ds_read_b64_tr_b16 v[204:205], v224 offset:53248\n"
    "ds_read_b64_tr_b16 v[206:207], v225 offset:53248\n"
    "s_waitcnt vmcnt(4)\n"
    "s_barrier\n"
    "s_waitcnt lgkmcnt(8)\n"
    "v_mfma_f32_16x16x32_bf16 v[128:131], v[252:255], v[28:31], v[128:131]\n"
    "s_add_u32 m0, s74, 0x10000\n"
    "s_mov_b32 s89, 0x300\n"
    "buffer_load_dwordx4 v160, s[36:39], s89 offen lds\n"
    "v_xor_b32 v220, 0x10000, v220\n"
    "v_xor_b32 v221, 0x10000, v221\n"
    "v_xor_b32 v222, 0x10000, v222\n"
    "v_xor_b32 v223, 0x10000, v223\n"
    "ds_read_b64_tr_b16 v[208:209], v220 offset:16384\n"
    "v_xor_b32 v163, 0x10000, v163\n"
    "v_xor_b32 v164, 0x10000, v164\n"
    "ds_read_b128 v[32:35], v163 offset:0\n"
    "v_mfma_f32_16x16x32_bf16 v[132:135], v[252:255], v[92:95], v[132:135]\n"
    "ds_read_b64_tr_b16 v[210:211], v221 offset:16384\n"
    "s_waitcnt lgkmcnt(9)\n"
    "v_mfma_f32_16x16x32_bf16 v[136:139], v[192:195], v[28:31], v[136:139]\n"
    "ds_read_b64_tr_b16 v[252:253], v222 offset:16384\n"
    "ds_read_b128 v[96:99], v163 offset:2048\n"
    "v_mfma_f32_16x16x32_bf16 v[140:143], v[192:195], v[92:95], v[140:143]\n"
    "s_add_u32 m0, s74, 0x10400\n"
    "s_nop 0\n"
    "buffer_load_dwordx4 v161, s[36:39], s89 offen lds\n"
    "ds_read_b64_tr_b16 v[254:255], v223 offset:16384\n"
    "s_waitcnt lgkmcnt(10)\n"
    "v_mfma_f32_16x16x32_bf16 v[144:147], v[196:199], v[28:31], v[144:147]\n"
    "ds_read_b64_tr_b16 v[192:193], v220 offset:24576\n"
    "ds_read_b128 v[36:39], v164 offset:0\n"
    "v_mfma_f32_16x16x32_bf16 v[148:151], v[196:199], v[92:95], v[148:151]\n"
    "ds_read_b64_tr_b16 v[194:195], v221 offset:24576\n"
    "s_waitcnt lgkmcnt(11)\n"
    "v_mfma_f32_16x16x32_bf16 v[152:155], v[200:203], v[28:31], v[152:155]\n"
    "s_add_u32 m0, s73, 0x14000\n"
    "s_add_u32 s89, s75, 0x150000\n"
    "buffer_load_dwordx4 v228, s[44:47], s89 offen lds\n"
    "ds_read_b64_tr_b16 v[196:197], v222 offset:24576\n"
    "ds_read_b128 v[100:103], v164 offset:2048\n"
    "v_mfma_f32_16x16x32_bf16 v[156:159], v[200:203], v[92:95], v[156:159]\n"
    "ds_read_b64_tr_b16 v[198:199], v223 offset:24576\n"
    "s_waitcnt lgkmcnt(12)\n"
    "v_mfma_f32_16x16x32_bf16 v[216:219], v[204:207], v[212:215], v[216:219]\n"
    "v_xor_b32 v224, 0x10000, v224\n"
    "v_xor_b32 v225, 0x10000, v225\n"
    "ds_read_b64_tr_b16 v[200:201], v224 offset:16384\n"
    "ds_read_b64_tr_b16 v[202:203], v225 offset:16384\n"
    "s_waitcnt lgkmcnt(11)\n"
    "v_mfma_f32_16x16x32_bf16 v[128:131], v[208:211], v[32:35], v[128:131]\n"
    "s_add_u32 m0, s73, 0x16000\n"
    "s_nop 0\n"
    "buffer_load_dwordx4 v228, s[48:51], s89 offen lds\n"
    "ds_read_b64_tr_b16 v[204:205], v220 offset:20480\n"
    "s_waitcnt lgkmcnt(10)\n"
    "v_mfma_f32_16x16x32_bf16 v[132:135], v[208:211], v[96:99], v[132:135]\n"
    "ds_read_b64_tr_b16 v[206:207], v221 offset:20480\n"
    "s_waitcnt lgkmcnt(10)\n"
    "v_mfma_f32_16x16x32_bf16 v[136:139], v[252:255], v[32:35], v[136:139]\n"
    "ds_read_b64_tr_b16 v[208:209], v222 offset:20480\n"
    "v_mfma_f32_16x16x32_bf16 v[140:143], v[252:255], v[96:99], v[140:143]\n"
    "ds_read_b64_tr_b16 v[210:211], v223 offset:20480\n"
    "s_waitcnt lgkmcnt(9)\n"
    "v_mfma_f32_16x16x32_bf16 v[144:147], v[192:195], v[32:35], v[144:147]\n"
    "ds_read_b64_tr_b16 v[252:253], v220 offset:28672\n"
    "v_mfma_f32_16x16x32_bf16 v[148:151], v[192:195], v[96:99], v[148:151]\n"
    "ds_read_b64_tr_b16 v[254:255], v221 offset:28672\n"
    "s_waitcnt lgkmcnt(8)\n"
    "v_mfma_f32_16x16x32_bf16 v[152:155], v[196:199], v[32:35], v[152:155]\n"
    "ds_read_b64_tr_b16 v[192:193], v222 offset:28672\n"
    "v_mfma_f32_16x16x32_bf16 v[156:159], v[196:199], v[96:99], v[156:159]\n"
    "ds_read_b64_tr_b16 v[194:195], v223 offset:28672\n"
    "s_waitcnt lgkmcnt(8)\n"
    "v_mfma_f32_16x16x32_bf16 v[216:219], v[200:203], v[212:215], v[216:219]\n"
    "ds_read_b64_tr_b16 v[196:197], v224 offset:20480\n"
    "ds_read_b64_tr_b16 v[198:199], v225 offset:20480\n"
    "s_waitcnt vmcnt(4)\n"
    "s_barrier\n"
    "s_waitcnt lgkmcnt(8)\n"
    "v_mfma_f32_16x16x32_bf16 v[128:131], v[204:207], v[36:39], v[128:131]\n"
    "s_add_u32 m0, s74, 0x18000\n"
    "s_mov_b32 s89, 0x380\n"
    "buffer_load_dwordx4 v160, s[36:39], s89 offen lds\n"
    "ds_read_b64_tr_b16 v[200:201], v220 offset:49152\n"
    "ds_read_b128 v[40:43], v163 offset:32768\n"
    "v_and_b32 v237, 15, v235\n"
    "v_mfma_f32_16x16x32_bf16 v[132:135], v[204:207], v[100:103], v[132:135]\n"
    "ds_read_b64_tr_b16 v[202:203], v221 offset:49152\n"
    "v_lshlrev_b32 v237, 4, v237\n"
    "s_lshl_b32 s88, s68, 4\n"
    "s_add_u32 s88, s88, s77\n"
    "s_lshl_b32 s88, s88, 4\n"
    "s_add_u32 s88, s88, 0x902000\n"
    "buffer_load_dwordx4 v[168:171], v237, s[52:55], s88 offen sc1\n"
    "s_waitcnt lgkmcnt(9)\n"
    "v_mfma_f32_16x16x32_bf16 v[136:139], v[208:211], v[36:39], v[136:139]\n"
    "ds_read_b64_tr_b16 v[204:205], v222 offset:49152\n"
    "ds_read_b128 v[104:107], v163 offset:34816\n"
    "v_mfma_f32_16x16x32_bf16 v[140:143], v[208:211], v[100:103], v[140:143]\n"
    "s_add_u32 m0, s74, 0x18400\n"
    "s_nop 0\n"
    "buffer_load_dwordx4 v161, s[36:39], s89 offen lds\n"
    "ds_read_b64_tr_b16 v[206:207], v223 offset:49152\n"
    "s_waitcnt lgkmcnt(10)\n"
    "v_mfma_f32_16x16x32_bf16 v[144:147], v[252:255], v[36:39], v[144:147]\n"
    "ds_read_b64_tr_b16 v[208:209], v220 offset:57344\n"
    "ds_read_b128 v[44:47], v164 offset:32768\n"
    "v_mfma_f32_16x16x32_bf16 v[148:151], v[252:255], v[100:103], v[148:151]\n"
    "ds_read_b64_tr_b16 v[210:211], v221 offset:57344\n"
    "s_waitcnt lgkmcnt(11)\n"
    "v_mfma_f32_16x16x32_bf16 v[152:155], v[192:195], v[36:39], v[152:155]\n"
    "s_add_u32 m0, s73, 0x1c000\n"
    "s_add_u32 s89, s75, 0x188000\n"
    "buffer_load_dwordx4 v228, s[44:47], s89 offen lds\n"
    "ds_read_b64_tr_b16 v[252:253], v222 offset:57344\n"
    "ds_read_b128 v[108:111], v164 offset:34816\n"
    "v_mfma_f32_16x16x32_bf16 v[156:159], v[192:195], v[100:103], v[156:159]\n"
    "ds_read_b64_tr_b16 v[254:255], v223 offset:57344\n"
    "s_waitcnt lgkmcnt(12)\n"
    "v_mfma_f32_16x16x32_bf16 v[216:219], v[196:199], v[212:215], v[216:219]\n"
    "ds_read_b64_tr_b16 v[192:193], v224 offset:49152\n"
    "ds_read_b64_tr_b16 v[194:195], v225 offset:49152\n"
    "s_waitcnt lgkmcnt(11)\n"
    "v_mfma_f32_16x16x32_bf16 v[128:131], v[200:203], v[40:43], v[128:131]\n"
    "s_add_u32 m0, s73, 0x1e000\n"
    "s_nop 0\n"
    "buffer_load_dwordx4 v228, s[48:51], s89 offen lds\n"
    "ds_read_b64_tr_b16 v[196:197], v220 offset:53248\n"
    "s_waitcnt lgkmcnt(10)\n"
    "v_mfma_f32_16x16x32_bf16 v[132:135], v[200:203], v[104:107], v[132:135]\n"
    "ds_read_b64_tr_b16 v[198:199], v221 offset:53248\n"
    "s_waitcnt lgkmcnt(10)\n"
    "v_mfma_f32_16x16x32_bf16 v[136:139], v[204:207], v[40:43], v[136:139]\n"
    "ds_read_b64_tr_b16 v[200:201], v222 offset:53248\n"
    "v_mfma_f32_16x16x32_bf16 v[140:143], v[204:207], v[104:107], v[140:143]\n"
    "ds_read_b64_tr_b16 v[202:203], v223 offset:53248\n"
    "s_waitcnt lgkmcnt(9)\n"
    "v_mfma_f32_16x16x32_bf16 v[144:147], v[208:211], v[40:43], v[144:147]\n"
    "ds_read_b64_tr_b16 v[204:205], v220 offset:61440\n"
    "v_mfma_f32_16x16x32_bf16 v[148:151], v[208:211], v[104:107], v[148:151]\n"
    "ds_read_b64_tr_b16 v[206:207], v221 offset:61440\n"
    "s_waitcnt lgkmcnt(8)\n"
    "v_mfma_f32_16x16x32_bf16 v[152:155], v[252:255], v[40:43], v[152:155]\n"
    "ds_read_b64_tr_b16 v[208:209], v222 offset:61440\n"
    "v_mfma_f32_16x16x32_bf16 v[156:159], v[252:255], v[104:107], v[156:159]\n"
    "ds_read_b64_tr_b16 v[210:211], v223 offset:61440\n"
    "s_waitcnt lgkmcnt(8)\n"
    "v_mfma_f32_16x16x32_bf16 v[216:219], v[192:195], v[212:215], v[216:219]\n"
    "ds_read_b64_tr_b16 v[252:253], v224 offset:53248\n"
    "ds_read_b64_tr_b16 v[254:255], v225 offset:53248\n"
    "s_waitcnt vmcnt(5)\n"
    "s_barrier\n"
    "s_waitcnt lgkmcnt(8)\n"
    "v_mfma_f32_16x16x32_bf16 v[128:131], v[196:199], v[44:47], v[128:131]\n"
    "s_add_u32 m0, s73, 0\n"
    "s_add_u32 s89, s76, 0\n"
    "buffer_load_dwordx4 v176, s[44:47], s89 offen lds\n"
    "v_xor_b32 v220, 0x10000, v220\n"
    "v_xor_b32 v221, 0x10000, v221\n"
    "v_xor_b32 v222, 0x10000, v222\n"
    "v_xor_b32 v223, 0x10000, v223\n"
    "ds_read_b64_tr_b16 v[192:193], v220 offset:16384\n"
    "v_xor_b32 v163, 0x10000, v163\n"
    "v_xor_b32 v164, 0x10000, v164\n"
    "ds_read_b128 v[48:51], v163 offset:0\n"
    "v_mfma_f32_16x16x32_bf16 v[132:135], v[196:199], v[108:111], v[132:135]\n"
    "ds_read_b64_tr_b16 v[194:195], v221 offset:16384\n"
    "s_waitcnt lgkmcnt(9)\n"
    "v_mfma_f32_16x16x32_bf16 v[136:139], v[200:203], v[44:47], v[136:139]\n"
    "ds_read_b64_tr_b16 v[196:197], v222 offset:16384\n"
    "ds_read_b128 v[112:115], v163 offset:2048\n"
    "v_mfma_f32_16x16x32_bf16 v[140:143], v[200:203], v[108:111], v[140:143]\n"
    "s_add_u32 m0, s73, 0x2000\n"
    "s_nop 0\n"
    "buffer_load_dwordx4 v176, s[48:51], s89 offen lds\n"
    "ds_read_b64_tr_b16 v[198:199], v223 offset:16384\n"
    "s_waitcnt lgkmcnt(10)\n"
    "v_mfma_f32_16x16x32_bf16 v[144:147], v[204:207], v[44:47], v[144:147]\n"
    "ds_read_b64_tr_b16 v[200:201], v220 offset:24576\n"
    "ds_read_b128 v[52:55], v164 offset:0\n"
    "v_mfma_f32_16x16x32_bf16 v[148:151], v[204:207], v[108:111], v[148:151]\n"
    "ds_read_b64_tr_b16 v[202:203], v221 offset:24576\n"
    "s_waitcnt lgkmcnt(11)\n"
    "v_mfma_f32_16x16x32_bf16 v[152:155], v[208:211], v[44:47], v[152:155]\n"
    "s_add_u32 m0, s73, 0x4000\n"
    "s_add_u32 s89, s76, 0x70000\n"
    "buffer_load_dwordx4 v176, s[44:47], s89 offen lds\n"
    "ds_read_b64_tr_b16 v[204:205], v222 offset:24576\n"
    "ds_read_b128 v[116:119], v164 offset:2048\n"
    "v_mfma_f32_16x16x32_bf16 v[156:159], v[208:211], v[108:111], v[156:159]\n"
    "ds_read_b64_tr_b16 v[206:207], v223 offset:24576\n"
    "s_waitcnt lgkmcnt(12)\n"
    "v_mfma_f32_16x16x32_bf16 v[216:219], v[252:255], v[212:215], v[216:219]\n"
    "v_xor_b32 v224, 0x10000, v224\n"
    "v_xor_b32 v225, 0x10000, v225\n"
    "ds_read_b64_tr_b16 v[208:209], v224 offset:16384\n"
    "ds_read_b64_tr_b16 v[210:211], v225 offset:16384\n"
    "s_waitcnt lgkmcnt(11)\n"
    "v_mfma_f32_16x16x32_bf16 v[128:131], v[192:195], v[48:51], v[128:131]\n"
    "s_add_u32 m0, s73, 0x6000\n"
    "s_nop 0\n"
    "buffer_load_dwordx4 v176, s[48:51], s89 offen lds\n"
    "ds_read_b64_tr_b16 v[252:253], v220 offset:20480\n"
    "s_waitcnt lgkmcnt(10)\n"
    "v_mfma_f32_16x16x32_bf16 v[132:135], v[192:195], v[112:115], v[132:135]\n"
    "ds_read_b64_tr_b16 v[254:255], v221 offset:20480\n"
    "s_waitcnt lgkmcnt(10)\n"
    "v_mfma_f32_16x16x32_bf16 v[136:139], v[196:199], v[48:51], v[136:139]\n"
    "ds_read_b64_tr_b16 v[192:193], v222 offset:20480\n"
    "v_mfma_f32_16x16x32_bf16 v[140:143], v[196:199], v[112:115], v[140:143]\n"
    "ds_read_b64_tr_b16 v[194:195], v223 offset:20480\n"
    "s_waitcnt lgkmcnt(9)\n"
    "v_mfma_f32_16x16x32_bf16 v[144:147], v[200:203], v[48:51], v[144:147]\n"
    "ds_read_b64_tr_b16 v[196:197], v220 offset:28672\n"
    "v_mfma_f32_16x16x32_bf16 v[148:151], v[200:203], v[112:115], v[148:151]\n"
    "ds_read_b64_tr_b16 v[198:199], v221 offset:28672\n"
    "s_waitcnt lgkmcnt(8)\n"
    "v_mfma_f32_16x16x32_bf16 v[152:155], v[204:207], v[48:51], v[152:155]\n"
    "ds_read_b64_tr_b16 v[200:201], v222 offset:28672\n"
    "v_mfma_f32_16x16x32_bf16 v[156:159], v[204:207], v[112:115], v[156:159]\n"
    "ds_read_b64_tr_b16 v[202:203], v223 offset:28672\n"
    "s_waitcnt lgkmcnt(8)\n"
    "v_mfma_f32_16x16x32_bf16 v[216:219], v[208:211], v[212:215], v[216:219]\n"
    "ds_read_b64_tr_b16 v[204:205], v224 offset:20480\n"
    "ds_read_b64_tr_b16 v[206:207], v225 offset:20480\n"
    "s_waitcnt vmcnt(4)\n"
    "s_barrier\n"
    "s_waitcnt lgkmcnt(8)\n"
    "v_mfma_f32_16x16x32_bf16 v[128:131], v[252:255], v[52:55], v[128:131]\n"
    "s_add_u32 m0, s73, 0x8000\n"
    "s_add_u32 s89, s76, 0xe0000\n"
    "buffer_load_dwordx4 v176, s[44:47], s89 offen lds\n"
    "ds_read_b64_tr_b16 v[208:209], v220 offset:49152\n"
    "ds_read_b128 v[56:59], v163 offset:32768\n"
    "v_cmp_eq_u32 vcc, s78, v169\n"
    "v_cmp_eq_u32 s[94:95], s79, v171\n"
    "s_nop 4\n"
    "s_and_b64 vcc, vcc, s[94:95]\n"
    "s_and_b64 vcc, vcc, exec\n"
    "s_cmp_eq_u64 vcc, exec\n"
    "s_cbranch_scc0 LB_slowstat_1\n"
    "LB_backstat_2:\n"
    "v_mfma_f32_16x16x32_bf16 v[132:135], v[252:255], v[116:119], v[132:135]\n"
    "ds_read_b64_tr_b16 v[210:211], v221 offset:49152\n"
    "v_lshrrev_b32 v238, 2, v237\n"
    "s_lshl_b32 s87, s68, 6\n"
    "v_add_u32 v238, s87, v238\n"
    "s_waitcnt lgkmcnt(9)\n"
    "v_mfma_f32_16x16x32_bf16 v[136:139], v[192:195], v[52:55], v[136:139]\n"
    "ds_read_b64_tr_b16 v[252:253], v222 offset:49152\n"
    "ds_read_b128 v[120:123], v163 offset:34816\n"
    "ds_write_b32 v238, v168 offset:27648\n"
    "ds_write_b32 v238, v170 offset:28224\n"
    "v_mfma_f32_16x16x32_bf16 v[140:143], v[192:195], v[116:119], v[140:143]\n"
    "s_add_u32 m0, s73, 0xa000\n"
    "s_nop 0\n"
    "buffer_load_dwordx4 v176, s[48:51], s89 offen lds\n"
    "ds_read_b64_tr_b16 v[254:255], v223 offset:49152\n"
    "s_waitcnt lgkmcnt(12)\n"
    "v_mfma_f32_16x16x32_bf16 v[144:147], v[196:199], v[52:55], v[144:147]\n"
    "ds_read_b64_tr_b16 v[192:193], v220 offset:57344\n"
    "ds_read_b128 v[60:63], v164 offset:32768\n"
    "v_mfma_f32_16x16x32_bf16 v[148:151], v[196:199], v[116:119], v[148:151]\n"
    "ds_read_b64_tr_b16 v[194:195], v221 offset:57344\n"
    "s_waitcnt lgkmcnt(13)\n"
    "v_mfma_f32_16x16x32_bf16 v[152:155], v[200:203], v[52:55], v[152:155]\n"
    "s_add_u32 m0, s73, 0xc000\n"
    "s_add_u32 s89, s76, 0x150000\n"
    "buffer_load_dwordx4 v176, s[44:47], s89 offen lds\n"
    "ds_read_b64_tr_b16 v[196:197], v222 offset:57344\n"
    "ds_read_b128 v[124:127], v164 offset:34816\n"
    "v_mfma_f32_16x16x32_bf16 v[156:159], v[200:203], v[116:119], v[156:159]\n"
    "ds_read_b64_tr_b16 v[198:199], v223 offset:57344\n"
    "s_waitcnt lgkmcnt(14)\n"
    "v_mfma_f32_16x16x32_bf16 v[216:219], v[204:207], v[212:215], v[216:219]\n"
    "ds_read_b64_tr_b16 v[200:201], v224 offset:49152\n"
    "ds_read_b64_tr_b16 v[202:203], v225 offset:49152\n"
    "s_waitcnt lgkmcnt(13)\n"
    "v_mfma_f32_16x16x32_bf16 v[128:131], v[208:211], v[56:59], v[128:131]\n"
    "s_add_u32 m0, s73, 0xe000\n"
    "s_nop 0\n"
    "buffer_load_dwordx4 v176, s[48:51], s89 offen lds\n"
    "ds_read_b64_tr_b16 v[204:205], v220 offset:53248\n"
    "s_waitcnt lgkmcnt(12)\n"
    "v_mfma_f32_16x16x32_bf16 v[132:135], v[208:211], v[120:123], v[132:135]\n"
    "ds_read_b64_tr_b16 v[206:207], v221 offset:53248\n"
    "s_waitcnt lgkmcnt(10)\n"
    "v_mfma_f32_16x16x32_bf16 v[136:139], v[252:255], v[56:59], v[136:139]\n"
    "ds_read_b64_tr_b16 v[208:209], v222 offset:53248\n"
    "v_mfma_f32_16x16x32_bf16 v[140:143], v[252:255], v[120:123], v[140:143]\n"
    "ds_read_b64_tr_b16 v[210:211], v223 offset:53248\n"
    "s_waitcnt lgkmcnt(9)\n"
    "v_mfma_f32_16x16x32_bf16 v[144:147], v[192:195], v[56:59], v[144:147]\n"
    "ds_read_b64_tr_b16 v[252:253], v220 offset:61440\n"
    "v_mfma_f32_16x16x32_bf16 v[148:151], v[192:195], v[120:123], v[148:151]\n"
    "ds_read_b64_tr_b16 v[254:255], v221 offset:61440\n"
    "s_waitcnt lgkmcnt(8)\n"
    "v_mfma_f32_16x16x32_bf16 v[152:155], v[196:199], v[56:59], v[152:155]\n"
    "ds_read_b64_tr_b16 v[192:193], v222 offset:61440\n"
    "v_mfma_f32_16x16x32_bf16 v[156:159], v[196:199], v[120:123], v[156:159]\n"
    "ds_read_b64_tr_b16 v[194:195], v223 offset:61440\n"
    "s_waitcnt lgkmcnt(8)\n"
    "v_mfma_f32_16x16x32_bf16 v[216:219], v[200:203], v[212:215], v[216:219]\n"
    "ds_read_b64_tr_b16 v[196:197], v224 offset:53248\n"
    "ds_read_b64_tr_b16 v[198:199], v225 offset:53248\n"
    "s_waitcnt vmcnt(6)\n"
    "s_barrier\n"
    "s_waitcnt lgkmcnt(8)\n"
    "v_mfma_f32_16x16x32_bf16 v[128:131], v[204:207], v[60:63], v[128:131]\n"
    "s_add_u32 m0, s73, 0x10000\n"
    "s_add_u32 s89, s75, 0x80\n"
    "buffer_load_dwordx4 v228, s[44:47], s89 offen lds\n"
    "ds_read_b64_tr_b16 v[200:201], v226 offset:0\n"
    "v_and_b32 v237, 15, v235\n"
    "v_mfma_f32_16x16x32_bf16 v[132:135], v[204:207], v[124:127], v[132:135]\n"
    "ds_read_b64_tr_b16 v[202:203], v226 offset:256\n"
    "v_lshlrev_b32 v237, 2, v237\n"
    "s_lshl_b32 s87, s69, 7\n"
    "v_add_u32 v237, s87, v237\n"
    "s_waitcnt lgkmcnt(8)\n"
    "v_mfma_f32_16x16x32_bf16 v[136:139], v[208:211], v[60:63], v[136:139]\n"
    "ds_read_b64_tr_b16 v[204:205], v226 offset:8192\n"
    "ds_read_b32 v231, v237 offset:27648\n"
    "ds_read_b32 v233, v237 offset:28224\n"
    "ds_read_b32 v232, v237 offset:27712\n"
    "ds_read_b32 v234, v237 offset:28288\n"
    "v_mfma_f32_16x16x32_bf16 v[140:143], v[208:211], v[124:127], v[140:143]\n"
    "s_add_u32 m0, s73, 0x12000\n"
    "s_nop 0\n"
    "buffer_load_dwordx4 v228, s[48:51], s89 offen lds\n"
    "ds_read_b64_tr_b16 v[206:207], v226 offset:8448\n"
    "s_waitcnt lgkmcnt(12)\n"
    "v_mfma_f32_16x16x32_bf16 v[144:147], v[252:255], v[60:63], v[144:147]\n"
    "ds_read_b64_tr_b16 v[208:209], v226 offset:2048\n"
    "v_mfma_f32_16x16x32_bf16 v[148:151], v[252:255], v[124:127], v[148:151]\n"
    "ds_read_b64_tr_b16 v[210:211], v226 offset:2304\n"
    "s_waitcnt lgkmcnt(12)\n"
    "v_mfma_f32_16x16x32_bf16 v[152:155], v[192:195], v[60:63], v[152:155]\n"
    "s_add_u32 m0, s73, 0x14000\n"
    "s_add_u32 s89, s75, 0x38080\n"
    "buffer_load_dwordx4 v228, s[44:47], s89 offen lds\n"
    "ds_read_b64_tr_b16 v[252:253], v226 offset:10240\n"
    "v_mfma_f32_16x16x32_bf16 v[156:159], v[192:195], v[124:127], v[156:159]\n"
    "ds_read_b64_tr_b16 v[254:255], v226 offset:10496\n"
    "s_waitcnt lgkmcnt(12)\n"
    "v_mfma_f32_16x16x32_bf16 v[216:219], v[196:199], v[212:215], v[216:219]\n"
    "ds_read_b64_tr_b16 v[192:193], v227 offset:0\n"
    "ds_read_b64_tr_b16 v[194:195], v227 offset:256\n"
    "s_lshl_b32 s87, s68, 7\n"
    "v_lshrrev_b32 v237, 4, v235\n"
    "v_lshlrev_b32 v237, 4, v237\n"
    "s_waitcnt lgkmcnt(12)\n"
    "v_mfma_f32_16x16x32_bf16 v[160:163], v[200:203], v[0:3], 0\n"
    "s_add_u32 m0, s73, 0x16000\n"
    "s_nop 0\n"
    "buffer_load_dwordx4 v228, s[48:51], s89 offen lds\n"
    "ds_read_b64_tr_b16 v[196:197], v226 offset:4096\n"
    "v_add_u32 v237, s87, v237\n"
    "v_mfma_f32_16x16x32_bf16 v[164:167], v[200:203], v[64:67], 0\n"
    "ds_read_b64_tr_b16 v[198:199], v226 offset:4352\n"
    "s_mov_b32 exec_lo, 0x10001\n"
    "s_mov_b32 exec_hi, 0x10001\n"
    "ds_write_b128 v237, v[216:219] offset:24576\n"
    "s_mov_b64 exec, -1\n"
    "s_waitcnt lgkmcnt(9)\n"
    "s_nop 2\n"
    "v_mfma_f32_16x16x32_bf16 v[168:171], v[204:207], v[0:3], 0\n"
    "ds_read_b64_tr_b16 v[200:201], v226 offset:12288\n"
    "v_mfma_f32_16x16x32_bf16 v[172:175], v[204:207], v[64:67], 0\n"
    "ds_read_b64_tr_b16 v[202:203], v226 offset:12544\n"
    "s_waitcnt vmcnt(8) lgkmcnt(2)\n"
    "s_barrier\n"
    "v_mfma_f32_16x16x32_bf16 v[160:163], v[208:211], v[4:7], v[160:163]\n"
    "s_add_u32 m0, s73, 0x18000\n"
    "s_add_u32 s89, s75, 0x70080\n"
    "buffer_load_dwordx4 v228, s[44:47], s89 offen lds\n"
    "ds_read_b64_tr_b16 v[204:205], v226 offset:6144\n"
    "ds_read_b128 v[238:241], v236 offset:24576\n"
    "ds_read_b128 v[242:245], v236 offset:25088\n"
    "v_mul_f32 v250, 0xbfb8aa3b, v233\n"
    "v_mfma_f32_16x16x32_bf16 v[164:167], v[208:211], v[68:71], v[164:167]\n"
    "ds_read_b64_tr_b16 v[206:207], v226 offset:6400\n"
    "v_mul_f32 v251, v233, v233\n"
    "v_mfma_f32_16x16x32_bf16 v[168:171], v[252:255], v[4:7], v[168:171]\n"
    "ds_read_b64_tr_b16 v[208:209], v226 offset:14336\n"
    "v_rcp_f32 v251, v251\n"
    "v_mfma_f32_16x16x32_bf16 v[172:175], v[252:255], v[68:71], v[172:175]\n"
    "s_add_u32 m0, s73, 0x1a000\n"
    "s_nop 0\n"
    "buffer_load_dwordx4 v228, s[48:51], s89 offen lds\n"
    "ds_read_b64_tr_b16 v[210:211], v226 offset:14592\n"
    "v_mfma_f32_16x16x32_bf16 v[216:219], v[192:195], v[212:215], 0\n"
    "ds_read_b64_tr_b16 v[252:253], v227 offset:4096\n"
    "ds_read_b64_tr_b16 v[254:255], v227 offset:4352\n"
    "v_mfma_f32_16x16x32_bf16 v[160:163], v[196:199], v[8:11], v[160:163]\n"
    "ds_read_b64_tr_b16 v[192:193], v226 offset:16384\n"
    "s_waitcnt lgkmcnt(7)\n"
    "v_fma_f32 v128, -v231, v238, v128\n"
    "v_fma_f32 v129, -v231, v239, v129\n"
    "v_mfma_f32_16x16x32_bf16 v[164:167], v[196:199], v[72:75], v[164:167]\n"
    "s_add_u32 m0, s73, 0x1c000\n"
    "s_add_u32 s89, s75, 0xa8080\n"
    "buffer_load_dwordx4 v228, s[44:47], s89 offen lds\n"
    "ds_read_b64_tr_b16 v[194:195], v226 offset:16640\n"
    "v_fma_f32 v130, -v231, v240, v130\n"
    "v_mfma_f32_16x16x32_bf16 v[168:171], v[200:203], v[8:11], v[168:171]\n"
    "ds_read_b64_tr_b16 v[196:197], v226 offset:24576\n"
    "v_fma_f32 v131, -v231, v241, v131\n"
    "s_waitcnt lgkmcnt(8)\n"
    "v_fma_f32 v144, -v231, v242, v144\n"
    "v_mfma_f32_16x16x32_bf16 v[172:175], v[200:203], v[72:75], v[172:175]\n"
    "ds_read_b64_tr_b16 v[198:199], v226 offset:24832\n"
    "v_fma_f32 v145, -v231, v243, v145\n"
    "v_fma_f32 v146, -v231, v244, v146\n"
    "s_waitcnt lgkmcnt(8)\n"
    "v_mfma_f32_16x16x32_bf16 v[160:163], v[204:207], v[12:15], v[160:163]\n"
    "s_add_u32 m0, s73, 0x1e000\n"
    "s_nop 0\n"
    "buffer_load_dwordx4 v228, s[48:51], s89 offen lds\n"
    "ds_read_b64_tr_b16 v[200:201], v226 offset:18432\n"
    "v_fma_f32 v147, -v231, v245, v147\n"
    "v_mfma_f32_16x16x32_bf16 v[164:167], v[204:207], v[76:79], v[164:167]\n"
    "ds_read_b64_tr_b16 v[202:203], v226 offset:18688\n"
    "ds_read_b128 v[238:241], v236 offset:24704\n"
    "ds_read_b128 v[242:245], v236 offset:25216\n"
    "v_mul_f32 v246, v128, v250\n"
    "v_mul_f32 v247, v129, v250\n"
    "s_waitcnt lgkmcnt(10)\n"
    "v_mfma_f32_16x16x32_bf16 v[168:171], v[208:211], v[12:15], v[168:171]\n"
    "ds_read_b64_tr_b16 v[204:205], v226 offset:26624\n"
    "v_mul_f32 v248, v130, v250\n"
    "v_mul_f32 v249, v131, v250\n"
    "v_mfma_f32_16x16x32_bf16 v[172:175], v[208:211], v[76:79], v[172:175]\n"
    "ds_read_b64_tr_b16 v[206:207], v226 offset:26880\n"
    "v_exp_f32 v246, v246\n"
    "s_waitcnt lgkmcnt(10)\n"
    "v_mfma_f32_16x16x32_bf16 v[216:219], v[252:255], v[212:215], v[216:219]\n"
    "ds_read_b64_tr_b16 v[208:209], v227 offset:16384\n"
    "ds_read_b64_tr_b16 v[210:211], v227 offset:16640\n"
    "v_exp_f32 v247, v247\n"
    "s_waitcnt lgkmcnt(10)\n"
    "v_mfma_f32_16x16x32_bf16 v[160:163], v[192:195], v[16:19], v[160:163]\n"
    "ds_read_b64_tr_b16 v[252:253], v226 offset:20480\n"
    "v_exp_f32 v248, v248\n"
    "v_mfma_f32_16x16x32_bf16 v[164:167], v[192:195], v[80:83], v[164:167]\n"
    "ds_read_b64_tr_b16 v[254:255], v226 offset:20736\n"
    "v_exp_f32 v249, v249\n"
    "s_waitcnt lgkmcnt(10)\n"
    "v_mfma_f32_16x16x32_bf16 v[168:171], v[196:199], v[16:19], v[168:171]\n"
    "ds_read_b64_tr_b16 v[192:193], v226 offset:28672\n"
    "v_mul_f32 v128, v128, v144\n"
    "v_mul_f32 v129, v129, v145\n"
    "v_mfma_f32_16x16x32_bf16 v[172:175], v[196:199], v[80:83], v[172:175]\n"
    "ds_read_b64_tr_b16 v[194:195], v226 offset:28928\n"
    "v_mul_f32 v130, v130, v146\n"
    "v_mul_f32 v131, v131, v147\n"
    "s_waitcnt vmcnt(10)\n"
    "s_barrier\n"
    "s_waitcnt lgkmcnt(10)\n"
    "v_mfma_f32_16x16x32_bf16 v[160:163], v[200:203], v[20:23], v[160:163]\n"
    "s_add_u32 m0, s73, 0\n"
    "s_add_u32 s89, s75, 0xe0080\n"
    "buffer_load_dwordx4 v228, s[44:47], s89 offen lds\n"
    "ds_read_b64_tr_b16 v[196:197], v226 offset:22528\n"
    "v_fma_f32 v246, v246, v251, v251\n"
    "v_mfma_f32_16x16x32_bf16 v[164:167], v[200:203], v[84:87], v[164:167]\n"
    "ds_read_b64_tr_b16 v[198:199], v226 offset:22784\n"
    "v_fma_f32 v247, v247, v251, v251\n"
    "v_fma_f32 v248, v248, v251, v251\n"
    "s_waitcnt lgkmcnt(8)\n"
    "v_mfma_f32_16x16x32_bf16 v[168:171], v[204:207], v[20:23], v[168:171]\n"
    "ds_read_b64_tr_b16 v[200:201], v226 offset:30720\n"
    "v_fma_f32 v249, v249, v251, v251\n"
    "v_mfma_f32_16x16x32_bf16 v[172:175], v[204:207], v[84:87], v[172:175]\n"
    "s_add_u32 m0, s73, 0x2000\n"
    "s_nop 0\n"
    "buffer_load_dwordx4 v228, s[48:51], s89 offen lds\n"
    "ds_read_b64_tr_b16 v[202:203], v226 offset:30976\n"
    "s_waitcnt lgkmcnt(8)\n"
    "v_mfma_f32_16x16x32_bf16 v[216:219], v[208:211], v[212:215], v[216:219]\n"
    "ds_read_b64_tr_b16 v[204:205], v227 offset:20480\n"
    "ds_read_b64_tr_b16 v[206:207], v227 offset:20736\n"
    "v_rcp_f32 v246, v246\n"
    "s_waitcnt lgkmcnt(8)\n"
    "v_mfma_f32_16x16x32_bf16 v[160:163], v[252:255], v[24:27], v[160:163]\n"
    "ds_read_b64_tr_b16 v[208:209], v226 offset:32768\n"
    "v_rcp_f32 v247, v247\n"
    "v_mfma_f32_16x16x32_bf16 v[164:167], v[252:255], v[88:91], v[164:167]\n"
    "ds_read_b64_tr_b16 v[210:211], v226 offset:33024\n"
    "v_rcp_f32 v248, v248\n"
    "s_waitcnt lgkmcnt(8)\n"
    "v_mfma_f32_16x16x32_bf16 v[168:171], v[192:195], v[24:27], v[168:171]\n"
    "ds_read_b64_tr_b16 v[252:253], v226 offset:40960\n"
    "v_rcp_f32 v249, v249\n"
    "v_mfma_f32_16x16x32_bf16 v[172:175], v[192:195], v[88:91], v[172:175]\n"
    "ds_read_b64_tr_b16 v[254:255], v226 offset:41216\n"
    "v_mul_f32 v128, v128, v246\n"
    "v_mul_f32 v129, v129, v247\n"
    "s_waitcnt lgkmcnt(8)\n"
    "v_mfma_f32_16x16x32_bf16 v[160:163], v[196:199], v[28:31], v[160:163]\n"
    "ds_read_b64_tr_b16 v[192:193], v226 offset:34816\n"
    "v_mul_f32 v130, v130, v248\n"
    "v_mul_f32 v131, v131, v249\n"
    "v_mfma_f32_16x16x32_bf16 v[164:167], v[196:199], v[92:95], v[164:167]\n"
    "ds_read_b64_tr_b16 v[194:195], v226 offset:35072\n"
    "v_cvt_pk_bf16_f32 v144, v128, v129\n"
    "v_cvt_pk_bf16_f32 v145, v130, v131\n"
    "s_waitcnt lgkmcnt(8)\n"
    "v_mfma_f32_16x16x32_bf16 v[168:171], v[200:203], v[28:31], v[168:171]\n"
    "ds_read_b64_tr_b16 v[196:197], v226 offset:43008\n"
    "v_fma_f32 v136, -v231, v238, v136\n"
    "v_fma_f32 v137, -v231, v239, v137\n"
    "v_mfma_f32_16x16x32_bf16 v[172:175], v[200:203], v[92:95], v[172:175]\n"
    "ds_read_b64_tr_b16 v[198:199], v226 offset:43264\n"
    "v_fma_f32 v138, -v231, v240, v138\n"
    "v_fma_f32 v139, -v231, v241, v139\n"
    "s_waitcnt lgkmcnt(8)\n"
    "v_mfma_f32_16x16x32_bf16 v[216:219], v[204:207], v[212:215], v[216:219]\n"
    "ds_read_b64_tr_b16 v[200:201], v227 offset:32768\n"
    "ds_read_b64_tr_b16 v[202:203], v227 offset:33024\n"
    "v_fma_f32 v152, -v231, v242, v152\n"
    "v_fma_f32 v153, -v231, v243, v153\n"
    "s_waitcnt lgkmcnt(8)\n"
    "v_mfma_f32_16x16x32_bf16 v[160:163], v[208:211], v[32:35], v[160:163]\n"
    "ds_read_b64_tr_b16 v[204:205], v226 offset:36864\n"
    "v_fma_f32 v154, -v231, v244, v154\n"
    "v_fma_f32 v155, -v231, v245, v155\n"
    "v_mfma_f32_16x16x32_bf16 v[164:167], v[208:211], v[96:99], v[164:167]\n"
    "ds_read_b64_tr_b16 v[206:207], v226 offset:37120\n"
    "v_mul_f32 v246, v136, v250\n"
    "v_mul_f32 v247, v137, v250\n"
    "s_waitcnt lgkmcnt(8)\n"
    "v_mfma_f32_16x16x32_bf16 v[168:171], v[252:255], v[32:35], v[168:171]\n"
    "ds_read_b64_tr_b16 v[208:209], v226 offset:45056\n"
    "v_mul_f32 v248, v138, v250\n"
    "v_mul_f32 v249, v139, v250\n"
    "v_mfma_f32_16x16x32_bf16 v[172:175], v[252:255], v[96:99], v[172:175]\n"
    "ds_read_b64_tr_b16 v[210:211], v226 offset:45312\n"
    "v_exp_f32 v246, v246\n"
    "s_waitcnt vmcnt(10)\n"
    "s_barrier\n"
    "s_waitcnt lgkmcnt(8)\n"
    "v_mfma_f32_16x16x32_bf16 v[160:163], v[192:195], v[36:39], v[160:163]\n"
    "s_add_u32 m0, s73, 0x4000\n"
    "s_add_u32 s89, s75, 0x118080\n"
    "buffer_load_dwordx4 v228, s[44:47], s89 offen lds\n"
    "ds_read_b64_tr_b16 v[252:253], v226 offset:38912\n"
    "v_mfma_f32_16x16x32_bf16 v[164:167], v[192:195], v[100:103], v[164:167]\n"
    "ds_read_b64_tr_b16 v[254:255], v226 offset:39168\n"
    "v_exp_f32 v247, v247\n"
    "s_waitcnt lgkmcnt(8)\n"
    "v_mfma_f32_16x16x32_bf16 v[168:171], v[196:199], v[36:39], v[168:171]\n"
    "ds_read_b64_tr_b16 v[192:193], v226 offset:47104\n"
    "v_exp_f32 v248, v248\n"
    "v_mfma_f32_16x16x32_bf16 v[172:175], v[196:199], v[100:103], v[172:175]\n"
    "s_add_u32 m0, s73, 0x6000\n"
    "s_nop 0\n"
    "buffer_load_dwordx4 v228, s[48:51], s89 offen lds\n"
    "ds_read_b64_tr_b16 v[194:195], v226 offset:47360\n"
    "s_waitcnt lgkmcnt(8)\n"
    "v_mfma_f32_16x16x32_bf16 v[216:219], v[200:203], v[212:215], v[216:219]\n"
    "ds_read_b64_tr_b16 v[196:197], v227 offset:36864\n"
    "ds_read_b64_tr_b16 v[198:199], v227 offset:37120\n"
    "v_exp_f32 v249, v249\n"
    "s_waitcnt lgkmcnt(8)\n"
    "v_mfma_f32_16x16x32_bf16 v[160:163], v[204:207], v[40:43], v[160:163]\n"
    "ds_read_b64_tr_b16 v[200:201], v226 offset:49152\n"
    "v_mul_f32 v136, v136, v152\n"
    "v_mul_f32 v137, v137, v153\n"
    "v_mfma_f32_16x16x32_bf16 v[164:167], v[204:207], v[104:107], v[164:167]\n"
    "ds_read_b64_tr_b16 v[202:203], v226 offset:49408\n"
    "v_mul_f32 v138, v138, v154\n"
    "v_mul_f32 v139, v139, v155\n"
    "s_waitcnt lgkmcnt(8)\n"
    "v_mfma_f32_16x16x32_bf16 v[168:171], v[208:211], v[40:43], v[168:171]\n"
    "ds_read_b64_tr_b16 v[204:205], v226 offset:57344\n"
    "v_fma_f32 v246, v246, v251, v251\n"
    "v_fma_f32 v247, v247, v251, v251\n"
    "v_mfma_f32_16x16x32_bf16 v[172:175], v[208:211], v[104:107], v[172:175]\n"
    "ds_read_b64_tr_b16 v[206:207], v226 offset:57600\n"
    "v_fma_f32 v248, v248, v251, v251\n"
    "v_fma_f32 v249, v249, v251, v251\n"
    "s_waitcnt lgkmcnt(8)\n"
    "v_mfma_f32_16x16x32_bf16 v[160:163], v[252:255], v[44:47], v[160:163]\n"
    "ds_read_b64_tr_b16 v[208:209], v226 offset:51200\n"
    "v_rcp_f32 v246, v246\n"
    "v_mfma_f32_16x16x32_bf16 v[164:167], v[252:255], v[108:111], v[164:167]\n"
    "ds_read_b64_tr_b16 v[210:211], v226 offset:51456\n"
    "v_rcp_f32 v247, v247\n"
    "s_waitcnt lgkmcnt(8)\n"
    "v_mfma_f32_16x16x32_bf16 v[168:171], v[192:195], v[44:47], v[168:171]\n"
    "ds_read_b64_tr_b16 v[252:253], v226 offset:59392\n"
    "v_rcp_f32 v248, v248\n"
    "v_mfma_f32_16x16x32_bf16 v[172:175], v[192:195], v[108:111], v[172:175]\n"
    "ds_read_b64_tr_b16 v[254:255], v226 offset:59648\n"
    "v_rcp_f32 v249, v249\n"
    "s_waitcnt lgkmcnt(8)\n"
    "v_mfma_f32_16x16x32_bf16 v[216:219], v[196:199], v[212:215], v[216:219]\n"
    "ds_read_b64_tr_b16 v[192:193], v227 offset:49152\n"
    "ds_read_b64_tr_b16 v[194:195], v227 offset:49408\n"
    "v_mul_f32 v136, v136, v246\n"
    "v_mul_f32 v137, v137, v247\n"
    "s_waitcnt lgkmcnt(8)\n"
    "v_mfma_f32_16x16x32_bf16 v[160:163], v[200:203], v[48:51], v[160:163]\n"
    "ds_read_b64_tr_b16 v[196:197], v226 offset:53248\n"
    "v_mul_f32 v138, v138, v248\n"
    "v_mul_f32 v139, v139, v249\n"
    "v_mfma_f32_16x16x32_bf16 v[164:167], v[200:203], v[112:115], v[164:167]\n"
    "ds_read_b64_tr_b16 v[198:199], v226 offset:53504\n"
    "v_cvt_pk_bf16_f32 v146, v136, v137\n"
    "v_cvt_pk_bf16_f32 v147, v138, v139\n"
    "s_waitcnt lgkmcnt(8)\n"
    "v_mfma_f32_16x16x32_bf16 v[168:171], v[204:207], v[48:51], v[168:171]\n"
    "ds_read_b64_tr_b16 v[200:201], v226 offset:61440\n"
    "s_add_u32 s87, s98, 0\n"
    "buffer_store_dwordx4 v[144:147], v230, s[52:55], s87 offen sc1\n"
    "ds_read_b128 v[238:241], v236 offset:24576\n"
    "ds_read_b128 v[242:245], v236 offset:25088\n"
    "v_mul_f32 v250, 0xbfb8aa3b, v234\n"
    "v_mul_f32 v251, v234, v234\n"
    "v_mfma_f32_16x16x32_bf16 v[172:175], v[204:207], v[112:115], v[172:175]\n"
    "ds_read_b64_tr_b16 v[202:203], v226 offset:61696\n"
    "v_rcp_f32 v251, v251\n"
    "s_waitcnt vmcnt(11)\n"
    "s_barrier\n"
    "s_waitcnt lgkmcnt(10)\n"
    "v_mfma_f32_16x16x32_bf16 v[160:163], v[208:211], v[52:55], v[160:163]\n"
    "s_add_u32 m0, s73, 0x8000\n"
    "s_add_u32 s89, s75, 0x150080\n"
    "buffer_load_dwordx4 v228, s[44:47], s89 offen lds\n"
    "ds_read_b64_tr_b16 v[204:205], v226 offset:55296\n"
    "v_mfma_f32_16x16x32_bf16 v[164:167], v[208:211], v[116:119], v[164:167]\n"
    "ds_read_b64_tr_b16 v[206:207], v226 offset:55552\n"
    "s_waitcnt lgkmcnt(10)\n"
    "v_mfma_f32_16x16x32_bf16 v[168:171], v[252:255], v[52:55], v[168:171]\n"
    "ds_read_b64_tr_b16 v[208:209], v226 offset:63488\n"
    "s_waitcnt lgkmcnt(5)\n"
    "v_fma_f32 v132, -v232, v238, v132\n"
    "v_fma_f32 v133, -v232, v239, v133\n"
    "v_mfma_f32_16x16x32_bf16 v[172:175], v[252:255], v[116:119], v[172:175]\n"
    "s_add_u32 m0, s73, 0xa000\n"
    "s_nop 0\n"
    "buffer_load_dwordx4 v228, s[48:51], s89 offen lds\n"
    "ds_read_b64_tr_b16 v[210:211], v226 offset:63744\n"
    "v_fma_f32 v134, -v232, v240, v134\n"
    "v_mfma_f32_16x16x32_bf16 v[216:219], v[192:195], v[212:215], v[216:219]\n"
    "ds_read_b64_tr_b16 v[252:253], v227 offset:53248\n"
    "ds_read_b64_tr_b16 v[254:255], v227 offset:53504\n"
    "v_fma_f32 v135, -v232, v241, v135\n"
    "s_waitcnt lgkmcnt(7)\n"
    "v_fma_f32 v148, -v232, v242, v148\n"
    "v_mfma_f32_16x16x32_bf16 v[160:163], v[196:199], v[56:59], v[160:163]\n"
    "ds_read_b64_tr_b16 v[192:193], v220 offset:0\n"
    "v_fma_f32 v149, -v232, v243, v149\n"
    "v_fma_f32 v150, -v232, v244, v150\n"
    "v_mfma_f32_16x16x32_bf16 v[164:167], v[196:199], v[120:123], v[164:167]\n"
    "ds_read_b64_tr_b16 v[194:195], v221 offset:0\n"
    "v_fma_f32 v151, -v232, v245, v151\n"
    "ds_read_b128 v[238:241], v236 offset:24704\n"
    "ds_read_b128 v[242:245], v236 offset:25216\n"
    "v_mul_f32 v246, v132, v250\n"
    "s_waitcnt lgkmcnt(10)\n"
    "v_mfma_f32_16x16x32_bf16 v[168:171], v[200:203], v[56:59], v[168:171]\n"
    "ds_read_b64_tr_b16 v[196:197], v222 offset:0\n"
    "v_mul_f32 v247, v133, v250\n"
    "v_mul_f32 v248, v134, v250\n"
    "v_mfma_f32_16x16x32_bf16 v[172:175], v[200:203], v[120:123], v[172:175]\n"
    "ds_read_b64_tr_b16 v[198:199], v223 offset:0\n"
    "v_mul_f32 v249, v135, v250\n"
    "s_waitcnt lgkmcnt(10)\n"
    "v_mfma_f32_16x16x32_bf16 v[160:163], v[204:207], v[60:63], v[160:163]\n"
    "ds_read_b64_tr_b16 v[200:201], v220 offset:8192\n"
    "v_exp_f32 v246, v246\n"
    "v_mfma_f32_16x16x32_bf16 v[164:167], v[204:207], v[124:127], v[164:167]\n"
    "ds_read_b64_tr_b16 v[202:203], v221 offset:8192\n"
    "v_exp_f32 v247, v247\n"
    "s_waitcnt lgkmcnt(10)\n"
    "v_mfma_f32_16x16x32_bf16 v[168:171], v[208:211], v[60:63], v[168:171]\n"
    "ds_read_b64_tr_b16 v[204:205], v222 offset:8192\n"
    "v_exp_f32 v248, v248\n"
    "v_mfma_f32_16x16x32_bf16 v[172:175], v[208:211], v[124:127], v[172:175]\n"
    "ds_read_b64_tr_b16 v[206:207], v223 offset:8192\n"
    "v_exp_f32 v249, v249\n"
    "s_waitcnt lgkmcnt(10)\n"
    "v_mfma_f32_16x16x32_bf16 v[216:219], v[252:255], v[212:215], v[216:219]\n"
    "ds_read_b64_tr_b16 v[208:209], v220 offset:4096\n"
    "ds_read_b64_tr_b16 v[210:211], v221 offset:4096\n"
    "s_lshl_b32 s87, s69, 7\n"
    "s_lshl_b32 s88, s70, 6\n"
    "s_add_u32 s87, s87, s88\n"
    "v_lshrrev_b32 v237, 4, v235\n"
    "v_lshlrev_b32 v237, 4, v237\n"
    "v_add_u32 v237, s87, v237\n"
    "s_waitcnt lgkmcnt(10)\n"
    "v_mfma_f32_16x16x32_bf16 v[128:131], v[192:195], v[0:3], 0\n"
    "ds_read_b64_tr_b16 v[252:253], v222 offset:4096\n"
    "s_mov_b32 exec_lo, 0x10001\n"
    "s_mov_b32 exec_hi, 0x10001\n"
    "ds_write_b128 v237, v[216:219] offset:25600\n"
    "s_mov_b64 exec, -1\n"
    "v_mul_f32 v132, v132, v148\n"
    "v_mul_f32 v133, v133, v149\n"
    "s_nop 1\n"
    "v_mfma_f32_16x16x32_bf16 v[176:179], v[192:195], v[64:67], 0\n"
    "ds_read_b64_tr_b16 v[254:255], v223 offset:4096\n"
    "v_mul_f32 v134, v134, v150\n"
    "v_mul_f32 v135, v135, v151\n"
    "s_waitcnt lgkmcnt(9)\n"
    "v_mfma_f32_16x16x32_bf16 v[136:139], v[196:199], v[0:3], 0\n"
    "ds_read_b64_tr_b16 v[192:193], v220 offset:12288\n"
    "v_fma_f32 v246, v246, v251, v251\n"
    "v_fma_f32 v247, v247, v251, v251\n"
    "v_mfma_f32_16x16x32_bf16 v[180:183], v[196:199], v[64:67], 0\n"
    "ds_read_b64_tr_b16 v[194:195], v221 offset:12288\n"
    "v_fma_f32 v248, v248, v251, v251\n"
    "v_fma_f32 v249, v249, v251, v251\n"
    "s_waitcnt lgkmcnt(9)\n"
    "v_mfma_f32_16x16x32_bf16 v[144:147], v[200:203], v[0:3], 0\n"
    "ds_read_b64_tr_b16 v[196:197], v222 offset:12288\n"
    "v_rcp_f32 v246, v246\n"
    "v_mfma_f32_16x16x32_bf16 v[184:187], v[200:203], v[64:67], 0\n"
    "ds_read_b64_tr_b16 v[198:199], v223 offset:12288\n"
    "v_rcp_f32 v247, v247\n"
    "s_waitcnt lgkmcnt(9)\n"
    "v_mfma_f32_16x16x32_bf16 v[152:155], v[204:207], v[0:3], 0\n"
    "ds_read_b64_tr_b16 v[200:201], v224 offset:0\n"
    "v_rcp_f32 v248, v248\n"
    "v_mfma_f32_16x16x32_bf16 v[188:191], v[204:207], v[64:67], 0\n"
    "ds_read_b64_tr_b16 v[202:203], v225 offset:0\n"
    "v_rcp_f32 v249, v249\n"
    "s_waitcnt vmcnt(11) lgkmcnt(7)\n"
    "s_barrier\n"
    "v_mfma_f32_16x16x32_bf16 v[128:131], v[208:211], v[4:7], v[128:131]\n"
    "s_add_u32 m0, s73, 0xc000\n"
    "s_add_u32 s89, s75, 0x188080\n"
    "buffer_load_dwordx4 v228, s[44:47], s89 offen lds\n"
    "ds_read_b64_tr_b16 v[204:205], v224 offset:4096\n"
    "v_mul_f32 v132, v132, v246\n"
    "v_mfma_f32_16x16x32_bf16 v[176:179], v[208:211], v[68:71], v[176:179]\n"
    "ds_read_b64_tr_b16 v[206:207], v225 offset:4096\n"
    "v_mul_f32 v133, v133, v247\n"
    "v_mul_f32 v134, v134, v248\n"
    "s_waitcnt lgkmcnt(8)\n"
    "v_mfma_f32_16x16x32_bf16 v[136:139], v[252:255], v[4:7], v[136:139]\n"
    "ds_read_b64_tr_b16 v[208:209], v220 offset:16384\n"
    "v_mul_f32 v135, v135, v249\n"
    "v_cvt_pk_bf16_f32 v148, v132, v133\n"
    "v_mfma_f32_16x16x32_bf16 v[180:183], v[252:255], v[68:71], v[180:183]\n"
    "s_add_u32 m0, s73, 0xe000\n"
    "s_nop 0\n"
    "buffer_load_dwordx4 v228, s[48:51], s89 offen lds\n"
    "ds_read_b64_tr_b16 v[210:211], v221 offset:16384\n"
    "v_cvt_pk_bf16_f32 v149, v134, v135\n"
    "s_waitcnt lgkmcnt(8)\n"
    "v_mfma_f32_16x16x32_bf16 v[144:147], v[192:195], v[4:7], v[144:147]\n"
    "ds_read_b64_tr_b16 v[252:253], v222 offset:16384\n"
    "v_fma_f32 v140, -v232, v238, v140\n"
    "v_fma_f32 v141, -v232, v239, v141\n"
    "v_mfma_f32_16x16x32_bf16 v[184:187], v[192:195], v[68:71], v[184:187]\n"
    "ds_read_b64_tr_b16 v[254:255], v223 offset:16384\n"
    "v_fma_f32 v142, -v232, v240, v142\n"
    "v_fma_f32 v143, -v232, v241, v143\n"
    "s_waitcnt lgkmcnt(8)\n"
    "v_mfma_f32_16x16x32_bf16 v[152:155], v[196:199], v[4:7], v[152:155]\n"
    "ds_read_b64_tr_b16 v[192:193], v220 offset:24576\n"
    "v_fma_f32 v156, -v232, v242, v156\n"
    "v_fma_f32 v157, -v232, v243, v157\n"
    "v_mfma_f32_16x16x32_bf16 v[188:191], v[196:199], v[68:71], v[188:191]\n"
    "ds_read_b64_tr_b16 v[194:195], v221 offset:24576\n"
    "v_fma_f32 v158, -v232, v244, v158\n"
    "v_fma_f32 v159, -v232, v245, v159\n"
    "s_waitcnt lgkmcnt(8)\n"
    "v_mfma_f32_16x16x32_bf16 v[216:219], v[200:203], v[212:215], 0\n"
    "ds_read_b64_tr_b16 v[196:197], v222 offset:24576\n"
    "ds_read_b64_tr_b16 v[198:199], v223 offset:24576\n"
    "v_mul_f32 v246, v140, v250\n"
    "v_mul_f32 v247, v141, v250\n"
    "s_waitcnt lgkmcnt(8)\n"
    "v_mfma_f32_16x16x32_bf16 v[216:219], v[204:207], v[212:215], v[216:219]\n"
    "ds_read_b64_tr_b16 v[200:201], v224 offset:16384\n"
    "ds_read_b64_tr_b16 v[202:203], v225 offset:16384\n"
    "v_mul_f32 v248, v142, v250\n"
    "v_mul_f32 v249, v143, v250\n"
    "s_waitcnt lgkmcnt(8)\n"
    "v_mfma_f32_16x16x32_bf16 v[128:131], v[208:211], v[8:11], v[128:131]\n"
    "ds_read_b64_tr_b16 v[204:205], v220 offset:20480\n"
    "v_exp_f32 v246, v246\n"
    "v_mfma_f32_16x16x32_bf16 v[176:179], v[208:211], v[72:75], v[176:179]\n"
    "ds_read_b64_tr_b16 v[206:207], v221 offset:20480\n"
    "v_exp_f32 v247, v247\n"
    "s_waitcnt lgkmcnt(8)\n"
    "v_mfma_f32_16x16x32_bf16 v[136:139], v[252:255], v[8:11], v[136:139]\n"
    "ds_read_b64_tr_b16 v[208:209], v222 offset:20480\n"
    "v_exp_f32 v248, v248\n"
    "v_mfma_f32_16x16x32_bf16 v[180:183], v[252:255], v[72:75], v[180:183]\n"
    "ds_read_b64_tr_b16 v[210:211], v223 offset:20480\n"
    "v_exp_f32 v249, v249\n"
    "s_waitcnt lgkmcnt(8)\n"
    "v_mfma_f32_16x16x32_bf16 v[144:147], v[192:195], v[8:11], v[144:147]\n"
    "ds_read_b64_tr_b16 v[252:253], v220 offset:28672\n"
    "v_mul_f32 v140, v140, v156\n"
    "v_mul_f32 v141, v141, v157\n"
    "v_mfma_f32_16x16x32_bf16 v[184:187], v[192:195], v[72:75], v[184:187]\n"
    "ds_read_b64_tr_b16 v[254:255], v221 offset:28672\n"
    "v_mul_f32 v142, v142, v158\n"
    "v_mul_f32 v143, v143, v159\n"
    "s_waitcnt lgkmcnt(8)\n"
    "v_mfma_f32_16x16x32_bf16 v[152:155], v[196:199], v[8:11], v[152:155]\n"
    "ds_read_b64_tr_b16 v[192:193], v222 offset:28672\n"
    "v_fma_f32 v246, v246, v251, v251\n"
    "v_fma_f32 v247, v247, v251, v251\n"
    "v_mfma_f32_16x16x32_bf16 v[188:191], v[196:199], v[72:75], v[188:191]\n"
    "ds_read_b64_tr_b16 v[194:195], v223 offset:28672\n"
    "v_fma_f32 v248, v248, v251, v251\n"
    "v_fma_f32 v249, v249, v251, v251\n"
    "s_waitcnt lgkmcnt(8)\n"
    "v_mfma_f32_16x16x32_bf16 v[216:219], v[200:203], v[212:215], v[216:219]\n"
    "ds_read_b64_tr_b16 v[196:197], v224 offset:20480\n"
    "ds_read_b64_tr_b16 v[198:199], v225 offset:20480\n"
    "v_rcp_f32 v246, v246\n"
    "s_waitcnt vmcnt(11)\n"
    "s_barrier\n"
    "s_waitcnt lgkmcnt(8)\n"
    "v_mfma_f32_16x16x32_bf16 v[128:131], v[204:207], v[12:15], v[128:131]\n"
    "s_add_u32 m0, s93, 0x10000\n"
    "s_mul_i32 s89, s70, 0xa0000\n"
    "s_add_u32 s89, s89, 0x80000\n"
    "buffer_load_dwordx4 v229, s[60:63], s89 offen lds\n"
    "ds_read_b64_tr_b16 v[200:201], v220 offset:32768\n"
    "v_mfma_f32_16x16x32_bf16 v[176:179], v[204:207], v[76:79], v[176:179]\n"
    "ds_read_b64_tr_b16 v[202:203], v221 offset:32768\n"
    "v_rcp_f32 v247, v247\n"
    "s_waitcnt lgkmcnt(8)\n"
    "v_mfma_f32_16x16x32_bf16 v[136:139], v[208:211], v[12:15], v[136:139]\n"
    "ds_read_b64_tr_b16 v[204:205], v222 offset:32768\n"
    "v_rcp_f32 v248, v248\n"
    "v_mfma_f32_16x16x32_bf16 v[180:183], v[208:211], v[76:79], v[180:183]\n"
    "s_add_u32 m0, s93, 0x11000\n"
    "s_mul_i32 s89, s70, 0xa0000\n"
    "s_add_u32 s89, s89, 0x88000\n"
    "buffer_load_dwordx4 v229, s[60:63], s89 offen lds\n"
    "ds_read_b64_tr_b16 v[206:207], v223 offset:32768\n"
    "s_waitcnt lgkmcnt(8)\n"
    "v_mfma_f32_16x16x32_bf16 v[144:147], v[252:255], v[12:15], v[144:147]\n"
    "ds_read_b64_tr_b16 v[208:209], v220 offset:40960\n"
    "v_rcp_f32 v249, v249\n"
    "v_mfma_f32_16x16x32_bf16 v[184:187], v[252:255], v[76:79], v[184:187]\n"
    "ds_read_b64_tr_b16 v[210:211], v221 offset:40960\n"
    "v_mul_f32 v140, v140, v246\n"
    "v_mul_f32 v141, v141, v247\n"
    "s_waitcnt lgkmcnt(8)\n"
    "v_mfma_f32_16x16x32_bf16 v[152:155], v[192:195], v[12:15], v[152:155]\n"
    "ds_read_b64_tr_b16 v[252:253], v222 offset:40960\n"
    "v_mul_f32 v142, v142, v248\n"
    "v_mul_f32 v143, v143, v249\n"
    "v_mfma_f32_16x16x32_bf16 v[188:191], v[192:195], v[76:79], v[188:191]\n"
    "ds_read_b64_tr_b16 v[254:255], v223 offset:40960\n"
    "v_cvt_pk_bf16_f32 v150, v140, v141\n"
    "v_cvt_pk_bf16_f32 v151, v142, v143\n"
    "s_waitcnt lgkmcnt(8)\n"
    "v_mfma_f32_16x16x32_bf16 v[216:219], v[196:199], v[212:215], v[216:219]\n"
    "ds_read_b64_tr_b16 v[192:193], v224 offset:32768\n"
    "ds_read_b64_tr_b16 v[194:195], v225 offset:32768\n"
    "s_add_u32 s87, s98, 0x12000\n"
    "buffer_store_dwordx4 v[148:151], v230, s[52:55], s87 offen sc1\n"
    "v_lshrrev_b32 v237, 4, v235\n"
    "v_lshlrev_b32 v237, 4, v237\n"
    "s_lshl_b32 s87, s70, 7\n"
    "v_add_u32 v237, s87, v237\n"
    "s_waitcnt lgkmcnt(8)\n"
    "v_mfma_f32_16x16x32_bf16 v[128:131], v[200:203], v[16:19], v[128:131]\n"
    "ds_read_b64_tr_b16 v[196:197], v220 offset:36864\n"
    "ds_read_b128 v[238:241], v237 offset:25600\n"
    "ds_read_b128 v[246:249], v237 offset:25664\n"
    "ds_read_b128 v[242:245], v237 offset:25856\n"
    "v_mfma_f32_16x16x32_bf16 v[176:179], v[200:203], v[80:83], v[176:179]\n"
    "ds_read_b64_tr_b16 v[198:199], v221 offset:36864\n"
    "s_waitcnt lgkmcnt(2)\n"
    "v_add_f32 v238, v238, v246\n"
    "v_add_f32 v239, v239, v247\n"
    "v_mfma_f32_16x16x32_bf16 v[136:139], v[204:207], v[16:19], v[136:139]\n"
    "ds_read_b64_tr_b16 v[200:201], v222 offset:36864\n"
    "v_add_f32 v240, v240, v248\n"
    "v_add_f32 v241, v241, v249\n"
    "v_mfma_f32_16x16x32_bf16 v[180:183], v[204:207], v[80:83], v[180:183]\n"
    "ds_read_b64_tr_b16 v[202:203], v223 offset:36864\n"
    "ds_read_b128 v[246:249], v237 offset:25920\n"
    "v_mfma_f32_16x16x32_bf16 v[144:147], v[208:211], v[16:19], v[144:147]\n"
    "ds_read_b64_tr_b16 v[204:205], v220 offset:45056\n"
    "s_waitcnt lgkmcnt(1)\n"
    "v_add_f32 v242, v242, v246\n"
    "v_add_f32 v243, v243, v247\n"
    "v_mfma_f32_16x16x32_bf16 v[184:187], v[208:211], v[80:83], v[184:187]\n"
    "ds_read_b64_tr_b16 v[206:207], v221 offset:45056\n"
    "v_add_f32 v244, v244, v248\n"
    "v_add_f32 v245, v245, v249\n"
    "v_mfma_f32_16x16x32_bf16 v[152:155], v[252:255], v[16:19], v[152:155]\n"
    "ds_read_b64_tr_b16 v[208:209], v222 offset:45056\n"
    "v_mul_f32 v250, 0xbfb8aa3b, v233\n"
    "v_mul_f32 v251, v233, v233\n"
    "v_mfma_f32_16x16x32_bf16 v[188:191], v[252:255], v[80:83], v[188:191]\n"
    "ds_read_b64_tr_b16 v[210:211], v223 offset:45056\n"
    "v_rcp_f32 v251, v251\n"
    "v_mfma_f32_16x16x32_bf16 v[216:219], v[192:195], v[212:215], v[216:219]\n"
    "ds_read_b64_tr_b16 v[252:253], v224 offset:36864\n"
    "ds_read_b64_tr_b16 v[254:255], v225 offset:36864\n"
    "v_fma_f32 v160, -v231, v238, v160\n"
    "v_fma_f32 v161, -v231, v239, v161\n"
    "s_waitcnt vmcnt(12)\n"
    "s_barrier\n"
    "v_mfma_f32_16x16x32_bf16 v[128:131], v[196:199], v[20:23], v[128:131]\n"
    "s_add_u32 m0, s93, 0x14000\n"
    "s_mul_i32 s89, s70, 0xa0000\n"
    "s_add_u32 s89, s89, 0xb0000\n"
    "buffer_load_dwordx4 v229, s[60:63], s89 offen lds\n"
    "ds_read_b64_tr_b16 v[192:193], v220 offset:49152\n"
    "v_fma_f32 v162, -v231, v240, v162\n"
    "v_mfma_f32_16x16x32_bf16 v[176:179], v[196:199], v[84:87], v[176:179]\n"
    "ds_read_b64_tr_b16 v[194:195], v221 offset:49152\n"
    "v_fma_f32 v163, -v231, v241, v163\n"
    "v_fma_f32 v168, -v231, v242, v168\n"
    "v_mfma_f32_16x16x32_bf16 v[136:139], v[200:203], v[20:23], v[136:139]\n"
    "ds_read_b64_tr_b16 v[196:197], v222 offset:49152\n"
    "v_fma_f32 v169, -v231, v243, v169\n"
    "v_fma_f32 v170, -v231, v244, v170\n"
    "v_mfma_f32_16x16x32_bf16 v[180:183], v[200:203], v[84:87], v[180:183]\n"
    "s_add_u32 m0, s93, 0x15000\n"
    "s_mul_i32 s89, s70, 0xa0000\n"
    "s_add_u32 s89, s89, 0xb8000\n"
    "buffer_load_dwordx4 v229, s[60:63], s89 offen lds\n"
    "ds_read_b64_tr_b16 v[198:199], v223 offset:49152\n"
    "v_fma_f32 v171, -v231, v245, v171\n"
    "s_waitcnt lgkmcnt(8)\n"
    "v_mfma_f32_16x16x32_bf16 v[144:147], v[204:207], v[20:23], v[144:147]\n"
    "ds_read_b64_tr_b16 v[200:201], v220 offset:57344\n"
    "v_mul_f32 v246, v160, v250\n"
    "v_mul_f32 v247, v161, v250\n"
    "v_mfma_f32_16x16x32_bf16 v[184:187], v[204:207], v[84:87], v[184:187]\n"
    "ds_read_b64_tr_b16 v[202:203], v221 offset:57344\n"
    "v_mul_f32 v248, v162, v250\n"
    "v_mul_f32 v249, v163, v250\n"
    "s_waitcnt lgkmcnt(8)\n"
    "v_mfma_f32_16x16x32_bf16 v[152:155], v[208:211], v[20:23], v[152:155]\n"
    "ds_read_b64_tr_b16 v[204:205], v222 offset:57344\n"
    "v_exp_f32 v246, v246\n"
    "v_mfma_f32_16x16x32_bf16 v[188:191], v[208:211], v[84:87], v[188:191]\n"
    "ds_read_b64_tr_b16 v[206:207], v223 offset:57344\n"
    "v_exp_f32 v247, v247\n"
    "s_waitcnt lgkmcnt(8)\n"
    "v_mfma_f32_16x16x32_bf16 v[216:219], v[252:255], v[212:215], v[216:219]\n"
    "ds_read_b64_tr_b16 v[208:209], v224 offset:49152\n"
    "ds_read_b64_tr_b16 v[210:211], v225 offset:49152\n"
    "v_exp_f32 v248, v248\n"
    "s_waitcnt lgkmcnt(8)\n"
    "v_mfma_f32_16x16x32_bf16 v[128:131], v[192:195], v[24:27], v[128:131]\n"
    "ds_read_b64_tr_b16 v[252:253], v220 offset:53248\n"
    "v_exp_f32 v249, v249\n"
    "v_mfma_f32_16x16x32_bf16 v[176:179], v[192:195], v[88:91], v[176:179]\n"
    "ds_read_b64_tr_b16 v[254:255], v221 offset:53248\n"
    "v_mul_f32 v160, v160, v168\n"
    "v_mul_f32 v161, v161, v169\n"
    "s_waitcnt lgkmcnt(8)\n"
    "v_mfma_f32_16x16x32_bf16 v[136:139], v[196:199], v[24:27], v[136:139]\n"
    "ds_read_b64_tr_b16 v[192:193], v222 offset:53248\n"
    "v_mul_f32 v162, v162, v170\n"
    "v_mul_f32 v163, v163, v171\n"
    "v_mfma_f32_16x16x32_bf16 v[180:183], v[196:199], v[88:91], v[180:183]\n"
    "ds_read_b64_tr_b16 v[194:195], v223 offset:53248\n"
    "v_fma_f32 v246, v246, v251, v251\n"
    "v_fma_f32 v247, v247, v251, v251\n"
    "s_waitcnt lgkmcnt(8)\n"
    "v_mfma_f32_16x16x32_bf16 v[144:147], v[200:203], v[24:27], v[144:147]\n"
    "ds_read_b64_tr_b16 v[196:197], v220 offset:61440\n"
    "v_fma_f32 v248, v248, v251, v251\n"
    "v_fma_f32 v249, v249, v251, v251\n"
    "v_mfma_f32_16x16x32_bf16 v[184:187], v[200:203], v[88:91], v[184:187]\n"
    "ds_read_b64_tr_b16 v[198:199], v221 offset:61440\n"
    "v_rcp_f32 v246, v246\n"
    "s_waitcnt lgkmcnt(8)\n"
    "v_mfma_f32_16x16x32_bf16 v[152:155], v[204:207], v[24:27], v[152:155]\n"
    "ds_read_b64_tr_b16 v[200:201], v222 offset:61440\n"
    "v_rcp_f32 v247, v247\n"
    "v_mfma_f32_16x16x32_bf16 v[188:191], v[204:207], v[88:91], v[188:191]\n"
    "ds_read_b64_tr_b16 v[202:203], v223 offset:61440\n"
    "v_rcp_f32 v248, v248\n"
    "s_waitcnt lgkmcnt(8)\n"
    "v_mfma_f32_16x16x32_bf16 v[216:219], v[208:211], v[212:215], v[216:219]\n"
    "ds_read_b64_tr_b16 v[204:205], v224 offset:53248\n"
    "ds_read_b64_tr_b16 v[206:207], v225 offset:53248\n"
    "v_rcp_f32 v249, v249\n"
    "s_waitcnt vmcnt(2)\n"
    "s_barrier\n"
    "s_waitcnt lgkmcnt(8)\n"
    "v_mfma_f32_16x16x32_bf16 v[128:131], v[252:255], v[28:31], v[128:131]\n"
    "s_add_u32 m0, s93, 0x18000\n"
    "s_mul_i32 s89, s70, 0xa0000\n"
    "s_add_u32 s89, s89, 0xd0000\n"
    "buffer_load_dwordx4 v229, s[60:63], s89 offen lds\n"
    "v_xor_b32 v220, 0x10000, v220\n"
    "v_xor_b32 v221, 0x10000, v221\n"
    "v_xor_b32 v222, 0x10000, v222\n"
    "v_xor_b32 v223, 0x10000, v223\n"
    "ds_read_b64_tr_b16 v[208:209], v220 offset:0\n"
    "v_mfma_f32_16x16x32_bf16 v[176:179], v[252:255], v[92:95], v[176:179]\n"
    "ds_read_b64_tr_b16 v[210:211], v221 offset:0\n"
    "s_cmp_eq_u32 s68, 0\n"
    "s_cbranch_scc0 LB_skip_4\n"
    "s_mul_i32 s87, s72, 8\n"
    "s_add_u32 s87, s87, 0xc0\n"
    "s_add_u32 s87, s87, s90\n"
    "v_mov_b32 v226, s78\n"
    "v_mov_b32 v227, s79\n"
    "s_mov_b64 exec, 1\n"
    "buffer_store_dwordx2 v[226:227], off, s[52:55], s87 sc1\n"
    "s_mov_b64 exec, -1\n"
    "LB_skip_4:\n"
    "s_waitcnt lgkmcnt(8)\n"
    "s_nop 2\n"
    "v_mfma_f32_16x16x32_bf16 v[136:139], v[192:195], v[28:31], v[136:139]\n"
    "ds_read_b64_tr_b16 v[252:253], v222 offset:0\n"
    "v_mul_f32 v160, v160, v246\n"
    "v_mul_f32 v161, v161, v247\n"
    "v_mfma_f32_16x16x32_bf16 v[180:183], v[192:195], v[92:95], v[180:183]\n"
    "s_add_u32 m0, s93, 0x19000\n"
    "s_mul_i32 s89, s70, 0xa0000\n"
    "s_add_u32 s89, s89, 0xd8000\n"
    "buffer_load_dwordx4 v229, s[60:63], s89 offen lds\n"
    "ds_read_b64_tr_b16 v[254:255], v223 offset:0\n"
    "v_mul_f32 v162, v162, v248\n"
    "s_waitcnt lgkmcnt(8)\n"
    "v_mfma_f32_16x16x32_bf16 v[144:147], v[196:199], v[28:31], v[144:147]\n"
    "ds_read_b64_tr_b16 v[192:193], v220 offset:8192\n"
    "v_mul_f32 v163, v163, v249\n"
    "v_cvt_pk_bf16_f32 v168, v160, v161\n"
    "v_mfma_f32_16x16x32_bf16 v[184:187], v[196:199], v[92:95], v[184:187]\n"
    "ds_read_b64_tr_b16 v[194:195], v221 offset:8192\n"
    "v_cvt_pk_bf16_f32 v169, v162, v163\n"
    "v_mul_f32 v250, 0xbfb8aa3b, v234\n"
    "s_waitcnt lgkmcnt(8)\n"
    "v_mfma_f32_16x16x32_bf16 v[152:155], v[200:203], v[28:31], v[152:155]\n"
    "ds_read_b64_tr_b16 v[196:197], v222 offset:8192\n"
    "v_mul_f32 v251, v234, v234\n"
    "v_mfma_f32_16x16x32_bf16 v[188:191], v[200:203], v[92:95], v[188:191]\n"
    "ds_read_b64_tr_b16 v[198:199], v223 offset:8192\n"
    "v_rcp_f32 v251, v251\n"
    "s_waitcnt lgkmcnt(8)\n"
    "v_mfma_f32_16x16x32_bf16 v[216:219], v[204:207], v[212:215], v[216:219]\n"
    "v_xor_b32 v224, 0x10000, v224\n"
    "v_xor_b32 v225, 0x10000, v225\n"
    "ds_read_b64_tr_b16 v[200:201], v224 offset:0\n"
    "ds_read_b64_tr_b16 v[202:203], v225 offset:0\n"
    "s_waitcnt lgkmcnt(8)\n"
    "v_mfma_f32_16x16x32_bf16 v[128:131], v[208:211], v[32:35], v[128:131]\n"
    "ds_read_b64_tr_b16 v[204:205], v220 offset:4096\n"
    "v_fma_f32 v164, -v232, v238, v164\n"
    "v_fma_f32 v165, -v232, v239, v165\n"
    "v_mfma_f32_16x16x32_bf16 v[176:179], v[208:211], v[96:99], v[176:179]\n"
    "ds_read_b64_tr_b16 v[206:207], v221 offset:4096\n"
    "v_fma_f32 v166, -v232, v240, v166\n"
    "v_fma_f32 v167, -v232, v241, v167\n"
    "s_waitcnt lgkmcnt(8)\n"
    "v_mfma_f32_16x16x32_bf16 v[136:139], v[252:255], v[32:35], v[136:139]\n"
    "ds_read_b64_tr_b16 v[208:209], v222 offset:4096\n"
    "v_fma_f32 v172, -v232, v242, v172\n"
    "v_fma_f32 v173, -v232, v243, v173\n"
    "v_mfma_f32_16x16x32_bf16 v[180:183], v[252:255], v[96:99], v[180:183]\n"
    "ds_read_b64_tr_b16 v[210:211], v223 offset:4096\n"
    "v_fma_f32 v174, -v232, v244, v174\n"
    "v_fma_f32 v175, -v232, v245, v175\n"
    "s_waitcnt lgkmcnt(8)\n"
    "v_mfma_f32_16x16x32_bf16 v[144:147], v[192:195], v[32:35], v[144:147]\n"
    "ds_read_b64_tr_b16 v[252:253], v220 offset:12288\n"
    "v_mul_f32 v246, v164, v250\n"
    "v_mul_f32 v247, v165, v250\n"
    "v_mfma_f32_16x16x32_bf16 v[184:187], v[192:195], v[96:99], v[184:187]\n"
    "ds_read_b64_tr_b16 v[254:255], v221 offset:12288\n"
    "v_mul_f32 v248, v166, v250\n"
    "v_mul_f32 v249, v167, v250\n"
    "s_waitcnt lgkmcnt(8)\n"
    "v_mfma_f32_16x16x32_bf16 v[152:155], v[196:199], v[32:35], v[152:155]\n"
    "ds_read_b64_tr_b16 v[192:193], v222 offset:12288\n"
    "v_exp_f32 v246, v246\n"
    "v_mfma_f32_16x16x32_bf16 v[188:191], v[196:199], v[96:99], v[188:191]\n"
    "ds_read_b64_tr_b16 v[194:195], v223 offset:12288\n"
    "v_exp_f32 v247, v247\n"
    "s_waitcnt lgkmcnt(8)\n"
    "v_mfma_f32_16x16x32_bf16 v[216:219], v[200:203], v[212:215], v[216:219]\n"
    "ds_read_b64_tr_b16 v[196:197], v224 offset:4096\n"
    "ds_read_b64_tr_b16 v[198:199], v225 offset:4096\n"
    "v_exp_f32 v248, v248\n"
    "s_barrier\n"
    "s_waitcnt lgkmcnt(8)\n"
    "v_mfma_f32_16x16x32_bf16 v[128:131], v[204:207], v[36:39], v[128:131]\n"
    "s_add_u32 m0, s93, 0x1c000\n"
    "s_mul_i32 s89, s70, 0xa0000\n"
    "s_add_u32 s89, s89, 0x100000\n"
    "buffer_load_dwordx4 v229, s[60:63], s89 offen lds\n"
    "ds_read_b64_tr_b16 v[200:201], v220 offset:16384\n"
    "v_mfma_f32_16x16x32_bf16 v[176:179], v[204:207], v[100:103], v[176:179]\n"
    "ds_read_b64_tr_b16 v[202:203], v221 offset:16384\n"
    "v_exp_f32 v249, v249\n"
    "s_waitcnt lgkmcnt(8)\n"
    "v_mfma_f32_16x16x32_bf16 v[136:139], v[208:211], v[36:39], v[136:139]\n"
    "ds_read_b64_tr_b16 v[204:205], v222 offset:16384\n"
    "v_mul_f32 v164, v164, v172\n"
    "v_mul_f32 v165, v165, v173\n"
    "v_mfma_f32_16x16x32_bf16 v[180:183], v[208:211], v[100:103], v[180:183]\n"
    "s_add_u32 m0, s93, 0x1d000\n"
    "s_mul_i32 s89, s70, 0xa0000\n"
    "s_add_u32 s89, s89, 0x108000\n"
    "buffer_load_dwordx4 v229, s[60:63], s89 offen lds\n"
    "ds_read_b64_tr_b16 v[206:207], v223 offset:16384\n"
    "v_mul_f32 v166, v166, v174\n"
    "s_waitcnt lgkmcnt(8)\n"
    "v_mfma_f32_16x16x32_bf16 v[144:147], v[252:255], v[36:39], v[144:147]\n"
    "ds_read_b64_tr_b16 v[208:209], v220 offset:24576\n"
    "v_mul_f32 v167, v167, v175\n"
    "v_fma_f32 v246, v246, v251, v251\n"
    "v_mfma_f32_16x16x32_bf16 v[184:187], v[252:255], v[100:103], v[184:187]\n"
    "ds_read_b64_tr_b16 v[210:211], v221 offset:24576\n"
    "v_fma_f32 v247, v247, v251, v251\n"
    "v_fma_f32 v248, v248, v251, v251\n"
    "s_waitcnt lgkmcnt(8)\n"
    "v_mfma_f32_16x16x32_bf16 v[152:155], v[192:195], v[36:39], v[152:155]\n"
    "ds_read_b64_tr_b16 v[252:253], v222 offset:24576\n"
    "v_fma_f32 v249, v249, v251, v251\n"
    "v_mfma_f32_16x16x32_bf16 v[188:191], v[192:195], v[100:103], v[188:191]\n"
    "ds_read_b64_tr_b16 v[254:255], v223 offset:24576\n"
    "v_rcp_f32 v246, v246\n"
    "s_waitcnt lgkmcnt(8)\n"
    "v_mfma_f32_16x16x32_bf16 v[216:219], v[196:199], v[212:215], v[216:219]\n"
    "ds_read_b64_tr_b16 v[192:193], v224 offset:16384\n"
    "ds_read_b64_tr_b16 v[194:195], v225 offset:16384\n"
    "v_rcp_f32 v247, v247\n"
    "s_waitcnt lgkmcnt(8)\n"
    "v_mfma_f32_16x16x32_bf16 v[128:131], v[200:203], v[40:43], v[128:131]\n"
    "ds_read_b64_tr_b16 v[196:197], v220 offset:20480\n"
    "v_rcp_f32 v248, v248\n"
    "v_mfma_f32_16x16x32_bf16 v[176:179], v[200:203], v[104:107], v[176:179]\n"
    "ds_read_b64_tr_b16 v[198:199], v221 offset:20480\n"
    "v_rcp_f32 v249, v249\n"
    "s_waitcnt lgkmcnt(8)\n"
    "v_mfma_f32_16x16x32_bf16 v[136:139], v[204:207], v[40:43], v[136:139]\n"
    "ds_read_b64_tr_b16 v[200:201], v222 offset:20480\n"
    "v_mul_f32 v164, v164, v246\n"
    "v_mul_f32 v165, v165, v247\n"
    "v_mfma_f32_16x16x32_bf16 v[180:183], v[204:207], v[104:107], v[180:183]\n"
    "ds_read_b64_tr_b16 v[202:203], v223 offset:20480\n"
    "v_mul_f32 v166, v166, v248\n"
    "v_mul_f32 v167, v167, v249\n"
    "s_waitcnt lgkmcnt(8)\n"
    "v_mfma_f32_16x16x32_bf16 v[144:147], v[208:211], v[40:43], v[144:147]\n"
    "ds_read_b64_tr_b16 v[204:205], v220 offset:28672\n"
    "v_cvt_pk_bf16_f32 v172, v164, v165\n"
    "v_cvt_pk_bf16_f32 v173, v166, v167\n"
    "v_mfma_f32_16x16x32_bf16 v[184:187], v[208:211], v[104:107], v[184:187]\n"
    "ds_read_b64_tr_b16 v[206:207], v221 offset:28672\n"
    "s_lshl_b32 s87, s69, 11\n"
    "s_lshl_b32 s88, s70, 3\n"
    "s_add_u32 s87, s87, s88\n"
    "v_lshlrev_b32 v237, 4, v235\n"
    "v_add_u32 v237, s87, v237\n"
    "s_waitcnt lgkmcnt(8)\n"
    "v_mfma_f32_16x16x32_bf16 v[152:155], v[252:255], v[40:43], v[152:155]\n"
    "ds_read_b64_tr_b16 v[208:209], v222 offset:28672\n"
    "ds_write_b64 v237, v[168:169] offset:16384\n"
    "ds_write_b64 v237, v[172:173] offset:17408\n"
    "v_mfma_f32_16x16x32_bf16 v[188:191], v[252:255], v[104:107], v[188:191]\n"
    "ds_read_b64_tr_b16 v[210:211], v223 offset:28672\n"
    "s_waitcnt lgkmcnt(10)\n"
    "v_mfma_f32_16x16x32_bf16 v[216:219], v[192:195], v[212:215], v[216:219]\n"
    "ds_read_b64_tr_b16 v[252:253], v224 offset:20480\n"
    "ds_read_b64_tr_b16 v[254:255], v225 offset:20480\n"
    "s_waitcnt lgkmcnt(3)\n"
    "s_barrier\n"
    "v_mfma_f32_16x16x32_bf16 v[128:131], v[196:199], v[44:47], v[128:131]\n"
    "s_add_u32 m0, s93, 0\n"
    "s_mul_i32 s89, s70, 0xa0000\n"
    "s_add_u32 s89, s89, 0xa0000\n"
    "buffer_load_dwordx4 v229, s[60:63], s89 offen lds\n"
    "ds_read_b64_tr_b16 v[192:193], v220 offset:32768\n"
    "s_cmp_eq_u32 s68, 0\n"
    "s_cbranch_scc0 LB_skip_6\n"
    "v_and_b32 v26, 15, v235\n"
    "v_lshlrev_b32 v26, 3, v26\n"
    "s_add_u32 s88, s90, 0x80\n"
    "buffer_load_dwordx2 v[24:25], v26, s[52:55], s88 offen sc1\n"
    "LB_skip_6:\n"
    "v_mfma_f32_16x16x32_bf16 v[176:179], v[196:199], v[108:111], v[176:179]\n"
    "ds_read_b64_tr_b16 v[194:195], v221 offset:32768\n"
    "s_lshl_b32 s87, s69, 11\n"
    "s_lshl_b32 s88, s70, 10\n"
    "s_add_u32 s87, s87, s88\n"
    "v_lshlrev_b32 v237, 4, v235\n"
    "v_add_u32 v237, s87, v237\n"
    "v_mfma_f32_16x16x32_bf16 v[136:139], v[200:203], v[44:47], v[136:139]\n"
    "ds_read_b64_tr_b16 v[196:197], v222 offset:32768\n"
    "ds_read_b128 v[160:163], v237 offset:16384\n"
    "v_mfma_f32_16x16x32_bf16 v[180:183], v[200:203], v[108:111], v[180:183]\n"
    "s_add_u32 m0, s93, 0x1000\n"
    "s_mul_i32 s89, s70, 0xa0000\n"
    "s_add_u32 s89, s89, 0xa8000\n"
    "buffer_load_dwordx4 v229, s[60:63], s89 offen lds\n"
    "ds_read_b64_tr_b16 v[198:199], v223 offset:32768\n"
    "s_add_u32 s87, s91, 0\n"
    "s_waitcnt lgkmcnt(1)\n"
    "buffer_store_dwordx4 v[160:163], v230, s[52:55], s87 offen sc1\n"
    "v_mfma_f32_16x16x32_bf16 v[144:147], v[204:207], v[44:47], v[144:147]\n"
    "ds_read_b64_tr_b16 v[200:201], v220 offset:40960\n"
    "v_mfma_f32_16x16x32_bf16 v[184:187], v[204:207], v[108:111], v[184:187]\n"
    "ds_read_b64_tr_b16 v[202:203], v221 offset:40960\n"
    "v_mfma_f32_16x16x32_bf16 v[152:155], v[208:211], v[44:47], v[152:155]\n"
    "ds_read_b64_tr_b16 v[204:205], v222 offset:40960\n"
    "v_mfma_f32_16x16x32_bf16 v[188:191], v[208:211], v[108:111], v[188:191]\n"
    "ds_read_b64_tr_b16 v[206:207], v223 offset:40960\n"
    "v_mfma_f32_16x16x32_bf16 v[216:219], v[252:255], v[212:215], v[216:219]\n"
    "ds_read_b64_tr_b16 v[208:209], v224 offset:32768\n"
    "ds_read_b64_tr_b16 v[210:211], v225 offset:32768\n"
    "v_and_b32 v102, 15, v235\n"
    "v_lshrrev_b32 v103, 4, v235\n"
    "v_mfma_f32_16x16x32_bf16 v[128:131], v[192:195], v[48:51], v[128:131]\n"
    "ds_read_b64_tr_b16 v[252:253], v220 offset:36864\n"
    "v_lshrrev_b32 v108, 2, v102\n"
    "v_and_b32 v109, 3, v102\n"
    "v_mfma_f32_16x16x32_bf16 v[176:179], v[192:195], v[112:115], v[176:179]\n"
    "ds_read_b64_tr_b16 v[254:255], v221 offset:36864\n"
    "v_lshrrev_b32 v110, 1, v103\n"
    "v_xor_b32 v110, v110, v103\n"
    "s_waitcnt lgkmcnt(8)\n"
    "v_mfma_f32_16x16x32_bf16 v[136:139], v[196:199], v[48:51], v[136:139]\n"
    "ds_read_b64_tr_b16 v[192:193], v222 offset:36864\n"
    "v_and_b32 v110, 1, v110\n"
    "v_lshrrev_b32 v111, 1, v108\n"
    "v_mfma_f32_16x16x32_bf16 v[180:183], v[196:199], v[112:115], v[180:183]\n"
    "ds_read_b64_tr_b16 v[194:195], v223 offset:36864\n"
    "v_lshl_or_b32 v111, v110, 1, v111\n"
    "v_lshl_add_u32 v110, v103, 2, v108\n"
    "s_waitcnt lgkmcnt(8)\n"
    "v_mfma_f32_16x16x32_bf16 v[144:147], v[200:203], v[48:51], v[144:147]\n"
    "ds_read_b64_tr_b16 v[196:197], v220 offset:45056\n"
    "v_lshlrev_b32 v110, 7, v110\n"
    "v_lshl_add_u32 v110, v109, 3, v110\n"
    "v_mfma_f32_16x16x32_bf16 v[184:187], v[200:203], v[112:115], v[184:187]\n"
    "ds_read_b64_tr_b16 v[198:199], v221 offset:45056\n"
    "s_lshl_b32 s87, s70, 13\n"
    "s_add_u32 s87, s87, 0x8000\n"
    "v_add_u32 v110, s87, v110\n"
    "v_lshl_add_u32 v96, v111, 5, v110\n"
    "s_waitcnt lgkmcnt(8)\n"
    "v_mfma_f32_16x16x32_bf16 v[152:155], v[204:207], v[48:51], v[152:155]\n"
    "ds_read_b64_tr_b16 v[200:201], v222 offset:45056\n"
    "v_xor_b32 v97, 32, v96\n"
    "v_xor_b32 v98, 64, v96\n"
    "v_mfma_f32_16x16x32_bf16 v[188:191], v[204:207], v[112:115], v[188:191]\n"
    "ds_read_b64_tr_b16 v[202:203], v223 offset:45056\n"
    "v_xor_b32 v99, 0x60, v96\n"
    "v_lshrrev_b32 v110, 3, v235\n"
    "s_waitcnt lgkmcnt(8)\n"
    "v_mfma_f32_16x16x32_bf16 v[216:219], v[208:211], v[212:215], v[216:219]\n"
    "ds_read_b64_tr_b16 v[204:205], v224 offset:36864\n"
    "ds_read_b64_tr_b16 v[206:207], v225 offset:36864\n"
    "v_lshlrev_b32 v110, 10, v110\n"
    "v_and_b32 v111, 7, v235\n"
    "s_cmp_eq_u32 s68, 0\n"
    "s_cbranch_scc0 LB_skip_10\n"
    "s_add_u32 s88, s90, 0x80\n"
    "s_mov_b32 s96, 0xff00ff00\n"
    "s_mov_b32 s97, 0xff00ff00\n"
    "s_waitcnt vmcnt(2)\n"
    "v_cmp_eq_u64 vcc, s[78:79], v[24:25]\n"
    "s_nop 4\n"
    "s_and_b64 vcc, vcc, s[96:97]\n"
    "s_cmp_eq_u64 vcc, s[96:97]\n"
    "s_cbranch_scc0 LB_slowE1_7\n"
    "LB_backE1_8:\n"
    "LB_skip_10:\n"
    "s_barrier\n"
    "s_mul_i32 s40, s70, 0x3000\n"
    "s_mul_i32 s41, s70, 0x2000\n"
    "s_mul_i32 s42, s70, 0x4000\n"
    "s_lshl_b32 s86, s71, 3\n"
    "s_add_u32 s86, s86, 4\n"
    "s_add_u32 s86, s86, s69\n"
    "s_mul_i32 s86, s86, 0x12000\n"
    "v_lshlrev_b32 v100, 4, v235\n"
    "v_add_u32 v100, s86, v100\n"
    "s_add_u32 s89, s40, 0x4000\n"
    "buffer_load_dwordx4 v[0:3], v100, s[52:55], s89 offen sc1\n"
    "s_add_u32 s89, s40, 0x4400\n"
    "buffer_load_dwordx4 v[4:7], v100, s[52:55], s89 offen sc1\n"
    "s_add_u32 s89, s40, 0x4c00\n"
    "buffer_load_dwordx4 v[8:11], v100, s[52:55], s89 offen sc1\n"
    "s_add_u32 s89, s40, 0x5000\n"
    "buffer_load_dwordx4 v[12:15], v100, s[52:55], s89 offen sc1\n"
    "s_add_u32 s89, s40, 0x5800\n"
    "buffer_load_dwordx4 v[16:19], v100, s[52:55], s89 offen sc1\n"
    "s_add_u32 s89, s40, 0x5c00\n"
    "buffer_load_dwordx4 v[20:23], v100, s[52:55], s89 offen sc1\n"
    "s_waitcnt lgkmcnt(8)\n"
    "v_mfma_f32_16x16x32_bf16 v[128:131], v[252:255], v[52:55], v[128:131]\n"
    "s_add_u32 m0, s93, 0x4000\n"
    "s_mul_i32 s89, s70, 0xa0000\n"
    "s_add_u32 s89, s89, 0xf0000\n"
    "buffer_load_dwordx4 v229, s[60:63], s89 offen lds\n"
    "ds_read_b64_tr_b16 v[208:209], v220 offset:49152\n"
    "s_cmp_eq_u32 s68, 0\n"
    "s_cbranch_scc0 LB_skip_12\n"
    "v_and_b32 v166, 15, v235\n"
    "v_lshlrev_b32 v166, 3, v166\n"
    "s_add_u32 s88, s90, 0x180\n"
    "buffer_load_dwordx2 v[164:165], v166, s[52:55], s88 offen sc1\n"
    "LB_skip_12:\n"
    "v_mfma_f32_16x16x32_bf16 v[176:179], v[252:255], v[116:119], v[176:179]\n"
    "ds_read_b64_tr_b16 v[210:211], v221 offset:49152\n"
    "v_lshl_add_u32 v110, v111, 4, v110\n"
    "s_lshl_b32 s87, s68, 3\n"
    "s_add_u32 s87, s87, s77\n"
    "s_add_u32 s87, s87, 64\n"
    "s_lshl_b32 s87, s87, 10\n"
    "s_lshl_b32 s88, s72, 7\n"
    "s_add_u32 s87, s87, s88\n"
    "v_add_u32 v101, s87, v110\n"
    "buffer_load_dwordx4 v[104:107], v101, s[36:39], 0 offen\n"
    "s_waitcnt lgkmcnt(8)\n"
    "v_mfma_f32_16x16x32_bf16 v[136:139], v[192:195], v[52:55], v[136:139]\n"
    "ds_read_b64_tr_b16 v[252:253], v222 offset:49152\n"
    "v_mfma_f32_16x16x32_bf16 v[180:183], v[192:195], v[116:119], v[180:183]\n"
    "s_add_u32 m0, s93, 0x5000\n"
    "s_mul_i32 s89, s70, 0xa0000\n"
    "s_add_u32 s89, s89, 0xf8000\n"
    "buffer_load_dwordx4 v229, s[60:63], s89 offen lds\n"
    "ds_read_b64_tr_b16 v[254:255], v223 offset:49152\n"
    "s_waitcnt lgkmcnt(8)\n"
    "v_mfma_f32_16x16x32_bf16 v[144:147], v[196:199], v[52:55], v[144:147]\n"
    "ds_read_b64_tr_b16 v[192:193], v220 offset:57344\n"
    "v_mfma_f32_16x16x32_bf16 v[184:187], v[196:199], v[116:119], v[184:187]\n"
    "ds_read_b64_tr_b16 v[194:195], v221 offset:57344\n"
    "s_waitcnt lgkmcnt(8)\n"
    "v_mfma_f32_16x16x32_bf16 v[152:155], v[200:203], v[52:55], v[152:155]\n"
    "ds_read_b64_tr_b16 v[196:197], v222 offset:57344\n"
    "v_mfma_f32_16x16x32_bf16 v[188:191], v[200:203], v[116:119], v[188:191]\n"
    "ds_read_b64_tr_b16 v[198:199], v223 offset:57344\n"
    "s_waitcnt lgkmcnt(8)\n"
    "v_mfma_f32_16x16x32_bf16 v[216:219], v[204:207], v[212:215], v[216:219]\n"
    "ds_read_b64_tr_b16 v[200:201], v224 offset:49152\n"
    "ds_read_b64_tr_b16 v[202:203], v225 offset:49152\n"
    "s_waitcnt lgkmcnt(8)\n"
    "v_mfma_f32_16x16x32_bf16 v[128:131], v[208:211], v[56:59], v[128:131]\n"
    "ds_read_b64_tr_b16 v[204:205], v220 offset:53248\n"
    "v_mfma_f32_16x16x32_bf16 v[176:179], v[208:211], v[120:123], v[176:179]\n"
    "ds_read_b64_tr_b16 v[206:207], v221 offset:53248\n"
    "s_waitcnt lgkmcnt(8)\n"
    "v_mfma_f32_16x16x32_bf16 v[136:139], v[252:255], v[56:59], v[136:139]\n"
    "ds_read_b64_tr_b16 v[208:209], v222 offset:53248\n"
    "v_mfma_f32_16x16x32_bf16 v[180:183], v[252:255], v[120:123], v[180:183]\n"
    "ds_read_b64_tr_b16 v[210:211], v223 offset:53248\n"
    "s_waitcnt lgkmcnt(8)\n"
    "v_mfma_f32_16x16x32_bf16 v[144:147], v[192:195], v[56:59], v[144:147]\n"
    "ds_read_b64_tr_b16 v[252:253], v220 offset:61440\n"
    "v_mfma_f32_16x16x32_bf16 v[184:187], v[192:195], v[120:123], v[184:187]\n"
    "ds_read_b64_tr_b16 v[254:255], v221 offset:61440\n"
    "s_waitcnt lgkmcnt(8)\n"
    "v_mfma_f32_16x16x32_bf16 v[152:155], v[196:199], v[56:59], v[152:155]\n"
    "ds_read_b64_tr_b16 v[192:193], v222 offset:61440\n"
    "v_mfma_f32_16x16x32_bf16 v[188:191], v[196:199], v[120:123], v[188:191]\n"
    "ds_read_b64_tr_b16 v[194:195], v223 offset:61440\n"
    "s_waitcnt lgkmcnt(8)\n"
    "v_mfma_f32_16x16x32_bf16 v[216:219], v[200:203], v[212:215], v[216:219]\n"
    "ds_read_b64_tr_b16 v[196:197], v224 offset:53248\n"
    "ds_read_b64_tr_b16 v[198:199], v225 offset:53248\n"
    "s_waitcnt vmcnt(9)\n"
    "s_barrier\n"
    "s_waitcnt lgkmcnt(8)\n"
    "v_mfma_f32_16x16x32_bf16 v[128:131], v[204:207], v[60:63], v[128:131]\n"
    "s_add_u32 m0, s93, 0x8000\n"
    "s_mul_i32 s89, s70, 0x40000\n"
    "s_add_u32 s89, s89, 0\n"
    "buffer_load_dwordx4 v229, s[56:59], s89 offen lds\n"
    "v_xor_b32 v96, 0x10000, v96\n"
    "v_xor_b32 v97, 0x10000, v97\n"
    "v_xor_b32 v98, 0x10000, v98\n"
    "v_xor_b32 v99, 0x10000, v99\n"
    "ds_read_b64_tr_b16 v[64:65], v96 offset:0\n"
    "v_mfma_f32_16x16x32_bf16 v[176:179], v[204:207], v[124:127], v[176:179]\n"
    "ds_read_b64_tr_b16 v[66:67], v96 offset:2048\n"
    "s_cmp_eq_u32 s68, 0\n"
    "s_cbranch_scc0 LB_skip_5\n"
    "s_mul_i32 s87, s72, 8\n"
    "s_add_u32 s87, s87, 0x1c0\n"
    "s_add_u32 s87, s87, s90\n"
    "v_mov_b32 v226, s78\n"
    "v_mov_b32 v227, s79\n"
    "s_mov_b64 exec, 1\n"
    "buffer_store_dwordx2 v[226:227], off, s[52:55], s87 sc1\n"
    "s_mov_b64 exec, -1\n"
    "LB_skip_5:\n"
    "s_waitcnt lgkmcnt(8)\n"
    "s_nop 2\n"
    "v_mfma_f32_16x16x32_bf16 v[136:139], v[208:211], v[60:63], v[136:139]\n"
    "ds_read_b64_tr_b16 v[68:69], v97 offset:0\n"
    "v_mfma_f32_16x16x32_bf16 v[180:183], v[208:211], v[124:127], v[180:183]\n"
    "s_add_u32 m0, s93, 0x9000\n"
    "s_mul_i32 s89, s70, 0x40000\n"
    "s_add_u32 s89, s89, 0x8000\n"
    "buffer_load_dwordx4 v229, s[56:59], s89 offen lds\n"
    "ds_read_b64_tr_b16 v[70:71], v97 offset:2048\n"
    "s_waitcnt lgkmcnt(8)\n"
    "v_mfma_f32_16x16x32_bf16 v[144:147], v[252:255], v[60:63], v[144:147]\n"
    "ds_read_b64_tr_b16 v[72:73], v98 offset:0\n"
    "v_mfma_f32_16x16x32_bf16 v[184:187], v[252:255], v[124:127], v[184:187]\n"
    "ds_read_b64_tr_b16 v[74:75], v98 offset:2048\n"
    "s_waitcnt lgkmcnt(8)\n"
    "v_mfma_f32_16x16x32_bf16 v[152:155], v[192:195], v[60:63], v[152:155]\n"
    "ds_read_b64_tr_b16 v[76:77], v99 offset:0\n"
    "v_mfma_f32_16x16x32_bf16 v[188:191], v[192:195], v[124:127], v[188:191]\n"
    "ds_read_b64_tr_b16 v[78:79], v99 offset:2048\n"
    "s_waitcnt lgkmcnt(8)\n"
    "v_mfma_f32_16x16x32_bf16 v[216:219], v[196:199], v[212:215], v[216:219]\n"
    "ds_read_b64_tr_b16 v[80:81], v96 offset:4096\n"
    "ds_read_b64_tr_b16 v[82:83], v96 offset:6144\n"
    "ds_read_b64_tr_b16 v[84:85], v97 offset:4096\n"
    "ds_read_b64_tr_b16 v[86:87], v97 offset:6144\n"
    "ds_read_b64_tr_b16 v[88:89], v98 offset:4096\n"
    "ds_read_b64_tr_b16 v[90:91], v98 offset:6144\n"
    "ds_read_b64_tr_b16 v[92:93], v99 offset:4096\n"
    "ds_read_b64_tr_b16 v[94:95], v99 offset:6144\n"
    "s_lshl_b32 s87, s68, 7\n"
    "v_lshrrev_b32 v172, 4, v235\n"
    "v_lshlrev_b32 v172, 4, v172\n"
    "v_add_u32 v172, s87, v172\n"
    "s_mov_b32 exec_lo, 0x10001\n"
    "s_mov_b32 exec_hi, 0x10001\n"
    "ds_write_b128 v172, v[216:219] offset:26624\n"
    "s_mov_b64 exec, -1\n"
    "s_waitcnt vmcnt(9) lgkmcnt(0)\n"
    "s_barrier\n"
    "s_add_u32 s89, s40, 0x6400\n"
    "buffer_load_dwordx4 v[24:27], v100, s[52:55], s89 offen sc1\n"
    "s_add_u32 s89, s40, 0x6800\n"
    "buffer_load_dwordx4 v[28:31], v100, s[52:55], s89 offen sc1\n"
    "ds_read_b128 v[238:241], v236 offset:26624\n"
    "ds_read_b128 v[242:245], v236 offset:27136\n"
    "v_mul_f32 v250, 0xbfb8aa3b, v233\n"
    "v_mul_f32 v251, v233, v233\n"
    "v_rcp_f32 v251, v251\n"
    "s_waitcnt lgkmcnt(1)\n"
    "v_fma_f32 v128, -v231, v238, v128\n"
    "v_fma_f32 v129, -v231, v239, v129\n"
    "v_fma_f32 v130, -v231, v240, v130\n"
    "v_fma_f32 v131, -v231, v241, v131\n"
    "s_waitcnt lgkmcnt(0)\n"
    "v_fma_f32 v144, -v231, v242, v144\n"
    "v_fma_f32 v145, -v231, v243, v145\n"
    "v_fma_f32 v146, -v231, v244, v146\n"
    "v_fma_f32 v147, -v231, v245, v147\n"
    "ds_read_b128 v[238:241], v236 offset:26752\n"
    "ds_read_b128 v[242:245], v236 offset:27264\n"
    "v_mul_f32 v246, v128, v250\n"
    "v_mul_f32 v247, v129, v250\n"
    "v_mul_f32 v248, v130, v250\n"
    "v_mul_f32 v249, v131, v250\n"
    "v_exp_f32 v246, v246\n"
    "v_exp_f32 v247, v247\n"
    "v_exp_f32 v248, v248\n"
    "v_exp_f32 v249, v249\n"
    "v_mul_f32 v128, v128, v144\n"
    "v_mul_f32 v129, v129, v145\n"
    "v_mul_f32 v130, v130, v146\n"
    "v_mul_f32 v131, v131, v147\n"
    "v_fma_f32 v246, v246, v251, v251\n"
    "v_fma_f32 v247, v247, v251, v251\n"
    "v_fma_f32 v248, v248, v251, v251\n"
    "v_fma_f32 v249, v249, v251, v251\n"
    "v_rcp_f32 v246, v246\n"
    "v_rcp_f32 v247, v247\n"
    "v_rcp_f32 v248, v248\n"
    "v_rcp_f32 v249, v249\n"
    "v_mul_f32 v128, v128, v246\n"
    "v_mul_f32 v129, v129, v247\n"
    "v_mul_f32 v130, v130, v248\n"
    "v_mul_f32 v131, v131, v249\n"
    "v_cvt_pk_bf16_f32 v144, v128, v129\n"
    "v_cvt_pk_bf16_f32 v145, v130, v131\n"
    "s_waitcnt lgkmcnt(1)\n"
    "v_fma_f32 v136, -v231, v238, v136\n"
    "v_fma_f32 v137, -v231, v239, v137\n"
    "v_fma_f32 v138, -v231, v240, v138\n"
    "v_fma_f32 v139, -v231, v241, v139\n"
    "s_waitcnt lgkmcnt(0)\n"
    "v_fma_f32 v152, -v231, v242, v152\n"
    "v_fma_f32 v153, -v231, v243, v153\n"
    "v_mfma_f32_16x16x32_bf16 v[32:35], v[64:67], v[0:3], 0\n"
    "ds_read_b64_tr_b16 v[48:49], v96 offset:16384\n"
    "ds_read_b64_tr_b16 v[50:51], v96 offset:18432\n"
    "v_fma_f32 v154, -v231, v244, v154\n"
    "v_fma_f32 v155, -v231, v245, v155\n"
    "v_mfma_f32_16x16x32_bf16 v[36:39], v[68:71], v[0:3], 0\n"
    "ds_read_b64_tr_b16 v[52:53], v97 offset:16384\n"
    "ds_read_b64_tr_b16 v[54:55], v97 offset:18432\n"
    "v_mul_f32 v246, v136, v250\n"
    "v_mul_f32 v247, v137, v250\n"
    "v_mfma_f32_16x16x32_bf16 v[40:43], v[72:75], v[0:3], 0\n"
    "ds_read_b64_tr_b16 v[56:57], v98 offset:16384\n"
    "ds_read_b64_tr_b16 v[58:59], v98 offset:18432\n"
    "v_mul_f32 v248, v138, v250\n"
    "v_mul_f32 v249, v139, v250\n"
    "v_mfma_f32_16x16x32_bf16 v[44:47], v[76:79], v[0:3], 0\n"
    "ds_read_b64_tr_b16 v[60:61], v99 offset:16384\n"
    "ds_read_b64_tr_b16 v[62:63], v99 offset:18432\n"
    "v_exp_f32 v246, v246\n"
    "v_mfma_f32_16x16x32_bf16 v[32:35], v[80:83], v[4:7], v[32:35]\n"
    "ds_read_b64_tr_b16 v[112:113], v96 offset:20480\n"
    "ds_read_b64_tr_b16 v[114:115], v96 offset:22528\n"
    "v_exp_f32 v247, v247\n"
    "v_mfma_f32_16x16x32_bf16 v[36:39], v[84:87], v[4:7], v[36:39]\n"
    "ds_read_b64_tr_b16 v[116:117], v97 offset:20480\n"
    "ds_read_b64_tr_b16 v[118:119], v97 offset:22528\n"
    "v_exp_f32 v248, v248\n"
    "v_mfma_f32_16x16x32_bf16 v[40:43], v[88:91], v[4:7], v[40:43]\n"
    "ds_read_b64_tr_b16 v[120:121], v98 offset:20480\n"
    "ds_read_b64_tr_b16 v[122:123], v98 offset:22528\n"
    "v_exp_f32 v249, v249\n"
    "v_mfma_f32_16x16x32_bf16 v[44:47], v[92:95], v[4:7], v[44:47]\n"
    "ds_read_b64_tr_b16 v[124:125], v99 offset:20480\n"
    "ds_read_b64_tr_b16 v[126:127], v99 offset:22528\n"
    "v_mul_f32 v136, v136, v152\n"
    "v_mul_f32 v137, v137, v153\n"
    "s_cmp_eq_u32 s68, 0\n"
    "s_cbranch_scc0 LB_skip_16\n"
    "s_add_u32 s88, s90, 0x180\n"
    "s_mov_b32 s96, 0xff00ff00\n"
    "s_mov_b32 s97, 0xff00ff00\n"
    "s_waitcnt vmcnt(6)\n"
    "v_cmp_eq_u64 vcc, s[78:79], v[164:165]\n"
    "s_nop 4\n"
    "s_and_b64 vcc, vcc, s[96:97]\n"
    "s_cmp_eq_u64 vcc, s[96:97]\n"
    "s_cbranch_scc0 LB_slowE2_13\n"
    "LB_backE2_14:\n"
    "LB_skip_16:\n"
    "s_waitcnt vmcnt(9)\n"
    "s_barrier\n"
    "s_add_u32 s89, s40, 0x4800\n"
    "buffer_load_dwordx4 v[0:3], v100, s[52:55], s89 offen sc1\n"
    "s_add_u32 s89, s40, 0x5400\n"
    "buffer_load_dwordx4 v[4:7], v100, s[52:55], s89 offen sc1\n"
    "s_cmp_eq_u32 s68, 0\n"
    "s_cbranch_scc0 LB_skip_17\n"
    "v_and_b32 v162, 15, v235\n"
    "v_lshlrev_b32 v162, 3, v162\n"
    "s_add_u32 s88, s90, 0x80\n"
    "buffer_load_dwordx2 v[160:161], v162, s[52:55], s88 offen sc1\n"
    "LB_skip_17:\n"
    "v_mul_f32 v138, v138, v154\n"
    "v_mul_f32 v139, v139, v155\n"
    "v_fma_f32 v246, v246, v251, v251\n"
    "v_fma_f32 v247, v247, v251, v251\n"
    "v_fma_f32 v248, v248, v251, v251\n"
    "v_fma_f32 v249, v249, v251, v251\n"
    "v_rcp_f32 v246, v246\n"
    "v_rcp_f32 v247, v247\n"
    "v_rcp_f32 v248, v248\n"
    "v_rcp_f32 v249, v249\n"
    "v_mul_f32 v136, v136, v246\n"
    "v_mul_f32 v137, v137, v247\n"
    "v_mul_f32 v138, v138, v248\n"
    "v_mul_f32 v139, v139, v249\n"
    "v_cvt_pk_bf16_f32 v146, v136, v137\n"
    "v_cvt_pk_bf16_f32 v147, v138, v139\n"
    "s_add_u32 s87, s99, 0\n"
    "buffer_store_dwordx4 v[144:147], v230, s[52:55], s87 offen sc1\n"
    "ds_read_b128 v[238:241], v236 offset:26624\n"
    "ds_read_b128 v[242:245], v236 offset:27136\n"
    "v_mul_f32 v250, 0xbfb8aa3b, v234\n"
    "v_mul_f32 v251, v234, v234\n"
    "v_rcp_f32 v251, v251\n"
    "s_waitcnt lgkmcnt(1)\n"
    "v_fma_f32 v176, -v232, v238, v176\n"
    "v_fma_f32 v177, -v232, v239, v177\n"
    "v_fma_f32 v178, -v232, v240, v178\n"
    "v_fma_f32 v179, -v232, v241, v179\n"
    "s_waitcnt lgkmcnt(0)\n"
    "v_fma_f32 v184, -v232, v242, v184\n"
    "v_fma_f32 v185, -v232, v243, v185\n"
    "v_fma_f32 v186, -v232, v244, v186\n"
    "v_fma_f32 v187, -v232, v245, v187\n"
    "ds_read_b128 v[238:241], v236 offset:26752\n"
    "ds_read_b128 v[242:245], v236 offset:27264\n"
    "v_mul_f32 v246, v176, v250\n"
    "v_mul_f32 v247, v177, v250\n"
    "v_mul_f32 v248, v178, v250\n"
    "v_mul_f32 v249, v179, v250\n"
    "v_exp_f32 v246, v246\n"
    "v_exp_f32 v247, v247\n"
    "v_exp_f32 v248, v248\n"
    "v_exp_f32 v249, v249\n"
    "v_mul_f32 v176, v176, v184\n"
    "v_mul_f32 v177, v177, v185\n"
    "v_mul_f32 v178, v178, v186\n"
    "v_mul_f32 v179, v179, v187\n"
    "v_fma_f32 v246, v246, v251, v251\n"
    "v_fma_f32 v247, v247, v251, v251\n"
    "v_mfma_f32_16x16x32_bf16 v[32:35], v[48:51], v[8:11], v[32:35]\n"
    "ds_read_b64_tr_b16 v[64:65], v96 offset:32768\n"
    "ds_read_b64_tr_b16 v[66:67], v96 offset:34816\n"
    "v_fma_f32 v248, v248, v251, v251\n"
    "v_fma_f32 v249, v249, v251, v251\n"
    "v_mfma_f32_16x16x32_bf16 v[36:39], v[52:55], v[8:11], v[36:39]\n"
    "ds_read_b64_tr_b16 v[68:69], v97 offset:32768\n"
    "ds_read_b64_tr_b16 v[70:71], v97 offset:34816\n"
    "v_rcp_f32 v246, v246\n"
    "v_mfma_f32_16x16x32_bf16 v[40:43], v[56:59], v[8:11], v[40:43]\n"
    "ds_read_b64_tr_b16 v[72:73], v98 offset:32768\n"
    "ds_read_b64_tr_b16 v[74:75], v98 offset:34816\n"
    "v_rcp_f32 v247, v247\n"
    "v_mfma_f32_16x16x32_bf16 v[44:47], v[60:63], v[8:11], v[44:47]\n"
    "ds_read_b64_tr_b16 v[76:77], v99 offset:32768\n"
    "ds_read_b64_tr_b16 v[78:79], v99 offset:34816\n"
    "v_rcp_f32 v248, v248\n"
    "v_mfma_f32_16x16x32_bf16 v[32:35], v[112:115], v[12:15], v[32:35]\n"
    "ds_read_b64_tr_b16 v[80:81], v96 offset:36864\n"
    "ds_read_b64_tr_b16 v[82:83], v96 offset:38912\n"
    "v_rcp_f32 v249, v249\n"
    "v_mfma_f32_16x16x32_bf16 v[36:39], v[116:119], v[12:15], v[36:39]\n"
    "ds_read_b64_tr_b16 v[84:85], v97 offset:36864\n"
    "ds_read_b64_tr_b16 v[86:87], v97 offset:38912\n"
    "v_mul_f32 v176, v176, v246\n"
    "v_mul_f32 v177, v177, v247\n"
    "v_mfma_f32_16x16x32_bf16 v[40:43], v[120:123], v[12:15], v[40:43]\n"
    "ds_read_b64_tr_b16 v[88:89], v98 offset:36864\n"
    "ds_read_b64_tr_b16 v[90:91], v98 offset:38912\n"
    "v_mul_f32 v178, v178, v248\n"
    "v_mul_f32 v179, v179, v249\n"
    "v_mfma_f32_16x16x32_bf16 v[44:47], v[124:127], v[12:15], v[44:47]\n"
    "ds_read_b64_tr_b16 v[92:93], v99 offset:36864\n"
    "ds_read_b64_tr_b16 v[94:95], v99 offset:38912\n"
    "v_cvt_pk_bf16_f32 v184, v176, v177\n"
    "v_cvt_pk_bf16_f32 v185, v178, v179\n"
    "s_waitcnt vmcnt(10)\n"
    "s_barrier\n"
    "s_add_u32 s89, s40, 0x6000\n"
    "buffer_load_dwordx4 v[8:11], v100, s[52:55], s89 offen sc1\n"
    "s_add_u32 s89, s40, 0x6c00\n"
    "buffer_load_dwordx4 v[12:15], v100, s[52:55], s89 offen sc1\n"
    "s_waitcnt lgkmcnt(14)\n"
    "v_mfma_f32_16x16x32_bf16 v[32:35], v[64:67], v[16:19], v[32:35]\n"
    "ds_read_b64_tr_b16 v[48:49], v96 offset:49152\n"
    "ds_read_b64_tr_b16 v[50:51], v96 offset:51200\n"
    "v_fma_f32 v180, -v232, v238, v180\n"
    "v_fma_f32 v181, -v232, v239, v181\n"
    "s_waitcnt lgkmcnt(14)\n"
    "v_mfma_f32_16x16x32_bf16 v[36:39], v[68:71], v[16:19], v[36:39]\n"
    "ds_read_b64_tr_b16 v[52:53], v97 offset:49152\n"
    "ds_read_b64_tr_b16 v[54:55], v97 offset:51200\n"
    "v_fma_f32 v182, -v232, v240, v182\n"
    "v_fma_f32 v183, -v232, v241, v183\n"
    "s_waitcnt lgkmcnt(14)\n"
    "v_mfma_f32_16x16x32_bf16 v[40:43], v[72:75], v[16:19], v[40:43]\n"
    "ds_read_b64_tr_b16 v[56:57], v98 offset:49152\n"
    "ds_read_b64_tr_b16 v[58:59], v98 offset:51200\n"
    "v_fma_f32 v188, -v232, v242, v188\n"
    "v_fma_f32 v189, -v232, v243, v189\n"
    "s_waitcnt lgkmcnt(14)\n"
    "v_mfma_f32_16x16x32_bf16 v[44:47], v[76:79], v[16:19], v[44:47]\n"
    "ds_read_b64_tr_b16 v[60:61], v99 offset:49152\n"
    "ds_read_b64_tr_b16 v[62:63], v99 offset:51200\n"
    "v_fma_f32 v190, -v232, v244, v190\n"
    "v_fma_f32 v191, -v232, v245, v191\n"
    "s_waitcnt lgkmcnt(14)\n"
    "v_mfma_f32_16x16x32_bf16 v[32:35], v[80:83], v[20:23], v[32:35]\n"
    "ds_read_b64_tr_b16 v[112:113], v96 offset:53248\n"
    "ds_read_b64_tr_b16 v[114:115], v96 offset:55296\n"
    "v_mul_f32 v246, v180, v250\n"
    "v_mul_f32 v247, v181, v250\n"
    "s_waitcnt lgkmcnt(14)\n"
    "v_mfma_f32_16x16x32_bf16 v[36:39], v[84:87], v[20:23], v[36:39]\n"
    "ds_read_b64_tr_b16 v[116:117], v97 offset:53248\n"
    "ds_read_b64_tr_b16 v[118:119], v97 offset:55296\n"
    "v_mul_f32 v248, v182, v250\n"
    "v_mul_f32 v249, v183, v250\n"
    "s_waitcnt lgkmcnt(14)\n"
    "v_mfma_f32_16x16x32_bf16 v[40:43], v[88:91], v[20:23], v[40:43]\n"
    "ds_read_b64_tr_b16 v[120:121], v98 offset:53248\n"
    "ds_read_b64_tr_b16 v[122:123], v98 offset:55296\n"
    "v_exp_f32 v246, v246\n"
    "s_waitcnt lgkmcnt(14)\n"
    "v_mfma_f32_16x16x32_bf16 v[44:47], v[92:95], v[20:23], v[44:47]\n"
    "ds_read_b64_tr_b16 v[124:125], v99 offset:53248\n"
    "ds_read_b64_tr_b16 v[126:127], v99 offset:55296\n"
    "v_exp_f32 v247, v247\n"
    "v_exp_f32 v248, v248\n"
    "v_exp_f32 v249, v249\n"
    "v_mul_f32 v180, v180, v188\n"
    "v_mul_f32 v181, v181, v189\n"
    "v_mul_f32 v182, v182, v190\n"
    "v_mul_f32 v183, v183, v191\n"
    "v_fma_f32 v246, v246, v251, v251\n"
    "v_fma_f32 v247, v247, v251, v251\n"
    "v_fma_f32 v248, v248, v251, v251\n"
    "v_fma_f32 v249, v249, v251, v251\n"
    "v_rcp_f32 v246, v246\n"
    "v_rcp_f32 v247, v247\n"
    "v_rcp_f32 v248, v248\n"
    "v_rcp_f32 v249, v249\n"
    "v_mul_f32 v180, v180, v246\n"
    "v_mul_f32 v181, v181, v247\n"
    "v_mul_f32 v182, v182, v248\n"
    "v_mul_f32 v183, v183, v249\n"
    "v_cvt_pk_bf16_f32 v186, v180, v181\n"
    "v_cvt_pk_bf16_f32 v187, v182, v183\n"
    "s_add_u32 s87, s99, 0x12000\n"
    "buffer_store_dwordx4 v[184:187], v230, s[52:55], s87 offen sc1\n"
    "s_cmp_eq_u32 s68, 0\n"
    "s_cbranch_scc0 LB_skip_21\n"
    "s_add_u32 s88, s90, 0x80\n"
    "s_mov_b32 s96, 0xff00ff\n"
    "s_mov_b32 s97, 0xff00ff\n"
    "s_waitcnt vmcnt(4)\n"
    "v_cmp_eq_u64 vcc, s[78:79], v[160:161]\n"
    "s_nop 4\n"
    "s_and_b64 vcc, vcc, s[96:97]\n"
    "s_cmp_eq_u64 vcc, s[96:97]\n"
    "s_cbranch_scc0 LB_slowE3_18\n"
    "LB_backE3_19:\n"
    "LB_skip_21:\n"
    "s_waitcnt vmcnt(6)\n"
    "s_barrier\n"
    "s_add_u32 s89, s41, 0\n"
    "buffer_load_dwordx4 v[16:19], v100, s[52:55], s89 offen sc1\n"
    "s_add_u32 s89, s41, 0x400\n"
    "buffer_load_dwordx4 v[20:23], v100, s[52:55], s89 offen sc1\n"
    "s_waitcnt lgkmcnt(14)\n"
    "v_mfma_f32_16x16x32_bf16 v[32:35], v[48:51], v[24:27], v[32:35]\n"
    "v_xor_b32 v96, 0x10000, v96\n"
    "v_xor_b32 v97, 0x10000, v97\n"
    "v_xor_b32 v98, 0x10000, v98\n"
    "v_xor_b32 v99, 0x10000, v99\n"
    "ds_read_b64_tr_b16 v[64:65], v96 offset:0\n"
    "ds_read_b64_tr_b16 v[66:67], v96 offset:2048\n"
    "s_waitcnt lgkmcnt(14)\n"
    "v_mfma_f32_16x16x32_bf16 v[36:39], v[52:55], v[24:27], v[36:39]\n"
    "ds_read_b64_tr_b16 v[68:69], v97 offset:0\n"
    "ds_read_b64_tr_b16 v[70:71], v97 offset:2048\n"
    "s_add_u32 m0, s93, 0xc000\n"
    "s_mul_i32 s89, s70, 0x40000\n"
    "s_add_u32 s89, s89, 0x10000\n"
    "buffer_load_dwordx4 v229, s[56:59], s89 offen lds\n"
    "s_waitcnt lgkmcnt(14)\n"
    "v_mfma_f32_16x16x32_bf16 v[40:43], v[56:59], v[24:27], v[40:43]\n"
    "ds_read_b64_tr_b16 v[72:73], v98 offset:0\n"
    "ds_read_b64_tr_b16 v[74:75], v98 offset:2048\n"
    "s_waitcnt lgkmcnt(14)\n"
    "v_mfma_f32_16x16x32_bf16 v[44:47], v[60:63], v[24:27], v[44:47]\n"
    "ds_read_b64_tr_b16 v[76:77], v99 offset:0\n"
    "ds_read_b64_tr_b16 v[78:79], v99 offset:2048\n"
    "s_add_u32 m0, s93, 0xd000\n"
    "s_mul_i32 s89, s70, 0x40000\n"
    "s_add_u32 s89, s89, 0x18000\n"
    "buffer_load_dwordx4 v229, s[56:59], s89 offen lds\n"
    "s_waitcnt lgkmcnt(14)\n"
    "v_mfma_f32_16x16x32_bf16 v[32:35], v[112:115], v[28:31], v[32:35]\n"
    "ds_read_b64_tr_b16 v[80:81], v96 offset:4096\n"
    "ds_read_b64_tr_b16 v[82:83], v96 offset:6144\n"
    "s_waitcnt lgkmcnt(14)\n"
    "v_mfma_f32_16x16x32_bf16 v[36:39], v[116:119], v[28:31], v[36:39]\n"
    "ds_read_b64_tr_b16 v[84:85], v97 offset:4096\n"
    "ds_read_b64_tr_b16 v[86:87], v97 offset:6144\n"
    "s_waitcnt lgkmcnt(14)\n"
    "v_mfma_f32_16x16x32_bf16 v[40:43], v[120:123], v[28:31], v[40:43]\n"
    "ds_read_b64_tr_b16 v[88:89], v98 offset:4096\n"
    "ds_read_b64_tr_b16 v[90:91], v98 offset:6144\n"
    "s_waitcnt lgkmcnt(14)\n"
    "v_mfma_f32_16x16x32_bf16 v[44:47], v[124:127], v[28:31], v[44:47]\n"
    "ds_read_b64_tr_b16 v[92:93], v99 offset:4096\n"
    "ds_read_b64_tr_b16 v[94:95], v99 offset:6144\n"
    "s_waitcnt vmcnt(4)\n"
    "s_barrier\n"
    "s_add_u32 s89, s41, 0x800\n"
    "buffer_load_dwordx4 v[24:27], v100, s[52:55], s89 offen sc1\n"
    "s_add_u32 s89, s41, 0xc00\n"
    "buffer_load_dwordx4 v[28:31], v100, s[52:55], s89 offen sc1\n"
    "s_waitcnt lgkmcnt(14)\n"
    "v_mfma_f32_16x16x32_bf16 v[32:35], v[64:67], v[0:3], v[32:35]\n"
    "ds_read_b64_tr_b16 v[48:49], v96 offset:16384\n"
    "ds_read_b64_tr_b16 v[50:51], v96 offset:18432\n"
    "s_cmp_eq_u32 s68, 0\n"
    "s_cbranch_scc0 LB_skip_11\n"
    "s_mul_i32 s87, s72, 8\n"
    "s_add_u32 s87, s87, 0x140\n"
    "s_add_u32 s87, s87, s90\n"
    "v_mov_b32 v226, s78\n"
    "v_mov_b32 v227, s79\n"
    "s_mov_b64 exec, 1\n"
    "buffer_store_dwordx2 v[226:227], off, s[52:55], s87 sc1\n"
    "s_mov_b64 exec, -1\n"
    "LB_skip_11:\n"
    "s_waitcnt lgkmcnt(14)\n"
    "s_nop 2\n"
    "v_mfma_f32_16x16x32_bf16 v[36:39], v[68:71], v[0:3], v[36:39]\n"
    "ds_read_b64_tr_b16 v[52:53], v97 offset:16384\n"
    "ds_read_b64_tr_b16 v[54:55], v97 offset:18432\n"
    "s_add_u32 m0, s93, 0x10000\n"
    "s_mul_i32 s89, s70, 0x40000\n"
    "s_add_u32 s89, s89, 0x20000\n"
    "buffer_load_dwordx4 v229, s[56:59], s89 offen lds\n"
    "s_waitcnt lgkmcnt(14)\n"
    "v_mfma_f32_16x16x32_bf16 v[40:43], v[72:75], v[0:3], v[40:43]\n"
    "ds_read_b64_tr_b16 v[56:57], v98 offset:16384\n"
    "ds_read_b64_tr_b16 v[58:59], v98 offset:18432\n"
    "s_waitcnt lgkmcnt(14)\n"
    "v_mfma_f32_16x16x32_bf16 v[44:47], v[76:79], v[0:3], v[44:47]\n"
    "ds_read_b64_tr_b16 v[60:61], v99 offset:16384\n"
    "ds_read_b64_tr_b16 v[62:63], v99 offset:18432\n"
    "s_add_u32 m0, s93, 0x11000\n"
    "s_mul_i32 s89, s70, 0x40000\n"
    "s_add_u32 s89, s89, 0x28000\n"
    "buffer_load_dwordx4 v229, s[56:59], s89 offen lds\n"
    "s_waitcnt lgkmcnt(14)\n"
    "v_mfma_f32_16x16x32_bf16 v[32:35], v[80:83], v[4:7], v[32:35]\n"
    "ds_read_b64_tr_b16 v[112:113], v96 offset:20480\n"
    "ds_read_b64_tr_b16 v[114:115], v96 offset:22528\n"
    "s_waitcnt lgkmcnt(14)\n"
    "v_mfma_f32_16x16x32_bf16 v[36:39], v[84:87], v[4:7], v[36:39]\n"
    "ds_read_b64_tr_b16 v[116:117], v97 offset:20480\n"
    "ds_read_b64_tr_b16 v[118:119], v97 offset:22528\n"
    "s_waitcnt lgkmcnt(14)\n"
    "v_mfma_f32_16x16x32_bf16 v[40:43], v[88:91], v[4:7], v[40:43]\n"
    "ds_read_b64_tr_b16 v[120:121], v98 offset:20480\n"
    "ds_read_b64_tr_b16 v[122:123], v98 offset:22528\n"
    "s_waitcnt lgkmcnt(14)\n"
    "v_mfma_f32_16x16x32_bf16 v[44:47], v[92:95], v[4:7], v[44:47]\n"
    "ds_read_b64_tr_b16 v[124:125], v99 offset:20480\n"
    "ds_read_b64_tr_b16 v[126:127], v99 offset:22528\n"
    "s_barrier\n"
    "s_add_u32 s89, s41, 0x1000\n"
    "buffer_load_dwordx4 v[0:3], v100, s[52:55], s89 offen sc1\n"
    "s_add_u32 s89, s41, 0x1400\n"
    "buffer_load_dwordx4 v[4:7], v100, s[52:55], s89 offen sc1\n"
    "s_waitcnt lgkmcnt(14)\n"
    "v_mfma_f32_16x16x32_bf16 v[32:35], v[48:51], v[8:11], v[32:35]\n"
    "ds_read_b64_tr_b16 v[64:65], v96 offset:32768\n"
    "ds_read_b64_tr_b16 v[66:67], v96 offset:34816\n"
    "s_cmp_eq_u32 s68, 0\n"
    "s_cbranch_scc0 LB_skip_22\n"
    "v_and_b32 v170, 15, v235\n"
    "v_lshlrev_b32 v170, 3, v170\n"
    "s_add_u32 s88, s90, 0x100\n"
    "buffer_load_dwordx2 v[168:169], v170, s[52:55], s88 offen sc1\n"
    "LB_skip_22:\n"
    "s_waitcnt lgkmcnt(14)\n"
    "v_mfma_f32_16x16x32_bf16 v[36:39], v[52:55], v[8:11], v[36:39]\n"
    "ds_read_b64_tr_b16 v[68:69], v97 offset:32768\n"
    "ds_read_b64_tr_b16 v[70:71], v97 offset:34816\n"
    "s_add_u32 m0, s93, 0x14000\n"
    "s_mul_i32 s89, s70, 0x40000\n"
    "s_add_u32 s89, s89, 0x30000\n"
    "buffer_load_dwordx4 v229, s[56:59], s89 offen lds\n"
    "s_waitcnt lgkmcnt(14)\n"
    "v_mfma_f32_16x16x32_bf16 v[40:43], v[56:59], v[8:11], v[40:43]\n"
    "ds_read_b64_tr_b16 v[72:73], v98 offset:32768\n"
    "ds_read_b64_tr_b16 v[74:75], v98 offset:34816\n"
    "s_waitcnt lgkmcnt(14)\n"
    "v_mfma_f32_16x16x32_bf16 v[44:47], v[60:63], v[8:11], v[44:47]\n"
    "ds_read_b64_tr_b16 v[76:77], v99 offset:32768\n"
    "ds_read_b64_tr_b16 v[78:79], v99 offset:34816\n"
    "s_add_u32 m0, s93, 0x15000\n"
    "s_mul_i32 s89, s70, 0x40000\n"
    "s_add_u32 s89, s89, 0x38000\n"
    "buffer_load_dwordx4 v229, s[56:59], s89 offen lds\n"
    "s_waitcnt lgkmcnt(14)\n"
    "v_mfma_f32_16x16x32_bf16 v[32:35], v[112:115], v[12:15], v[32:35]\n"
    "ds_read_b64_tr_b16 v[80:81], v96 offset:36864\n"
    "ds_read_b64_tr_b16 v[82:83], v96 offset:38912\n"
    "s_waitcnt lgkmcnt(14)\n"
    "v_mfma_f32_16x16x32_bf16 v[36:39], v[116:119], v[12:15], v[36:39]\n"
    "ds_read_b64_tr_b16 v[84:85], v97 offset:36864\n"
    "ds_read_b64_tr_b16 v[86:87], v97 offset:38912\n"
    "s_waitcnt lgkmcnt(14)\n"
    "v_mfma_f32_16x16x32_bf16 v[40:43], v[120:123], v[12:15], v[40:43]\n"
    "ds_read_b64_tr_b16 v[88:89], v98 offset:36864\n"
    "ds_read_b64_tr_b16 v[90:91], v98 offset:38912\n"
    "s_waitcnt lgkmcnt(14)\n"
    "v_mfma_f32_16x16x32_bf16 v[44:47], v[124:127], v[12:15], v[44:47]\n"
    "ds_read_b64_tr_b16 v[92:93], v99 offset:36864\n"
    "ds_read_b64_tr_b16 v[94:95], v99 offset:38912\n"
    "s_waitcnt vmcnt(8)\n"
    "s_barrier\n"
    "s_add_u32 s89, s41, 0x1800\n"
    "buffer_load_dwordx4 v[8:11], v100, s[52:55], s89 offen sc1\n"
    "s_add_u32 s89, s41, 0x1c00\n"
    "buffer_load_dwordx4 v[12:15], v100, s[52:55], s89 offen sc1\n"
    "s_waitcnt lgkmcnt(14)\n"
    "v_mfma_f32_16x16x32_bf16 v[32:35], v[64:67], v[16:19], v[32:35]\n"
    "ds_read_b64_tr_b16 v[48:49], v96 offset:49152\n"
    "ds_read_b64_tr_b16 v[50:51], v96 offset:51200\n"
    "s_waitcnt lgkmcnt(14)\n"
    "v_mfma_f32_16x16x32_bf16 v[36:39], v[68:71], v[16:19], v[36:39]\n"
    "ds_read_b64_tr_b16 v[52:53], v97 offset:49152\n"
    "ds_read_b64_tr_b16 v[54:55], v97 offset:51200\n"
    "s_add_u32 m0, s93, 0x18000\n"
    "s_mul_i32 s89, s70, 0x90000\n"
    "s_add_u32 s89, s89, 0\n"
    "buffer_load_dwordx4 v229, s[60:63], s89 offen lds\n"
    "s_waitcnt lgkmcnt(14)\n"
    "v_mfma_f32_16x16x32_bf16 v[40:43], v[72:75], v[16:19], v[40:43]\n"
    "ds_read_b64_tr_b16 v[56:57], v98 offset:49152\n"
    "ds_read_b64_tr_b16 v[58:59], v98 offset:51200\n"
    "s_waitcnt lgkmcnt(14)\n"
    "v_mfma_f32_16x16x32_bf16 v[44:47], v[76:79], v[16:19], v[44:47]\n"
    "ds_read_b64_tr_b16 v[60:61], v99 offset:49152\n"
    "ds_read_b64_tr_b16 v[62:63], v99 offset:51200\n"
    "s_add_u32 m0, s93, 0x19000\n"
    "s_mul_i32 s89, s70, 0x90000\n"
    "s_add_u32 s89, s89, 0x8000\n"
    "buffer_load_dwordx4 v229, s[60:63], s89 offen lds\n"
    "s_waitcnt lgkmcnt(14)\n"
    "v_mfma_f32_16x16x32_bf16 v[32:35], v[80:83], v[20:23], v[32:35]\n"
    "ds_read_b64_tr_b16 v[112:113], v96 offset:53248\n"
    "ds_read_b64_tr_b16 v[114:115], v96 offset:55296\n"
    "s_waitcnt lgkmcnt(14)\n"
    "v_mfma_f32_16x16x32_bf16 v[36:39], v[84:87], v[20:23], v[36:39]\n"
    "ds_read_b64_tr_b16 v[116:117], v97 offset:53248\n"
    "ds_read_b64_tr_b16 v[118:119], v97 offset:55296\n"
    "s_waitcnt lgkmcnt(14)\n"
    "v_mfma_f32_16x16x32_bf16 v[40:43], v[88:91], v[20:23], v[40:43]\n"
    "ds_read_b64_tr_b16 v[120:121], v98 offset:53248\n"
    "ds_read_b64_tr_b16 v[122:123], v98 offset:55296\n"
    "s_waitcnt lgkmcnt(14)\n"
    "v_mfma_f32_16x16x32_bf16 v[44:47], v[92:95], v[20:23], v[44:47]\n"
    "ds_read_b64_tr_b16 v[124:125], v99 offset:53248\n"
    "ds_read_b64_tr_b16 v[126:127], v99 offset:55296\n"
    "s_cmp_eq_u32 s68, 0\n"
    "s_cbranch_scc0 LB_skip_26\n"
    "s_add_u32 s88, s90, 0x100\n"
    "s_mov_b32 s96, 0xffffffff\n"
    "s_mov_b32 s97, 0xffffffff\n"
    "s_waitcnt vmcnt(6)\n"
    "v_cmp_eq_u64 vcc, s[78:79], v[168:169]\n"
    "s_nop 4\n"
    "s_and_b64 vcc, vcc, s[96:97]\n"
    "s_cmp_eq_u64 vcc, s[96:97]\n"
    "s_cbranch_scc0 LB_slowL_23\n"
    "LB_backL_24:\n"
    "LB_skip_26:\n"
    "s_waitcnt vmcnt(8)\n"
    "s_barrier\n"
    "s_add_u32 s89, s42, 0xa000\n"
    "buffer_load_dwordx4 v[16:19], v100, s[52:55], s89 offen sc1\n"
    "s_add_u32 s89, s42, 0xa400\n"
    "buffer_load_dwordx4 v[20:23], v100, s[52:55], s89 offen sc1\n"
    "s_waitcnt lgkmcnt(14)\n"
    "v_mfma_f32_16x16x32_bf16 v[32:35], v[48:51], v[24:27], v[32:35]\n"
    "v_xor_b32 v96, 0x10000, v96\n"
    "v_xor_b32 v97, 0x10000, v97\n"
    "v_xor_b32 v98, 0x10000, v98\n"
    "v_xor_b32 v99, 0x10000, v99\n"
    "ds_read_b64_tr_b16 v[64:65], v96 offset:0\n"
    "ds_read_b64_tr_b16 v[66:67], v96 offset:2048\n"
    "s_waitcnt lgkmcnt(14)\n"
    "v_mfma_f32_16x16x32_bf16 v[36:39], v[52:55], v[24:27], v[36:39]\n"
    "ds_read_b64_tr_b16 v[68:69], v97 offset:0\n"
    "ds_read_b64_tr_b16 v[70:71], v97 offset:2048\n"
    "s_add_u32 m0, s93, 0x1c000\n"
    "s_mul_i32 s89, s70, 0xb0000\n"
    "s_add_u32 s89, s89, 0x10000\n"
    "buffer_load_dwordx4 v229, s[60:63], s89 offen lds\n"
    "s_waitcnt lgkmcnt(14)\n"
    "v_mfma_f32_16x16x32_bf16 v[40:43], v[56:59], v[24:27], v[40:43]\n"
    "ds_read_b64_tr_b16 v[72:73], v98 offset:0\n"
    "ds_read_b64_tr_b16 v[74:75], v98 offset:2048\n"
    "s_waitcnt lgkmcnt(14)\n"
    "v_mfma_f32_16x16x32_bf16 v[44:47], v[60:63], v[24:27], v[44:47]\n"
    "ds_read_b64_tr_b16 v[76:77], v99 offset:0\n"
    "ds_read_b64_tr_b16 v[78:79], v99 offset:2048\n"
    "s_add_u32 m0, s93, 0x1d000\n"
    "s_mul_i32 s89, s70, 0xb0000\n"
    "s_add_u32 s89, s89, 0x18000\n"
    "buffer_load_dwordx4 v229, s[60:63], s89 offen lds\n"
    "s_waitcnt lgkmcnt(14)\n"
    "v_mfma_f32_16x16x32_bf16 v[32:35], v[112:115], v[28:31], v[32:35]\n"
    "ds_read_b64_tr_b16 v[80:81], v96 offset:4096\n"
    "ds_read_b64_tr_b16 v[82:83], v96 offset:6144\n"
    "s_waitcnt lgkmcnt(14)\n"
    "v_mfma_f32_16x16x32_bf16 v[36:39], v[116:119], v[28:31], v[36:39]\n"
    "ds_read_b64_tr_b16 v[84:85], v97 offset:4096\n"
    "ds_read_b64_tr_b16 v[86:87], v97 offset:6144\n"
    "s_waitcnt lgkmcnt(14)\n"
    "v_mfma_f32_16x16x32_bf16 v[40:43], v[120:123], v[28:31], v[40:43]\n"
    "ds_read_b64_tr_b16 v[88:89], v98 offset:4096\n"
    "ds_read_b64_tr_b16 v[90:91], v98 offset:6144\n"
    "s_waitcnt lgkmcnt(14)\n"
    "v_mfma_f32_16x16x32_bf16 v[44:47], v[124:127], v[28:31], v[44:47]\n"
    "ds_read_b64_tr_b16 v[92:93], v99 offset:4096\n"
    "ds_read_b64_tr_b16 v[94:95], v99 offset:6144\n"
    "s_waitcnt vmcnt(8)\n"
    "s_barrier\n"
    "s_add_u32 s89, s42, 0xa800\n"
    "buffer_load_dwordx4 v[24:27], v100, s[52:55], s89 offen sc1\n"
    "s_add_u32 s89, s42, 0xac00\n"
    "buffer_load_dwordx4 v[28:31], v100, s[52:55], s89 offen sc1\n"
    "s_waitcnt lgkmcnt(14)\n"
    "v_mfma_f32_16x16x32_bf16 v[32:35], v[64:67], v[0:3], v[32:35]\n"
    "ds_read_b64_tr_b16 v[48:49], v96 offset:16384\n"
    "ds_read_b64_tr_b16 v[50:51], v96 offset:18432\n"
    "s_waitcnt lgkmcnt(14)\n"
    "v_mfma_f32_16x16x32_bf16 v[36:39], v[68:71], v[0:3], v[36:39]\n"
    "ds_read_b64_tr_b16 v[52:53], v97 offset:16384\n"
    "ds_read_b64_tr_b16 v[54:55], v97 offset:18432\n"
    "s_add_u32 m0, s93, 0\n"
    "s_mul_i32 s89, s70, 0xc0000\n"
    "s_add_u32 s89, s89, 0x20000\n"
    "buffer_load_dwordx4 v229, s[60:63], s89 offen lds\n"
    "s_waitcnt lgkmcnt(14)\n"
    "v_mfma_f32_16x16x32_bf16 v[40:43], v[72:75], v[0:3], v[40:43]\n"
    "ds_read_b64_tr_b16 v[56:57], v98 offset:16384\n"
    "ds_read_b64_tr_b16 v[58:59], v98 offset:18432\n"
    "s_waitcnt lgkmcnt(14)\n"
    "v_mfma_f32_16x16x32_bf16 v[44:47], v[76:79], v[0:3], v[44:47]\n"
    "ds_read_b64_tr_b16 v[60:61], v99 offset:16384\n"
    "ds_read_b64_tr_b16 v[62:63], v99 offset:18432\n"
    "s_add_u32 m0, s93, 0x1000\n"
    "s_mul_i32 s89, s70, 0xc0000\n"
    "s_add_u32 s89, s89, 0x28000\n"
    "buffer_load_dwordx4 v229, s[60:63], s89 offen lds\n"
    "s_waitcnt lgkmcnt(14)\n"
    "v_mfma_f32_16x16x32_bf16 v[32:35], v[80:83], v[4:7], v[32:35]\n"
    "ds_read_b64_tr_b16 v[112:113], v96 offset:20480\n"
    "ds_read_b64_tr_b16 v[114:115], v96 offset:22528\n"
    "s_waitcnt lgkmcnt(14)\n"
    "v_mfma_f32_16x16x32_bf16 v[36:39], v[84:87], v[4:7], v[36:39]\n"
    "ds_read_b64_tr_b16 v[116:117], v97 offset:20480\n"
    "ds_read_b64_tr_b16 v[118:119], v97 offset:22528\n"
    "s_waitcnt lgkmcnt(14)\n"
    "v_mfma_f32_16x16x32_bf16 v[40:43], v[88:91], v[4:7], v[40:43]\n"
    "ds_read_b64_tr_b16 v[120:121], v98 offset:20480\n"
    "ds_read_b64_tr_b16 v[122:123], v98 offset:22528\n"
    "s_waitcnt lgkmcnt(14)\n"
    "v_mfma_f32_16x16x32_bf16 v[44:47], v[92:95], v[4:7], v[44:47]\n"
    "ds_read_b64_tr_b16 v[124:125], v99 offset:20480\n"
    "ds_read_b64_tr_b16 v[126:127], v99 offset:22528\n"
    "s_waitcnt vmcnt(8)\n"
    "s_barrier\n"
    "s_add_u32 s89, s42, 0xb000\n"
    "buffer_load_dwordx4 v[0:3], v100, s[52:55], s89 offen sc1\n"
    "s_add_u32 s89, s42, 0xb400\n"
    "buffer_load_dwordx4 v[4:7], v100, s[52:55], s89 offen sc1\n"
    "s_waitcnt lgkmcnt(14)\n"
    "v_mfma_f32_16x16x32_bf16 v[32:35], v[48:51], v[8:11], v[32:35]\n"
    "ds_read_b64_tr_b16 v[64:65], v96 offset:32768\n"
    "ds_read_b64_tr_b16 v[66:67], v96 offset:34816\n"
    "s_waitcnt lgkmcnt(14)\n"
    "v_mfma_f32_16x16x32_bf16 v[36:39], v[52:55], v[8:11], v[36:39]\n"
    "ds_read_b64_tr_b16 v[68:69], v97 offset:32768\n"
    "ds_read_b64_tr_b16 v[70:71], v97 offset:34816\n"
    "s_add_u32 m0, s93, 0x4000\n"
    "s_mul_i32 s89, s70, 0xe0000\n"
    "s_add_u32 s89, s89, 0x30000\n"
    "buffer_load_dwordx4 v229, s[60:63], s89 offen lds\n"
    "s_waitcnt lgkmcnt(14)\n"
    "v_mfma_f32_16x16x32_bf16 v[40:43], v[56:59], v[8:11], v[40:43]\n"
    "ds_read_b64_tr_b16 v[72:73], v98 offset:32768\n"
    "ds_read_b64_tr_b16 v[74:75], v98 offset:34816\n"
    "s_waitcnt lgkmcnt(14)\n"
    "v_mfma_f32_16x16x32_bf16 v[44:47], v[60:63], v[8:11], v[44:47]\n"
    "ds_read_b64_tr_b16 v[76:77], v99 offset:32768\n"
    "ds_read_b64_tr_b16 v[78:79], v99 offset:34816\n"
    "s_add_u32 m0, s93, 0x5000\n"
    "s_mul_i32 s89, s70, 0xe0000\n"
    "s_add_u32 s89, s89, 0x38000\n"
    "buffer_load_dwordx4 v229, s[60:63], s89 offen lds\n"
    "s_waitcnt lgkmcnt(14)\n"
    "v_mfma_f32_16x16x32_bf16 v[32:35], v[112:115], v[12:15], v[32:35]\n"
    "ds_read_b64_tr_b16 v[80:81], v96 offset:36864\n"
    "ds_read_b64_tr_b16 v[82:83], v96 offset:38912\n"
    "s_waitcnt lgkmcnt(14)\n"
    "v_mfma_f32_16x16x32_bf16 v[36:39], v[116:119], v[12:15], v[36:39]\n"
    "ds_read_b64_tr_b16 v[84:85], v97 offset:36864\n"
    "ds_read_b64_tr_b16 v[86:87], v97 offset:38912\n"
    "s_waitcnt lgkmcnt(14)\n"
    "v_mfma_f32_16x16x32_bf16 v[40:43], v[120:123], v[12:15], v[40:43]\n"
    "ds_read_b64_tr_b16 v[88:89], v98 offset:36864\n"
    "ds_read_b64_tr_b16 v[90:91], v98 offset:38912\n"
    "s_waitcnt lgkmcnt(14)\n"
    "v_mfma_f32_16x16x32_bf16 v[44:47], v[124:127], v[12:15], v[44:47]\n"
    "ds_read_b64_tr_b16 v[92:93], v99 offset:36864\n"
    "ds_read_b64_tr_b16 v[94:95], v99 offset:38912\n"
    "s_waitcnt vmcnt(8)\n"
    "s_barrier\n"
    "s_add_u32 s89, s42, 0xb800\n"
    "buffer_load_dwordx4 v[8:11], v100, s[52:55], s89 offen sc1\n"
    "s_add_u32 s89, s42, 0xbc00\n"
    "buffer_load_dwordx4 v[12:15], v100, s[52:55], s89 offen sc1\n"
    "s_waitcnt lgkmcnt(14)\n"
    "v_mfma_f32_16x16x32_bf16 v[32:35], v[64:67], v[16:19], v[32:35]\n"
    "ds_read_b64_tr_b16 v[48:49], v96 offset:49152\n"
    "ds_read_b64_tr_b16 v[50:51], v96 offset:51200\n"
    "s_waitcnt lgkmcnt(14)\n"
    "v_mfma_f32_16x16x32_bf16 v[36:39], v[68:71], v[16:19], v[36:39]\n"
    "ds_read_b64_tr_b16 v[52:53], v97 offset:49152\n"
    "ds_read_b64_tr_b16 v[54:55], v97 offset:51200\n"
    "s_add_u32 m0, s93, 0x8000\n"
    "s_mul_i32 s89, s70, 0xf0000\n"
    "s_add_u32 s89, s89, 0x40000\n"
    "buffer_load_dwordx4 v229, s[60:63], s89 offen lds\n"
    "s_waitcnt lgkmcnt(14)\n"
    "v_mfma_f32_16x16x32_bf16 v[40:43], v[72:75], v[16:19], v[40:43]\n"
    "ds_read_b64_tr_b16 v[56:57], v98 offset:49152\n"
    "ds_read_b64_tr_b16 v[58:59], v98 offset:51200\n"
    "s_waitcnt lgkmcnt(14)\n"
    "v_mfma_f32_16x16x32_bf16 v[44:47], v[76:79], v[16:19], v[44:47]\n"
    "ds_read_b64_tr_b16 v[60:61], v99 offset:49152\n"
    "ds_read_b64_tr_b16 v[62:63], v99 offset:51200\n"
    "s_add_u32 m0, s93, 0x9000\n"
    "s_mul_i32 s89, s70, 0xf0000\n"
    "s_add_u32 s89, s89, 0x48000\n"
    "buffer_load_dwordx4 v229, s[60:63], s89 offen lds\n"
    "s_waitcnt lgkmcnt(14)\n"
    "v_mfma_f32_16x16x32_bf16 v[32:35], v[80:83], v[20:23], v[32:35]\n"
    "ds_read_b64_tr_b16 v[112:113], v96 offset:53248\n"
    "ds_read_b64_tr_b16 v[114:115], v96 offset:55296\n"
    "s_waitcnt lgkmcnt(14)\n"
    "v_mfma_f32_16x16x32_bf16 v[36:39], v[84:87], v[20:23], v[36:39]\n"
    "ds_read_b64_tr_b16 v[116:117], v97 offset:53248\n"
    "ds_read_b64_tr_b16 v[118:119], v97 offset:55296\n"
    "s_waitcnt lgkmcnt(14)\n"
    "v_mfma_f32_16x16x32_bf16 v[40:43], v[88:91], v[20:23], v[40:43]\n"
    "ds_read_b64_tr_b16 v[120:121], v98 offset:53248\n"
    "ds_read_b64_tr_b16 v[122:123], v98 offset:55296\n"
    "s_waitcnt lgkmcnt(14)\n"
    "v_mfma_f32_16x16x32_bf16 v[44:47], v[92:95], v[20:23], v[44:47]\n"
    "ds_read_b64_tr_b16 v[124:125], v99 offset:53248\n"
    "ds_read_b64_tr_b16 v[126:127], v99 offset:55296\n"
    "s_waitcnt vmcnt(8)\n"
    "s_barrier\n"
    "s_add_u32 s89, s42, 0xc000\n"
    "buffer_load_dwordx4 v[16:19], v100, s[52:55], s89 offen sc1\n"
    "s_add_u32 s89, s42, 0xc400\n"
    "buffer_load_dwordx4 v[20:23], v100, s[52:55], s89 offen sc1\n"
    "s_waitcnt lgkmcnt(14)\n"
    "v_mfma_f32_16x16x32_bf16 v[32:35], v[48:51], v[24:27], v[32:35]\n"
    "v_xor_b32 v96, 0x10000, v96\n"
    "v_xor_b32 v97, 0x10000, v97\n"
    "v_xor_b32 v98, 0x10000, v98\n"
    "v_xor_b32 v99, 0x10000, v99\n"
    "ds_read_b64_tr_b16 v[64:65], v96 offset:0\n"
    "ds_read_b64_tr_b16 v[66:67], v96 offset:2048\n"
    "s_waitcnt lgkmcnt(14)\n"
    "v_mfma_f32_16x16x32_bf16 v[36:39], v[52:55], v[24:27], v[36:39]\n"
    "ds_read_b64_tr_b16 v[68:69], v97 offset:0\n"
    "ds_read_b64_tr_b16 v[70:71], v97 offset:2048\n"
    "s_add_u32 m0, s93, 0xc000\n"
    "s_mul_i32 s89, s70, 0x110000\n"
    "s_add_u32 s89, s89, 0x50000\n"
    "buffer_load_dwordx4 v229, s[60:63], s89 offen lds\n"
    "s_waitcnt lgkmcnt(14)\n"
    "v_mfma_f32_16x16x32_bf16 v[40:43], v[56:59], v[24:27], v[40:43]\n"
    "ds_read_b64_tr_b16 v[72:73], v98 offset:0\n"
    "ds_read_b64_tr_b16 v[74:75], v98 offset:2048\n"
    "s_waitcnt lgkmcnt(14)\n"
    "v_mfma_f32_16x16x32_bf16 v[44:47], v[60:63], v[24:27], v[44:47]\n"
    "ds_read_b64_tr_b16 v[76:77], v99 offset:0\n"
    "ds_read_b64_tr_b16 v[78:79], v99 offset:2048\n"
    "s_add_u32 m0, s93, 0xd000\n"
    "s_mul_i32 s89, s70, 0x110000\n"
    "s_add_u32 s89, s89, 0x58000\n"
    "buffer_load_dwordx4 v229, s[60:63], s89 offen lds\n"
    "s_waitcnt lgkmcnt(14)\n"
    "v_mfma_f32_16x16x32_bf16 v[32:35], v[112:115], v[28:31], v[32:35]\n"
    "ds_read_b64_tr_b16 v[80:81], v96 offset:4096\n"
    "ds_read_b64_tr_b16 v[82:83], v96 offset:6144\n"
    "s_waitcnt lgkmcnt(14)\n"
    "v_mfma_f32_16x16x32_bf16 v[36:39], v[116:119], v[28:31], v[36:39]\n"
    "ds_read_b64_tr_b16 v[84:85], v97 offset:4096\n"
    "ds_read_b64_tr_b16 v[86:87], v97 offset:6144\n"
    "s_waitcnt lgkmcnt(14)\n"
    "v_mfma_f32_16x16x32_bf16 v[40:43], v[120:123], v[28:31], v[40:43]\n"
    "ds_read_b64_tr_b16 v[88:89], v98 offset:4096\n"
    "ds_read_b64_tr_b16 v[90:91], v98 offset:6144\n"
    "s_waitcnt lgkmcnt(14)\n"
    "v_mfma_f32_16x16x32_bf16 v[44:47], v[124:127], v[28:31], v[44:47]\n"
    "ds_read_b64_tr_b16 v[92:93], v99 offset:4096\n"
    "ds_read_b64_tr_b16 v[94:95], v99 offset:6144\n"
    "s_waitcnt vmcnt(8)\n"
    "s_barrier\n"
    "s_add_u32 s89, s42, 0xc800\n"
    "buffer_load_dwordx4 v[24:27], v100, s[52:55], s89 offen sc1\n"
    "s_add_u32 s89, s42, 0xcc00\n"
    "buffer_load_dwordx4 v[28:31], v100, s[52:55], s89 offen sc1\n"
    "s_waitcnt lgkmcnt(14)\n"
    "v_mfma_f32_16x16x32_bf16 v[32:35], v[64:67], v[0:3], v[32:35]\n"
    "ds_read_b64_tr_b16 v[48:49], v96 offset:16384\n"
    "ds_read_b64_tr_b16 v[50:51], v96 offset:18432\n"
    "s_waitcnt lgkmcnt(14)\n"
    "v_mfma_f32_16x16x32_bf16 v[36:39], v[68:71], v[0:3], v[36:39]\n"
    "ds_read_b64_tr_b16 v[52:53], v97 offset:16384\n"
    "ds_read_b64_tr_b16 v[54:55], v97 offset:18432\n"
    "s_add_u32 m0, s93, 0x10000\n"
    "s_mul_i32 s89, s70, 0x120000\n"
    "s_add_u32 s89, s89, 0x60000\n"
    "buffer_load_dwordx4 v229, s[60:63], s89 offen lds\n"
    "s_waitcnt lgkmcnt(14)\n"
    "v_mfma_f32_16x16x32_bf16 v[40:43], v[72:75], v[0:3], v[40:43]\n"
    "ds_read_b64_tr_b16 v[56:57], v98 offset:16384\n"
    "ds_read_b64_tr_b16 v[58:59], v98 offset:18432\n"
    "s_waitcnt lgkmcnt(14)\n"
    "v_mfma_f32_16x16x32_bf16 v[44:47], v[76:79], v[0:3], v[44:47]\n"
    "ds_read_b64_tr_b16 v[60:61], v99 offset:16384\n"
    "ds_read_b64_tr_b16 v[62:63], v99 offset:18432\n"
    "s_add_u32 m0, s93, 0x11000\n"
    "s_mul_i32 s89, s70, 0x120000\n"
    "s_add_u32 s89, s89, 0x68000\n"
    "buffer_load_dwordx4 v229, s[60:63], s89 offen lds\n"
    "s_waitcnt lgkmcnt(14)\n"
    "v_mfma_f32_16x16x32_bf16 v[32:35], v[80:83], v[4:7], v[32:35]\n"
    "ds_read_b64_tr_b16 v[112:113], v96 offset:20480\n"
    "ds_read_b64_tr_b16 v[114:115], v96 offset:22528\n"
    "s_waitcnt lgkmcnt(14)\n"
    "v_mfma_f32_16x16x32_bf16 v[36:39], v[84:87], v[4:7], v[36:39]\n"
    "ds_read_b64_tr_b16 v[116:117], v97 offset:20480\n"
    "ds_read_b64_tr_b16 v[118:119], v97 offset:22528\n"
    "s_waitcnt lgkmcnt(14)\n"
    "v_mfma_f32_16x16x32_bf16 v[40:43], v[88:91], v[4:7], v[40:43]\n"
    "ds_read_b64_tr_b16 v[120:121], v98 offset:20480\n"
    "ds_read_b64_tr_b16 v[122:123], v98 offset:22528\n"
    "s_waitcnt lgkmcnt(14)\n"
    "v_mfma_f32_16x16x32_bf16 v[44:47], v[92:95], v[4:7], v[44:47]\n"
    "ds_read_b64_tr_b16 v[124:125], v99 offset:20480\n"
    "ds_read_b64_tr_b16 v[126:127], v99 offset:22528\n"
    "s_waitcnt vmcnt(8)\n"
    "s_barrier\n"
    "s_add_u32 s89, s42, 0xd000\n"
    "buffer_load_dwordx4 v[0:3], v100, s[52:55], s89 offen sc1\n"
    "s_add_u32 s89, s42, 0xd400\n"
    "buffer_load_dwordx4 v[4:7], v100, s[52:55], s89 offen sc1\n"
    "s_waitcnt lgkmcnt(14)\n"
    "v_mfma_f32_16x16x32_bf16 v[32:35], v[48:51], v[8:11], v[32:35]\n"
    "ds_read_b64_tr_b16 v[64:65], v96 offset:32768\n"
    "ds_read_b64_tr_b16 v[66:67], v96 offset:34816\n"
    "s_waitcnt lgkmcnt(14)\n"
    "v_mfma_f32_16x16x32_bf16 v[36:39], v[52:55], v[8:11], v[36:39]\n"
    "ds_read_b64_tr_b16 v[68:69], v97 offset:32768\n"
    "ds_read_b64_tr_b16 v[70:71], v97 offset:34816\n"
    "s_add_u32 m0, s93, 0x14000\n"
    "s_mul_i32 s89, s70, 0x140000\n"
    "s_add_u32 s89, s89, 0x70000\n"
    "buffer_load_dwordx4 v229, s[60:63], s89 offen lds\n"
    "s_waitcnt lgkmcnt(14)\n"
    "v_mfma_f32_16x16x32_bf16 v[40:43], v[56:59], v[8:11], v[40:43]\n"
    "ds_read_b64_tr_b16 v[72:73], v98 offset:32768\n"
    "ds_read_b64_tr_b16 v[74:75], v98 offset:34816\n"
    "s_waitcnt lgkmcnt(14)\n"
    "v_mfma_f32_16x16x32_bf16 v[44:47], v[60:63], v[8:11], v[44:47]\n"
    "ds_read_b64_tr_b16 v[76:77], v99 offset:32768\n"
    "ds_read_b64_tr_b16 v[78:79], v99 offset:34816\n"
    "s_add_u32 m0, s93, 0x15000\n"
    "s_mul_i32 s89, s70, 0x140000\n"
    "s_add_u32 s89, s89, 0x78000\n"
    "buffer_load_dwordx4 v229, s[60:63], s89 offen lds\n"
    "s_waitcnt lgkmcnt(14)\n"
    "v_mfma_f32_16x16x32_bf16 v[32:35], v[112:115], v[12:15], v[32:35]\n"
    "ds_read_b64_tr_b16 v[80:81], v96 offset:36864\n"
    "ds_read_b64_tr_b16 v[82:83], v96 offset:38912\n"
    "s_waitcnt lgkmcnt(14)\n"
    "v_mfma_f32_16x16x32_bf16 v[36:39], v[116:119], v[12:15], v[36:39]\n"
    "ds_read_b64_tr_b16 v[84:85], v97 offset:36864\n"
    "ds_read_b64_tr_b16 v[86:87], v97 offset:38912\n"
    "s_waitcnt lgkmcnt(14)\n"
    "v_mfma_f32_16x16x32_bf16 v[40:43], v[120:123], v[12:15], v[40:43]\n"
    "ds_read_b64_tr_b16 v[88:89], v98 offset:36864\n"
    "ds_read_b64_tr_b16 v[90:91], v98 offset:38912\n"
    "s_waitcnt lgkmcnt(14)\n"
    "v_mfma_f32_16x16x32_bf16 v[44:47], v[124:127], v[12:15], v[44:47]\n"
    "ds_read_b64_tr_b16 v[92:93], v99 offset:36864\n"
    "ds_read_b64_tr_b16 v[94:95], v99 offset:38912\n"
    "s_waitcnt vmcnt(8)\n"
    "s_barrier\n"
    "s_add_u32 s89, s42, 0xd800\n"
    "buffer_load_dwordx4 v[8:11], v100, s[52:55], s89 offen sc1\n"
    "s_add_u32 s89, s42, 0xdc00\n"
    "buffer_load_dwordx4 v[12:15], v100, s[52:55], s89 offen sc1\n"
    "s_waitcnt lgkmcnt(14)\n"
    "v_mfma_f32_16x16x32_bf16 v[32:35], v[64:67], v[16:19], v[32:35]\n"
    "ds_read_b64_tr_b16 v[48:49], v96 offset:49152\n"
    "ds_read_b64_tr_b16 v[50:51], v96 offset:51200\n"
    "s_waitcnt lgkmcnt(14)\n"
    "v_mfma_f32_16x16x32_bf16 v[36:39], v[68:71], v[16:19], v[36:39]\n"
    "ds_read_b64_tr_b16 v[52:53], v97 offset:49152\n"
    "ds_read_b64_tr_b16 v[54:55], v97 offset:51200\n"
    "s_waitcnt lgkmcnt(14)\n"
    "v_mfma_f32_16x16x32_bf16 v[40:43], v[72:75], v[16:19], v[40:43]\n"
    "ds_read_b64_tr_b16 v[56:57], v98 offset:49152\n"
    "ds_read_b64_tr_b16 v[58:59], v98 offset:51200\n"
    "s_waitcnt lgkmcnt(14)\n"
    "v_mfma_f32_16x16x32_bf16 v[44:47], v[76:79], v[16:19], v[44:47]\n"
    "ds_read_b64_tr_b16 v[60:61], v99 offset:49152\n"
    "ds_read_b64_tr_b16 v[62:63], v99 offset:51200\n"
    "s_waitcnt lgkmcnt(14)\n"
    "v_mfma_f32_16x16x32_bf16 v[32:35], v[80:83], v[20:23], v[32:35]\n"
    "ds_read_b64_tr_b16 v[112:113], v96 offset:53248\n"
    "ds_read_b64_tr_b16 v[114:115], v96 offset:55296\n"
    "s_waitcnt lgkmcnt(14)\n"
    "v_mfma_f32_16x16x32_bf16 v[36:39], v[84:87], v[20:23], v[36:39]\n"
    "ds_read_b64_tr_b16 v[116:117], v97 offset:53248\n"
    "ds_read_b64_tr_b16 v[118:119], v97 offset:55296\n"
    "s_waitcnt lgkmcnt(14)\n"
    "v_mfma_f32_16x16x32_bf16 v[40:43], v[88:91], v[20:23], v[40:43]\n"
    "ds_read_b64_tr_b16 v[120:121], v98 offset:53248\n"
    "ds_read_b64_tr_b16 v[122:123], v98 offset:55296\n"
    "s_waitcnt lgkmcnt(14)\n"
    "v_mfma_f32_16x16x32_bf16 v[44:47], v[92:95], v[20:23], v[44:47]\n"
    "ds_read_b64_tr_b16 v[124:125], v99 offset:53248\n"
    "ds_read_b64_tr_b16 v[126:127], v99 offset:55296\n"
    "s_waitcnt vmcnt(6)\n"
    "s_barrier\n"
    "s_waitcnt lgkmcnt(14)\n"
    "v_mfma_f32_16x16x32_bf16 v[32:35], v[48:51], v[24:27], v[32:35]\n"
    "v_xor_b32 v96, 0x10000, v96\n"
    "v_xor_b32 v97, 0x10000, v97\n"
    "v_xor_b32 v98, 0x10000, v98\n"
    "v_xor_b32 v99, 0x10000, v99\n"
    "ds_read_b64_tr_b16 v[64:65], v96 offset:0\n"
    "ds_read_b64_tr_b16 v[66:67], v96 offset:2048\n"
    "s_waitcnt lgkmcnt(14)\n"
    "v_mfma_f32_16x16x32_bf16 v[36:39], v[52:55], v[24:27], v[36:39]\n"
    "ds_read_b64_tr_b16 v[68:69], v97 offset:0\n"
    "ds_read_b64_tr_b16 v[70:71], v97 offset:2048\n"
    "s_waitcnt lgkmcnt(14)\n"
    "v_mfma_f32_16x16x32_bf16 v[40:43], v[56:59], v[24:27], v[40:43]\n"
    "ds_read_b64_tr_b16 v[72:73], v98 offset:0\n"
    "ds_read_b64_tr_b16 v[74:75], v98 offset:2048\n"
    "s_waitcnt lgkmcnt(14)\n"
    "v_mfma_f32_16x16x32_bf16 v[44:47], v[60:63], v[24:27], v[44:47]\n"
    "ds_read_b64_tr_b16 v[76:77], v99 offset:0\n"
    "ds_read_b64_tr_b16 v[78:79], v99 offset:2048\n"
    "s_waitcnt lgkmcnt(14)\n"
    "v_mfma_f32_16x16x32_bf16 v[32:35], v[112:115], v[28:31], v[32:35]\n"
    "ds_read_b64_tr_b16 v[80:81], v96 offset:4096\n"
    "ds_read_b64_tr_b16 v[82:83], v96 offset:6144\n"
    "s_waitcnt lgkmcnt(14)\n"
    "v_mfma_f32_16x16x32_bf16 v[36:39], v[116:119], v[28:31], v[36:39]\n"
    "ds_read_b64_tr_b16 v[84:85], v97 offset:4096\n"
    "ds_read_b64_tr_b16 v[86:87], v97 offset:6144\n"
    "s_waitcnt lgkmcnt(14)\n"
    "v_mfma_f32_16x16x32_bf16 v[40:43], v[120:123], v[28:31], v[40:43]\n"
    "ds_read_b64_tr_b16 v[88:89], v98 offset:4096\n"
    "ds_read_b64_tr_b16 v[90:91], v98 offset:6144\n"
    "s_waitcnt lgkmcnt(14)\n"
    "v_mfma_f32_16x16x32_bf16 v[44:47], v[124:127], v[28:31], v[44:47]\n"
    "ds_read_b64_tr_b16 v[92:93], v99 offset:4096\n"
    "ds_read_b64_tr_b16 v[94:95], v99 offset:6144\n"
    "s_waitcnt vmcnt(2)\n"
    "s_barrier\n"
    "s_waitcnt lgkmcnt(14)\n"
    "v_mfma_f32_16x16x32_bf16 v[32:35], v[64:67], v[0:3], v[32:35]\n"
    "ds_read_b64_tr_b16 v[48:49], v96 offset:16384\n"
    "ds_read_b64_tr_b16 v[50:51], v96 offset:18432\n"
    "s_waitcnt lgkmcnt(14)\n"
    "v_mfma_f32_16x16x32_bf16 v[36:39], v[68:71], v[0:3], v[36:39]\n"
    "ds_read_b64_tr_b16 v[52:53], v97 offset:16384\n"
    "ds_read_b64_tr_b16 v[54:55], v97 offset:18432\n"
    "s_waitcnt lgkmcnt(14)\n"
    "v_mfma_f32_16x16x32_bf16 v[40:43], v[72:75], v[0:3], v[40:43]\n"
    "ds_read_b64_tr_b16 v[56:57], v98 offset:16384\n"
    "ds_read_b64_tr_b16 v[58:59], v98 offset:18432\n"
    "s_waitcnt lgkmcnt(14)\n"
    "v_mfma_f32_16x16x32_bf16 v[44:47], v[76:79], v[0:3], v[44:47]\n"
    "ds_read_b64_tr_b16 v[60:61], v99 offset:16384\n"
    "ds_read_b64_tr_b16 v[62:63], v99 offset:18432\n"
    "s_waitcnt lgkmcnt(14)\n"
    "v_mfma_f32_16x16x32_bf16 v[32:35], v[80:83], v[4:7], v[32:35]\n"
    "ds_read_b64_tr_b16 v[112:113], v96 offset:20480\n"
    "ds_read_b64_tr_b16 v[114:115], v96 offset:22528\n"
    "s_waitcnt lgkmcnt(14)\n"
    "v_mfma_f32_16x16x32_bf16 v[36:39], v[84:87], v[4:7], v[36:39]\n"
    "ds_read_b64_tr_b16 v[116:117], v97 offset:20480\n"
    "ds_read_b64_tr_b16 v[118:119], v97 offset:22528\n"
    "s_waitcnt lgkmcnt(14)\n"
    "v_mfma_f32_16x16x32_bf16 v[40:43], v[88:91], v[4:7], v[40:43]\n"
    "ds_read_b64_tr_b16 v[120:121], v98 offset:20480\n"
    "ds_read_b64_tr_b16 v[122:123], v98 offset:22528\n"
    "s_waitcnt lgkmcnt(14)\n"
    "v_mfma_f32_16x16x32_bf16 v[44:47], v[92:95], v[4:7], v[44:47]\n"
    "ds_read_b64_tr_b16 v[124:125], v99 offset:20480\n"
    "ds_read_b64_tr_b16 v[126:127], v99 offset:22528\n"
    "s_waitcnt vmcnt(0)\n"
    "s_barrier\n"
    "s_waitcnt lgkmcnt(14)\n"
    "v_mfma_f32_16x16x32_bf16 v[32:35], v[48:51], v[8:11], v[32:35]\n"
    "s_waitcnt lgkmcnt(12)\n"
    "v_mfma_f32_16x16x32_bf16 v[36:39], v[52:55], v[8:11], v[36:39]\n"
    "s_waitcnt lgkmcnt(10)\n"
    "v_mfma_f32_16x16x32_bf16 v[40:43], v[56:59], v[8:11], v[40:43]\n"
    "s_waitcnt lgkmcnt(8)\n"
    "v_mfma_f32_16x16x32_bf16 v[44:47], v[60:63], v[8:11], v[44:47]\n"
    "s_waitcnt lgkmcnt(6)\n"
    "v_mfma_f32_16x16x32_bf16 v[32:35], v[112:115], v[12:15], v[32:35]\n"
    "s_waitcnt lgkmcnt(4)\n"
    "v_mfma_f32_16x16x32_bf16 v[36:39], v[116:119], v[12:15], v[36:39]\n"
    "s_waitcnt lgkmcnt(2)\n"
    "v_mfma_f32_16x16x32_bf16 v[40:43], v[120:123], v[12:15], v[40:43]\n"
    "s_waitcnt lgkmcnt(0)\n"
    "v_mfma_f32_16x16x32_bf16 v[44:47], v[124:127], v[12:15], v[44:47]\n"
    "s_barrier\n"
    "v_and_b32 v108, 15, v235\n"
    "v_lshrrev_b32 v109, 4, v235\n"
    "v_and_b32 v110, 7, v108\n"
    "v_xor_b32 v109, v109, v110\n"
    "v_lshlrev_b32 v109, 4, v109\n"
    "v_lshl_add_u32 v108, v108, 8, v109\n"
    "s_lshl_b32 s86, s69, 12\n"
    "s_lshl_b32 s87, s70, 14\n"
    "s_add_u32 s86, s86, s87\n"
    "s_add_u32 s86, s86, 0x8000\n"
    "v_add_u32 v108, s86, v108\n"
    "ds_write_b128 v108, v[32:35] offset:0\n"
    "v_xor_b32 v109, 64, v108\n"
    "ds_write_b128 v109, v[36:39] offset:0\n"
    "v_xor_b32 v109, 0x80, v108\n"
    "ds_write_b128 v109, v[40:43] offset:0\n"
    "v_xor_b32 v109, 0xc0, v108\n"
    "ds_write_b128 v109, v[44:47] offset:0\n"
    "s_waitcnt lgkmcnt(0)\n"
    "s_barrier\n"
    "v_lshrrev_b32 v108, 3, v235\n"
    "v_and_b32 v109, 7, v235\n"
    "v_lshlrev_b32 v109, 1, v109\n"
    "v_xor_b32 v109, v109, v108\n"
    "v_lshlrev_b32 v109, 4, v109\n"
    "v_lshl_add_u32 v108, v108, 8, v109\n"
    "s_lshl_b32 s86, s68, 11\n"
    "s_add_u32 s86, s86, 0x8000\n"
    "v_add_u32 v108, s86, v108\n"
    "v_xor_b32 v109, 16, v108\n"
    "ds_read_b128 v[0:3], v108 offset:0\n"
    "ds_read_b128 v[4:7], v108 offset:16384\n"
    "ds_read_b128 v[8:11], v109 offset:0\n"
    "ds_read_b128 v[12:15], v109 offset:16384\n"
    "s_waitcnt lgkmcnt(2)\n"
    "v_add_f32 v0, v0, v4\n"
    "v_add_f32 v1, v1, v5\n"
    "v_add_f32 v2, v2, v6\n"
    "v_add_f32 v3, v3, v7\n"
    "s_waitcnt lgkmcnt(0)\n"
    "v_add_f32 v8, v8, v12\n"
    "v_add_f32 v9, v9, v13\n"
    "v_add_f32 v10, v10, v14\n"
    "v_add_f32 v11, v11, v15\n"
    "v_lshlrev_b32 v16, 16, v104\n"
    "v_and_b32 v17, 0xffff0000, v104\n"
    "v_add_f32 v0, v0, v16\n"
    "v_add_f32 v1, v1, v17\n"
    "v_cvt_pk_bf16_f32 v20, v0, v1\n"
    "v_lshlrev_b32 v16, 16, v105\n"
    "v_and_b32 v17, 0xffff0000, v105\n"
    "v_add_f32 v2, v2, v16\n"
    "v_add_f32 v3, v3, v17\n"
    "v_cvt_pk_bf16_f32 v21, v2, v3\n"
    "v_lshlrev_b32 v16, 16, v106\n"
    "v_and_b32 v17, 0xffff0000, v106\n"
    "v_add_f32 v8, v8, v16\n"
    "v_add_f32 v9, v9, v17\n"
    "v_cvt_pk_bf16_f32 v22, v8, v9\n"
    "v_lshlrev_b32 v16, 16, v107\n"
    "v_and_b32 v17, 0xffff0000, v107\n"
    "v_add_f32 v10, v10, v16\n"
    "v_add_f32 v11, v11, v17\n"
    "v_cvt_pk_bf16_f32 v23, v10, v11\n"
    "buffer_store_dwordx4 v[20:23], v101, s[64:67], 0 offen\n"
    "s_endpgm\n"
    "LB_slowstat_1:\n"
    "s_mov_b32 s92, 0x40000\n"
    "LB_loopstat_3:\n"
    "s_sleep 1\n"
    "buffer_load_dwordx4 v[168:171], v237, s[52:55], s88 offen sc1\n"
    "s_waitcnt vmcnt(0)\n"
    "v_cmp_eq_u32 vcc, s78, v169\n"
    "s_nop 4\n"
    "v_cmp_eq_u32 s[94:95], s79, v171\n"
    "s_nop 4\n"
    "s_and_b64 vcc, vcc, s[94:95]\n"
    "s_and_b64 vcc, vcc, exec\n"
    "s_cmp_eq_u64 vcc, exec\n"
    "s_cbranch_scc1 LB_backstat_2\n"
    "s_sub_u32 s92, s92, 1\n"
    "s_cmp_lg_u32 s92, 0\n"
    "s_cbranch_scc1 LB_loopstat_3\n"
    "s_branch LB_backstat_2\n"
    "LB_slowE1_7:\n"
    "s_mov_b32 s92, 0x40000\n"
    "LB_loopE1_9:\n"
    "s_sleep 1\n"
    "buffer_load_dwordx2 v[24:25], v26, s[52:55], s88 offen sc1\n"
    "s_waitcnt vmcnt(0)\n"
    "v_cmp_eq_u64 vcc, s[78:79], v[24:25]\n"
    "s_nop 4\n"
    "s_and_b64 vcc, vcc, s[96:97]\n"
    "s_cmp_eq_u64 vcc, s[96:97]\n"
    "s_cbranch_scc1 LB_backE1_8\n"
    "s_sub_u32 s92, s92, 1\n"
    "s_cmp_lg_u32 s92, 0\n"
    "s_cbranch_scc1 LB_loopE1_9\n"
    "s_branch LB_backE1_8\n"
    "LB_slowE2_13:\n"
    "s_mov_b32 s92, 0x40000\n"
    "LB_loopE2_15:\n"
    "s_sleep 1\n"
    "buffer_load_dwordx2 v[164:165], v166, s[52:55], s88 offen sc1\n"
    "s_waitcnt vmcnt(0)\n"
    "v_cmp_eq_u64 vcc, s[78:79], v[164:165]\n"
    "s_nop 4\n"
    "s_and_b64 vcc, vcc, s[96:97]\n"
    "s_cmp_eq_u64 vcc, s[96:97]\n"
    "s_cbranch_scc1 LB_backE2_14\n"
    "s_sub_u32 s92, s92, 1\n"
    "s_cmp_lg_u32 s92, 0\n"
    "s_cbranch_scc1 LB_loopE2_15\n"
    "s_branch LB_backE2_14\n"
    "LB_slowE3_18:\n"
    "s_mov_b32 s92, 0x40000\n"
    "LB_loopE3_20:\n"
    "s_sleep 1\n"
    "buffer_load_dwordx2 v[160:161], v162, s[52:55], s88 offen sc1\n"
    "s_waitcnt vmcnt(0)\n"
    "v_cmp_eq_u64 vcc, s[78:79], v[160:161]\n"
    "s_nop 4\n"
    "s_and_b64 vcc, vcc, s[96:97]\n"
    "s_cmp_eq_u64 vcc, s[96:97]\n"
    "s_cbranch_scc1 LB_backE3_19\n"
    "s_sub_u32 s92, s92, 1\n"
    "s_cmp_lg_u32 s92, 0\n"
    "s_cbranch_scc1 LB_loopE3_20\n"
    "s_branch LB_backE3_19\n"
    "LB_slowL_23:\n"
    "s_mov_b32 s92, 0x40000\n"
    "LB_loopL_25:\n"
    "s_sleep 1\n"
    "buffer_load_dwordx2 v[168:169], v170, s[52:55], s88 offen sc1\n"
    "s_waitcnt vmcnt(0)\n"
    "v_cmp_eq_u64 vcc, s[78:79], v[168:169]\n"
    "s_nop 4\n"
    "s_and_b64 vcc, vcc, s[96:97]\n"
    "s_cmp_eq_u64 vcc, s[96:97]\n"
    "s_cbranch_scc1 LB_backL_24\n"
    "s_sub_u32 s92, s92, 1\n"
    "s_cmp_lg_u32 s92, 0\n"
    "s_cbranch_scc1 LB_loopL_25\n"
    "s_branch LB_backL_24\n"
    : "+v"(tid)
    : "s"(bid), "s"(x), "s"(wqkv), "s"(wo), "s"(w1), "s"(w3), "s"(w2), "s"(out), "s"(ws)
    : "memory", "vcc", "scc", "v1", "v2", "v3", "v4", "v5", "v6", "v7", "v8", "v9", "v10", "v11", "v12", "v13", "v14", "v15", "v16", "v17", "v18", "v19", "v20", "v21", "v22", "v23", "v24", "v25", "v26", "v27", "v28", "v29", "v30", "v31", "v32", "v33", "v34", "v35", "v36", "v37", "v38", "v39", "v40", "v41", "v42", "v43", "v44", "v45", "v46", "v47", "v48", "v49", "v50", "v51", "v52", "v53", "v54", "v55", "v56", "v57", "v58", "v59", "v60", "v61", "v62", "v63", "v64", "v65", "v66", "v67", "v68", "v69", "v70", "v71", "v72", "v73", "v74", "v75", "v76", "v77", "v78", "v79", "v80", "v81", "v82", "v83", "v84", "v85", "v86", "v87", "v88", "v89", "v90", "v91", "v92", "v93", "v94", "v95", "v96", "v97", "v98", "v99", "v100", "v101", "v102", "v103", "v104", "v105", "v106", "v107", "v108", "v109", "v110", "v111", "v112", "v113", "v114", "v115", "v116", "v117", "v118", "v119", "v120", "v121", "v122", "v123", "v124", "v125", "v126", "v127", "v128", "v129", "v130", "v131", "v132", "v133", "v134", "v135", "v136", "v137", "v138", "v139", "v140", "v141", "v142", "v143", "v144", "v145", "v146", "v147", "v148", "v149", "v150", "v151", "v152", "v153", "v154", "v155", "v156", "v157", "v158", "v159", "v160", "v161", "v162", "v163", "v164", "v165", "v166", "v167", "v168", "v169", "v170", "v171", "v172", "v173", "v174", "v175", "v176", "v177", "v178", "v179", "v180", "v181", "v182", "v183", "v184", "v185", "v186", "v187", "v188", "v189", "v190", "v191", "v192", "v193", "v194", "v195", "v196", "v197", "v198", "v199", "v200", "v201", "v202", "v203", "v204", "v205", "v206", "v207", "v208", "v209", "v210", "v211", "v212", "v213", "v214", "v215", "v216", "v217", "v218", "v219", "v220", "v221", "v222", "v223", "v224", "v225", "v226", "v227", "v228", "v229", "v230", "v231", "v232", "v233", "v234", "v235", "v236", "v237", "v238", "v239", "v240", "v241", "v242", "v243", "v244", "v245", "v246", "v247", "v248", "v249", "v250", "v251", "v252", "v253", "v254", "v255", "s36", "s37", "s38", "s39", "s40", "s41", "s42", "s43", "s44", "s45", "s46", "s47", "s48", "s49", "s50", "s51", "s52", "s53", "s54", "s55", "s56", "s57", "s58", "s59", "s60", "s61", "s62", "s63", "s64", "s65", "s66", "s67", "s68", "s69", "s70", "s71", "s72", "s73", "s74", "s75", "s76", "s77", "s78", "s79", "s80", "s81", "s82", "s83", "s84", "s85", "s86", "s87", "s88", "s89", "s90", "s91", "s92", "s93", "s94", "s95", "s96", "s97", "s98", "s99");
}
}

extern "C" void kernel_launch(void* const* d_in, const int* in_sizes, int n_in, void* d_out, int out_size, void* d_ws, size_t ws_size,
                              hipStream_t stream) {
  (void)hipFuncSetAttribute((const void*)k_fused, hipFuncAttributeMaxDynamicSharedMemorySize, LDS_BYTES);
  k_fused<<<256, 512, LDS_BYTES, stream>>>(d_in[0], d_in[1], d_in[2], d_in[4], d_in[5], d_in[6], d_out, d_ws);
}
```
